```python
import torch

def _softmax(x, axis=-1):
    return torch.softmax(x, dim=axis)
def _gelu(x):
    return torch.nn.functional.gelu(x, approximate="tanh")
def _silu(x):
    return torch.nn.functional.silu(x)
def _sigmoid(x):
    return torch.sigmoid(x)
def _layernorm(x, axis=-1, eps=1e-5):
    m = x.mean(dim=axis, keepdim=True)
    v = x.var(dim=axis, keepdim=True, unbiased=False)
    return (x - m) / torch.sqrt(v + eps)
def _scatter_add(tgt, idx, src, axis=0):
    return tgt.clone().index_add_(axis, idx, src)
def _segment_sum(src, seg, n):
    out = torch.zeros((n,) + src.shape[1:], dtype=src.dtype)
    return out.index_add_(0, seg, src)
def _pad(x, pw):
    flat = tuple(v for pair in reversed(pw) for v in pair)
    return torch.nn.functional.pad(x, flat)

kernel_name = 'sequential_preLN__sliding_block__glu_gated_reglu__prompt'

def setup_inputs():

    _w = (lambda a: (lambda t: t.to(torch.bfloat16) if t.is_floating_point() else t)(torch.as_tensor(a)))
    def _A_setup():
        B, S, D = 1, 2048, 640
        return {
            "x": _w((lambda *s: torch.randn(*s, dtype=torch.bfloat16) * 0.05)(B, S, D)),
            "wqkv": _w((lambda *s: torch.randn(*s, dtype=torch.bfloat16) * 0.05)(D, 3 * D)),
            "wo": _w((lambda *s: torch.randn(*s, dtype=torch.bfloat16) * 0.05)(D, D)),
        }
    _A_in = _A_setup()

    def _B_setup():
        B, S, D, F = 1, 2048, 640, 1728
        return {
            "x": _w((lambda *s: torch.randn(*s, dtype=torch.bfloat16) * 0.05)(B, S, D)),
            "W_gate": _w((lambda *s: torch.randn(*s, dtype=torch.bfloat16) * 0.8)(D, F) * (2.0 / D) ** 0.5),
            "W_up": _w((lambda *s: torch.randn(*s, dtype=torch.bfloat16) * 0.8)(D, F) * (2.0 / D) ** 0.5),
            "W_down": _w((lambda *s: torch.randn(*s, dtype=torch.bfloat16) * 0.8)(F, D) * (1.0 / F) ** 0.5),
        }
    _B_in = _B_setup()

    return {
        "A_x": _A_in["x"],
        "A_wqkv": _A_in["wqkv"],
        "A_wo": _A_in["wo"],
        "B_x": _B_in["x"],
        "B_W_gate": _B_in["W_gate"],
        "B_W_up": _B_in["W_up"],
        "B_W_down": _B_in["W_down"],
    }

def reference(A_x, A_wqkv, A_wo, B_x, B_W_gate, B_W_up, B_W_down):
    def _A_ref(x, wqkv, wo):
        B, S, D = x.shape
        H = 32; Dh = D // H; W = 256
        qkv = (lambda x, *d: x.permute(*d))((x @ wqkv).reshape(B, S, 3, H, Dh), 2, 0, 3, 1, 4)
        q, k, v = qkv[0], qkv[1], qkv[2]
        scores = torch.einsum("bhqd,bhkd->bhqk", q, k) * (Dh ** -0.5)
        idx = torch.arange(S)
        mask = (idx[:, None] - idx[None, :] >= 0) & (idx[:, None] - idx[None, :] < W)
        scores = torch.where(mask[None, None], scores, -1e9)
        p = _softmax(scores, axis=-1)
        o = torch.einsum("bhqk,bhkd->bhqd", p, v)
        return (lambda x, *d: x.permute(*d))(o, 0, 2, 1, 3).reshape(B, S, D) @ wo

    def _B_ref(x, W_gate, W_up, W_down):
        gate = torch.einsum("bsd,df->bsf", x, W_gate)
        up = torch.einsum("bsd,df->bsf", x, W_up)
        gate = gate * (lambda x, dt: x.to(dt))(gate > 0, gate.dtype)
        h = gate * up
        return torch.einsum("bsf,fd->bsd", h, W_down)

    x = A_x
    h = x + _A_ref(_layernorm(x, axis=-1), A_wqkv, A_wo)
    h2 = h + _B_ref(_layernorm(h, axis=-1), B_W_gate, B_W_up, B_W_down)
    return h2
```

```cpp
#include <hip/hip_runtime.h>
#include <hip/hip_bf16.h>
#include <stdint.h>

typedef unsigned short u16;
static constexpr int S_ = 2048, D_ = 640, H_ = 32, DH_ = 20, W_ = 256, F_ = 1728;

__device__ __forceinline__ float bf2f(u16 v) { return __uint_as_float(((unsigned)v) << 16); }
__device__ __forceinline__ u16 f2bf(float f) { unsigned u = __float_as_uint(f); u += 0x7fffu + ((u >> 16) & 1u); return (u16)(u >> 16); }

__global__ void ln_kernel(const u16* x, u16* y, int rows) {
    int row = blockIdx.x * (blockDim.x / 64) + threadIdx.x / 64, lane = threadIdx.x & 63;
    if (row >= rows) return;
    float v[10]; float s = 0.f;
    for (int i = 0; i < 10; ++i) { v[i] = bf2f(x[row * D_ + lane + 64 * i]); s += v[i]; }
    for (int o = 32; o >= 1; o >>= 1) s += __shfl_xor(s, o);
    float m = s / D_; float q = 0.f;
    for (int i = 0; i < 10; ++i) { float d = v[i] - m; q += d * d; }
    for (int o = 32; o >= 1; o >>= 1) q += __shfl_xor(q, o);
    float r = 1.0f / sqrtf(q / D_ + 1e-5f);
    for (int i = 0; i < 10; ++i) y[row * D_ + lane + 64 * i] = f2bf((v[i] - m) * r);
}

__global__ void gemm_naive(const u16* A, const u16* B, u16* C, const u16* res, int M, int N, int K) {
    __shared__ float As[16][17], Bs[16][17];
    int tx = threadIdx.x % 16, ty = threadIdx.x / 16;
    int row = blockIdx.y * 16 + ty, col = blockIdx.x * 16 + tx;
    float acc = 0.f;
    for (int k0 = 0; k0 < K; k0 += 16) {
        As[ty][tx] = bf2f(A[row * K + k0 + tx]);
        Bs[ty][tx] = bf2f(B[(k0 + ty) * N + col]);
        __syncthreads();
        for (int k = 0; k < 16; ++k) acc += As[ty][k] * Bs[k][tx];
        __syncthreads();
    }
    if (res) acc = bf2f(res[row * N + col]) + bf2f(f2bf(acc));
    C[row * N + col] = f2bf(acc);
}

__global__ void attn_naive(const u16* qkv, u16* o) {
    int idx = blockIdx.x * blockDim.x + threadIdx.x;
    int h = idx % H_, q = idx / H_;
    if (q >= S_) return;
    float qv[DH_];
    for (int d = 0; d < DH_; ++d) qv[d] = bf2f(qkv[q * 3 * D_ + h * DH_ + d]);
    float m = -1e30f, l = 0.f, acc[DH_];
    for (int d = 0; d < DH_; ++d) acc[d] = 0.f;
    int k0 = q - (W_ - 1); if (k0 < 0) k0 = 0;
    for (int k = k0; k <= q; ++k) {
        float s = 0.f;
        for (int d = 0; d < DH_; ++d) s += qv[d] * bf2f(qkv[k * 3 * D_ + D_ + h * DH_ + d]);
        s *= 0.22360679775f;
        float mn = fmaxf(m, s); float a = __expf(m - mn), p = __expf(s - mn);
        l = l * a + p;
        for (int d = 0; d < DH_; ++d) acc[d] = acc[d] * a + p * bf2f(qkv[k * 3 * D_ + 2 * D_ + h * DH_ + d]);
        m = mn;
    }
    for (int d = 0; d < DH_; ++d) o[q * D_ + h * DH_ + d] = f2bf(acc[d] / l);
}

__global__ void glu_kernel(const u16* g, const u16* u, u16* h, int n) {
    int i = blockIdx.x * blockDim.x + threadIdx.x;
    if (i < n) { float a = bf2f(g[i]); a = a > 0.f ? a : 0.f; h[i] = f2bf(a * bf2f(u[i])); }
}

extern "C" void kernel_launch(void* const* d_in, const int* in_sizes, int n_in,
                              void* d_out, int out_size, void* d_ws, size_t ws_size,
                              hipStream_t stream) {
    const u16* x = (const u16*)d_in[0];
    const u16* wqkv = (const u16*)d_in[1];
    const u16* wo = (const u16*)d_in[2];
    const u16* wg = (const u16*)d_in[4];
    const u16* wu = (const u16*)d_in[5];
    const u16* wd = (const u16*)d_in[6];
    u16* out = (u16*)d_out;
    u16* ws = (u16*)d_ws;
    u16* ln1 = ws; u16* qkv = ln1 + S_ * D_; u16* o = qkv + S_ * 3 * D_; u16* h = o + S_ * D_; u16* ln2 = h + S_ * D_;
    u16* g = ln2 + S_ * D_; u16* u = g + S_ * F_; u16* hh = u + S_ * F_;
    ln_kernel<<<S_ / 4, 256, 0, stream>>>(x, ln1, S_);
    gemm_naive<<<dim3(3 * D_ / 16, S_ / 16), 256, 0, stream>>>(ln1, wqkv, qkv, nullptr, S_, 3 * D_, D_);
    attn_naive<<<S_ * H_ / 256, 256, 0, stream>>>(qkv, o);
    gemm_naive<<<dim3(D_ / 16, S_ / 16), 256, 0, stream>>>(o, wo, h, x, S_, D_, D_);
    ln_kernel<<<S_ / 4, 256, 0, stream>>>(h, ln2, S_);
    gemm_naive<<<dim3(F_ / 16, S_ / 16), 256, 0, stream>>>(ln2, wg, g, nullptr, S_, F_, D_);
    gemm_naive<<<dim3(F_ / 16, S_ / 16), 256, 0, stream>>>(ln2, wu, u, nullptr, S_, F_, D_);
    glu_kernel<<<S_ * F_ / 256, 256, 0, stream>>>(g, u, hh, S_ * F_);
    gemm_naive<<<dim3(D_ / 16, S_ / 16), 256, 0, stream>>>(hh, wd, out, h, S_, D_, F_);
}
```

```cpp
#include <hip/hip_runtime.h>
#include <hip/hip_bf16.h>
#include <stdint.h>

#define LAS __attribute__((address_space(3)))
#define DI __device__ __forceinline__
typedef unsigned short u16;
typedef unsigned long long u64;
typedef short bf16x8 __attribute__((ext_vector_type(8)));
typedef short s16x4 __attribute__((ext_vector_type(4)));
typedef float f32x2 __attribute__((ext_vector_type(2)));
typedef float f32x4 __attribute__((ext_vector_type(4)));
typedef float f32x16 __attribute__((ext_vector_type(16)));
typedef unsigned u32x2 __attribute__((ext_vector_type(2)));
typedef unsigned u32x4 __attribute__((ext_vector_type(4)));
typedef __bf16 bf16x2_t __attribute__((ext_vector_type(2)));
typedef LAS char* lptr;
typedef LAS const char* lcptr;

static constexpr int S_ = 2048, D_ = 640, H_ = 32, DH_ = 20, F_ = 1728;
static constexpr size_t WS_FLAG = 0;
static constexpr size_t FL_E = 0, FL_START = 1024, FL_A = 4096, FL_B = 8192, FL_C = 12288, FL_D = 16384, FL_P = 20480;
static constexpr size_t WS_QKV = 32768;
static constexpr size_t WS_O = WS_QKV + (size_t)480 * 16384;
static constexpr size_t WS_H = WS_O + (size_t)80 * 32768;
static constexpr size_t WS_HH = WS_H + (size_t)80 * 32768;
static constexpr size_t WS_CSP = WS_HH + (size_t)216 * 32768;
static constexpr size_t WS_PS = WS_CSP + (size_t)16 * 16 * 224 * 4;
static constexpr size_t WS_S1 = WS_PS + (size_t)16 * 8 * 128 * 8;
static constexpr size_t WS_PWO = WS_S1 + (size_t)2048 * 8;
static constexpr size_t WS_PWD = WS_PWO + (size_t)8 * 20 * 5 * 1024;
static constexpr size_t WS_PWG = WS_PWD + (size_t)8 * 54 * 5 * 1024;
static constexpr size_t WS_END = WS_PWG + (size_t)16 * 40 * 7 * 1024;
static constexpr int LDS_BYTES = 163840;
static constexpr float C2_ = 0.22360679774997896f * 1.4426950408889634f;

struct Params { const u16* x; const u16* wqkv; const u16* wo; const u16* wg; const u16* wu; const u16* wd; char* out; char* ws; };

#define STAMP(i) do {} while (0)
#define STAMPC(i) do {} while (0)
#define STAMPW(i, w) do {} while (0)

DI unsigned pk2(float a, float b) { f32x2 v = {a, b}; return __builtin_bit_cast(unsigned, __builtin_convertvector(v, bf16x2_t)); }
DI float bflo(unsigned u) { return __uint_as_float(u << 16); }
DI float bfhi(unsigned u) { return __uint_as_float(u & 0xffff0000u); }
DI float rbf(float a) { return bflo(pk2(a, 0.f) & 0xffffu); }
template <bool SC1> DI void glds16(unsigned voff, const void* sbase, unsigned ldsaddr) {
    unsigned keep;
    if (SC1) asm volatile("s_mov_b32 %0, m0\n\ts_mov_b32 m0, %3\n\ts_nop 0\n\tglobal_load_lds_dwordx4 %1, %2 sc1\n\ts_mov_b32 m0, %0" : "=&s"(keep) : "v"(voff), "s"(sbase), "s"(ldsaddr) : "memory");
    else asm volatile("s_mov_b32 %0, m0\n\ts_mov_b32 m0, %3\n\ts_nop 0\n\tglobal_load_lds_dwordx4 %1, %2\n\ts_mov_b32 m0, %0" : "=&s"(keep) : "v"(voff), "s"(sbase), "s"(ldsaddr) : "memory");
}
#define GL_(v, off, sc) "global_load_lds_dwordx4 %" #v ", %[g] offset:" #off sc "\n\t"
#define GLHEAD "s_mov_b32 m0, %[l]\n\ts_nop 0\n\t"
template <bool SC1> DI void gl_lin5(unsigned v, const char* g, unsigned l) {
    if (SC1) asm volatile(GLHEAD GL_(0, -2048, " sc1") GL_(0, -1024, " sc1") GL_(0, 0, " sc1") GL_(0, 1024, " sc1") GL_(0, 2048, " sc1") :: "v"(v), [g] "s"(g), [l] "s"(l) : "memory");
    else asm volatile(GLHEAD GL_(0, -2048, "") GL_(0, -1024, "") GL_(0, 0, "") GL_(0, 1024, "") GL_(0, 2048, "") :: "v"(v), [g] "s"(g), [l] "s"(l) : "memory");
}
template <bool SC1> DI void gl_lin7(unsigned v, const char* g, unsigned l) {
    if (SC1) asm volatile(GLHEAD GL_(0, -3072, " sc1") GL_(0, -2048, " sc1") GL_(0, -1024, " sc1") GL_(0, 0, " sc1") GL_(0, 1024, " sc1") GL_(0, 2048, " sc1") GL_(0, 3072, " sc1") :: "v"(v), [g] "s"(g), [l] "s"(l) : "memory");
    else asm volatile(GLHEAD GL_(0, -3072, "") GL_(0, -2048, "") GL_(0, -1024, "") GL_(0, 0, "") GL_(0, 1024, "") GL_(0, 2048, "") GL_(0, 3072, "") :: "v"(v), [g] "s"(g), [l] "s"(l) : "memory");
}
template <bool SC1> DI void gl_str4(const unsigned* v, const char* g, unsigned l) {
    if (SC1) asm volatile(GLHEAD GL_(0, -2048, " sc1") GL_(1, -1024, " sc1") GL_(2, 0, " sc1") GL_(3, 1024, " sc1") :: "v"(v[0]), "v"(v[1]), "v"(v[2]), "v"(v[3]), [g] "s"(g), [l] "s"(l) : "memory");
    else asm volatile(GLHEAD GL_(0, -2048, "") GL_(1, -1024, "") GL_(2, 0, "") GL_(3, 1024, "") :: "v"(v[0]), "v"(v[1]), "v"(v[2]), "v"(v[3]), [g] "s"(g), [l] "s"(l) : "memory");
}
template <bool SC1> DI void gl_str4w(const unsigned* v, const char* g, unsigned l) {
    if (SC1) asm volatile(GLHEAD GL_(0, -4096, " sc1") GL_(1, -2048, " sc1") GL_(2, 0, " sc1") GL_(3, 2048, " sc1") :: "v"(v[0]), "v"(v[1]), "v"(v[2]), "v"(v[3]), [g] "s"(g), [l] "s"(l) : "memory");
    else asm volatile(GLHEAD GL_(0, -4096, "") GL_(1, -2048, "") GL_(2, 0, "") GL_(3, 2048, "") :: "v"(v[0]), "v"(v[1]), "v"(v[2]), "v"(v[3]), [g] "s"(g), [l] "s"(l) : "memory");
}
DI void gl_str8(const unsigned* v, const char* g, unsigned l) {
    asm volatile(GLHEAD GL_(0, -4096, "") GL_(1, -3072, "") GL_(2, -2048, "") GL_(3, -1024, "") GL_(4, 0, "") GL_(5, 1024, "") GL_(6, 2048, "") GL_(7, 3072, "")
                 :: "v"(v[0]), "v"(v[1]), "v"(v[2]), "v"(v[3]), "v"(v[4]), "v"(v[5]), "v"(v[6]), "v"(v[7]), [g] "s"(g), [l] "s"(l) : "memory");
}
template <bool SC1> DI void st16(void* ptr, u32x4 v) {
    if (SC1) asm volatile("global_store_dwordx4 %0, %1, off sc1\n\ts_nop 1" :: "v"(ptr), "v"(v) : "memory");
    else *(u32x4*)ptr = v;
}
DI u64 ld_dev(const void* p_) { return __hip_atomic_load((const u64*)p_, __ATOMIC_RELAXED, __HIP_MEMORY_SCOPE_AGENT); }
DI void st_dev(void* p_, u64 v) { __hip_atomic_store((u64*)p_, v, __ATOMIC_RELAXED, __HIP_MEMORY_SCOPE_AGENT); }
DI void ld64_async(u32x2& v, const void* ptr) { asm volatile("global_load_dwordx2 %0, %1, off sc1" : "=v"(v) : "v"(ptr) : "memory"); }
DI void ld32_async(float& v, const void* ptr) { asm volatile("global_load_dword %0, %1, off sc1" : "=v"(v) : "v"(ptr) : "memory"); }
DI void poll_flags(const void* flag, bool need, u64 tag) {
    for (unsigned spins = 0; spins < (1u << 17); ++spins) {
        const u64 v = need ? ld_dev(flag) : tag;
        if (__all(v == tag)) break;
        __builtin_amdgcn_s_sleep(1);
    }
    asm volatile("" ::: "memory");
}
DI void warm64(const void* base, unsigned voff, unsigned lds0) {
    unsigned keep;
    asm volatile("s_mov_b32 %0, m0\n\ts_mov_b32 m0, %3\n\ts_nop 0\n\tglobal_load_lds_dword %1, %2 sc1\n\ts_mov_b32 m0, %0" : "=&s"(keep) : "v"(voff), "s"(base), "s"(lds0 + (unsigned)(LDS_BYTES - 256)) : "memory");
}
#define VMWAIT(N) asm volatile("s_waitcnt vmcnt(%0)" :: "i"(N) : "memory")
#define BAR_L() asm volatile("s_waitcnt lgkmcnt(0)\n\ts_barrier" ::: "memory")
#define BAR_VL(N) asm volatile("s_waitcnt vmcnt(%0) lgkmcnt(0)\n\ts_barrier" :: "i"(N) : "memory")
#define WAIT_LGKM0() do { __builtin_amdgcn_sched_barrier(0); __builtin_amdgcn_s_waitcnt(0xC07F); __builtin_amdgcn_sched_barrier(0); } while (0)
#define CBAR() do { WAIT_LGKM0(); asm volatile("" ::: "memory"); __builtin_amdgcn_s_barrier(); asm volatile("" ::: "memory"); __builtin_amdgcn_sched_barrier(0); } while (0)
#define SGB(mask, n) __builtin_amdgcn_sched_group_barrier(mask, n, 0)
template <int STEP, int MAXN> DI void bar_vl_n(int n) {
    if (n >= MAXN) { BAR_VL((MAXN * STEP > 63 ? 63 : MAXN * STEP)); }
    else if constexpr (MAXN > 0) bar_vl_n<STEP, MAXN - 1>(n);
}
template <int N> struct IC { static constexpr int v = N; };
template <int K, int END, class F> DI void static_for(F&& f) { if constexpr (K < END) { f(IC<K>{}); static_for<K + 1, END>(f); } }
template <int R, int NST, int NP, int LAG, bool PROLOGUE, class FI, class FP, class FQ>
DI void ring_loader(FI&& issue, FP&& pre, FQ&& post) {
    constexpr int L0 = (R < NST ? R : NST) - 1;
    if (PROLOGUE) {
#pragma unroll 1
        for (int k = 0; k <= L0; ++k) issue();
    }
    static_for<0, LAG + 1>([&](auto kc) { constexpr int k = decltype(kc)::v; BAR_VL((L0 - k) * NP); pre(k); if constexpr (k >= LAG && k - LAG + R < NST) issue(); post(k); });
    constexpr int KS = NST - R + LAG;
    if constexpr (KS > LAG + 1) {
#pragma unroll 1
        for (int k = LAG + 1; k < KS; ++k) { BAR_VL((R - 1 - LAG) * NP); pre(k); issue(); post(k); }
    }
    static_for<(KS > LAG + 1 ? KS : LAG + 1), NST>([&](auto kc) { constexpr int k = decltype(kc)::v; BAR_VL((NST - 1 - k) * NP); pre(k); post(k); });
}
DI bf16x8 ld128(lcptr p) { return *(const LAS bf16x8*)p; }
DI s16x4 ld64(lcptr p) { return *(const LAS s16x4*)p; }
DI s16x4 ldtr(lcptr p) { typedef short v4 __attribute__((ext_vector_type(4))); return __builtin_bit_cast(s16x4, __builtin_amdgcn_ds_read_tr16_b64_v4i16((LAS v4*)p)); }
DI bf16x8 cat8(s16x4 a, s16x4 b) { return __builtin_shufflevector(a, b, 0, 1, 2, 3, 4, 5, 6, 7); }
#define MFMA16(a, b, c) __builtin_amdgcn_mfma_f32_16x16x32_bf16(a, b, c, 0, 0, 0)
#define MFMA32(a, b, c) __builtin_amdgcn_mfma_f32_32x32x16_bf16(a, b, c, 0, 0, 0)
#define MFMA32K8(a, b, c) __builtin_amdgcn_mfma_f32_32x32x8bf16_1k(a, b, c, 0, 0, 0)
template <int CTRL> DI float dpp_add(float v) { return v + __int_as_float(__builtin_amdgcn_update_dpp(0, __float_as_int(v), CTRL, 0xf, 0xf, true)); }
DI float sum8(float v) { v = dpp_add<0xB1>(v); v = dpp_add<0x4E>(v); return dpp_add<0x141>(v); }
DI float sum_x16(float v) { auto r = __builtin_amdgcn_permlane16_swap(__float_as_uint(v), __float_as_uint(v), false, false); return __uint_as_float(r[0]) + __uint_as_float(r[1]); }
DI float sum_x32(float v) { auto r = __builtin_amdgcn_permlane32_swap(__float_as_uint(v), __float_as_uint(v), false, false); return __uint_as_float(r[0]) + __uint_as_float(r[1]); }
DI bf16x8 ones8() { bf16x8 o = {0x3f80, 0x3f80, 0x3f80, 0x3f80, 0x3f80, 0x3f80, 0x3f80, 0x3f80}; asm volatile("" : "+v"(o)); return o; }

DI f32x2 ln_scalars(float sum, float sq) {
    const float mean = sum * (1.0f / D_);
    float var = sq * (1.0f / D_) - mean * mean; var = var > 0.f ? var : 0.f;
    const float sd = rbf(__builtin_sqrtf(rbf(rbf(var) + 1e-5f)));
    return (f32x2){rbf(mean), 1.0f / sd};
}
template <bool FUSED> DI void publish(const Params& p, size_t flag_off, int wid, int lane, u64 tag) {
    if (FUSED) { BAR_VL(0); if (wid == 0 && lane == 0) st_dev(p.ws + WS_FLAG + flag_off, tag); }
}

namespace pa { constexpr int R = 4, NST = 10, LA = 0, LB = R * 16384, L_STAT = 2 * R * 16384, L_CS = L_STAT + 1024; }
template <bool FUSED>
DI void phase_a_stats(const Params& p, int lane, int wid, int ti, u64 tag) {
    const int r = lane >> 3, c = lane & 7;
#pragma unroll
    for (int hlf = 0; hlf < 2; ++hlf) {
        const int row = 128 * ti + 16 * wid + 8 * hlf + r;
        const u32x4* src = (const u32x4*)(p.x + (size_t)row * D_) + c;
        u32x4 v[10];
#pragma unroll
        for (int i = 0; i < 10; ++i) v[i] = src[8 * i];
        float a = 0.f, q = 0.f;
#pragma unroll
        for (int i = 0; i < 10; ++i)
#pragma unroll
            for (int e = 0; e < 4; ++e) { const float lo = bflo(v[i][e]), hi = bfhi(v[i][e]); a += lo + hi; q += lo * lo + hi * hi; }
        const f32x2 st = ln_scalars(sum8(a), sum8(q));
        if (c == 0) { const u64 w = ((u64)__float_as_uint(st.y) << 32) | __float_as_uint(st.x); char* dst = p.ws + WS_S1 + (size_t)row * 8; if (FUSED) st_dev(dst, w); else *(u64*)dst = w; }
    }
    publish<FUSED>(p, FL_A + (ti * 16 + 15) * 8, wid, lane, tag);
}
template <bool FUSED>
DI void phase_a(const Params& p, lptr L, int lane, int wid, int ti, int tj, int rot, u64 tag) {
    using namespace pa;
    const unsigned lds0 = (unsigned)(uintptr_t)L;
    if (wid >= 4) {
        const int lw = wid - 4, r = lane >> 3, c = lane & 7;
        const bool isA = lw < 2;
        unsigned vo[2];
#pragma unroll
        for (int e = 0; e < 2; ++e) vo[e] = isA ? (unsigned)(r * D_ + ((c ^ ((4 * e + (r >> 1)) & 7)) * 8)) * 2u : (unsigned)(r * 3 * D_ + ((c ^ (4 * ((r >> 1) & 1) + 2 * e)) * 8)) * 2u;
        unsigned vp[8];
#pragma unroll
        for (int i = 0; i < 8; ++i) vp[i] = vo[i & 1] + (isA ? (unsigned)i * (8 * D_ * 2 - 1024) : (unsigned)(i & 3) * (8 * 3 * D_ * 2 - 1024));
        const char* gb = isA ? (const char*)p.x + (size_t)(128 * ti + 64 * lw) * D_ * 2 + 4096 + rot * 128 : (const char*)p.wqkv + ((size_t)(32 * (lw - 2) + 64 * rot) * 3 * D_ + 128 * tj) * 2 + 2048;
        const int gstep = isA ? 128 : 64 * 3 * D_ * 2;
        int ks = rot; unsigned sl = lds0 + (isA ? LA + lw * 8192 + 4096 : LB + (lw - 2) * 4096 + 2048), sslot = 0;
        auto issue = [&]() {
            if (isA) gl_str8(vp, gb, sl + sslot); else { gl_str4<false>(vp, gb, sl + sslot); gl_str4<false>(vp, gb + 128, sl + sslot + 8192); }
            gb += gstep; if (++ks == NST) { ks = 0; gb -= (size_t)NST * gstep; }
            sslot = (sslot + 16384) & (R * 16384 - 1);
        };
        const int g = lane >> 4, q = (lane & 15) >> 2, pp = lane & 3;
        unsigned aC[2];
#pragma unroll
        for (int t = 0; t < 2; ++t) aC[t] = (unsigned)(LB + ((lw >> 1) * 8 + g) * 1024 + q * 128 + (((2 * (2 * (lw & 1) + t) + (pp >> 1)) ^ (4 * ((q >> 1) & 1) + 2 * (g & 1))) * 16) + (pp & 1) * 8);
        f32x4 cs[2] = {{0.f, 0.f, 0.f, 0.f}, {0.f, 0.f, 0.f, 0.f}};
        const bf16x8 ones = ones8();
        s16x4 cf[2][2][2];
        __builtin_amdgcn_s_setprio(3);
        ring_loader<R, NST, 8, 2, true>(issue,
            [&](int s) { lcptr q0 = (lcptr)L + (s & 3) * 16384;
#pragma unroll
                for (int ks2 = 0; ks2 < 2; ++ks2)
#pragma unroll
                    for (int t = 0; t < 2; ++t) { cf[ks2][t][0] = ldtr(q0 + aC[t] + ks2 * 4096); cf[ks2][t][1] = ldtr(q0 + aC[t] + ks2 * 4096 + 512); } },
            [&](int s) {
#pragma unroll
                for (int ks2 = 0; ks2 < 2; ++ks2)
#pragma unroll
                    for (int t = 0; t < 2; ++t) cs[t] = MFMA16(cat8(cf[ks2][t][0], cf[ks2][t][1]), ones, cs[t]);
                if (s == NST - 2 && isA) {
                    if (FUSED) poll_flags(p.ws + WS_FLAG + FL_A + (ti * 16 + 15) * 8, lane == 0, tag);
                    const char* src = p.ws + WS_S1 + (size_t)(128 * ti + 64 * lw + lane) * 8;
                    const u64 w = FUSED ? ld_dev(src) : *(const u64*)src;
                    *(LAS u64*)(L + L_STAT + (64 * lw + lane) * 8) = w;
                } });
        __builtin_amdgcn_s_setprio(0);
        if ((lane & 15) == 0) {
#pragma unroll
            for (int t = 0; t < 2; ++t) *(LAS f32x4*)(L + L_CS + (32 * lw + 16 * t + 4 * g) * 4) = cs[t];
        }
        BAR_L();
        BAR_L();
    } else {
        const int wm = wid >> 1, wn = wid & 1, r32 = lane & 31, h = lane >> 5, blk = (lane >> 4) & 1, q = (lane & 15) >> 2, pp = lane & 3;
        unsigned aX[4], aW[2];
        { const int row = 64 * wm + r32;
#pragma unroll
          for (int u = 0; u < 4; ++u) aX[u] = (unsigned)(LA + (row >> 3) * 1024 + (row & 7) * 128 + (((2 * u + h) ^ ((row >> 1) & 7)) * 16));
#pragma unroll
          for (int nt = 0; nt < 2; ++nt) aW[nt] = (unsigned)(LB + (wn * 8 + h) * 1024 + q * 128 + (((4 * nt + 2 * blk + (pp >> 1)) ^ (4 * ((q >> 1) & 1) + 2 * h)) * 16) + (pp & 1) * 8); }
        f32x16 acc[2][2];
#pragma unroll
        for (int i = 0; i < 16; ++i) { acc[0][0][i] = 0.f; acc[0][1][i] = 0.f; acc[1][0][i] = 0.f; acc[1][1][i] = 0.f; }
        bf16x8 X[4][2], W[4][2];
        auto rd = [&](int slot, int u) {
            lcptr sl = (lcptr)L + slot * 16384;
#pragma unroll
            for (int nt = 0; nt < 2; ++nt) W[u][nt] = cat8(ldtr(sl + aW[nt] + u * 2048), ldtr(sl + aW[nt] + u * 2048 + 512));
#pragma unroll
            for (int mt = 0; mt < 2; ++mt) X[u][mt] = ld128(sl + aX[u] + mt * 4096);
        };
        auto mm = [&](int u) {
#pragma unroll
            for (int mt = 0; mt < 2; ++mt)
#pragma unroll
                for (int nt = 0; nt < 2; ++nt) acc[mt][nt] = MFMA32(W[u][nt], X[u][mt], acc[mt][nt]);
        };
        auto il = [&]() { SGB(0x8, 1); SGB(0x100, 2); SGB(0x8, 1); SGB(0x100, 2); SGB(0x8, 1); SGB(0x100, 2); SGB(0x8, 1); __builtin_amdgcn_sched_barrier(0); };
        auto bar = [&]() { __builtin_amdgcn_sched_barrier(0); asm volatile("" ::: "memory"); __builtin_amdgcn_s_barrier(); asm volatile("" ::: "memory"); __builtin_amdgcn_sched_barrier(0); };
        bar();
        STAMP(1); STAMPC(26);
        rd(0, 0); rd(0, 1);
#pragma unroll 1
        for (int s = 0; s < NST - 1; ++s) {
            const int c = s & 3, n = (s + 1) & 3;
            rd(c, 2); mm(0); il();
            rd(c, 3); mm(1); il();
            bar();
            rd(n, 0); mm(2); il();
            rd(n, 1); mm(3); il();
        }
        rd((NST - 1) & 3, 2); mm(0); il();
        rd((NST - 1) & 3, 3); mm(1); il();
        mm(2); mm(3);
        STAMP(2); STAMPC(27);
        BAR_L();
#pragma unroll
        for (int mt = 0; mt < 2; ++mt) { const int tok = 64 * wm + 32 * mt + r32; const f32x2 st = *(const LAS f32x2*)(L + L_STAT + tok * 8);
#pragma unroll
            for (int nt = 0; nt < 2; ++nt)
#pragma unroll
                for (int u = 0; u < 4; ++u) { const int col = 64 * wn + 32 * nt + 8 * u + 4 * h;
                    const f32x4 cs = *(const LAS f32x4*)(L + L_CS + col * 4);
                    const float sc = (128 * tj + col < D_) ? st.y * C2_ : st.y;
                    const f32x4 a = {acc[mt][nt][4 * u], acc[mt][nt][4 * u + 1], acc[mt][nt][4 * u + 2], acc[mt][nt][4 * u + 3]};
                    const f32x4 v = (a - cs * st.x) * sc;
                    *(LAS u32x2*)(L + (col >> 2) * 1024 + tok * 8) = (u32x2){pk2(v[0], v[1]), pk2(v[2], v[3])}; } }
        BAR_L();
    }
    char* dst = p.ws + WS_QKV + (size_t)(32 * tj) * 16384 + (size_t)ti * 1024;
#pragma unroll
    for (int i = 0; i < 4; ++i) { const int gq = 4 * wid + i; st16<FUSED>(dst + (size_t)gq * 16384 + lane * 16, *(const LAS u32x4*)(L + gq * 1024 + lane * 16)); }
    STAMP(3);
    publish<FUSED>(p, FL_A + (ti * 16 + tj) * 8, wid, lane, tag);
}

namespace pb {
constexpr int GS = 3104;
constexpr int L_K = 0, L_V = 10 * GS, L_ONE = 20 * GS, L_Q = 21 * GS, L_WG = L_Q + 10240  , L_WO = L_WG + 21504  , L_O = L_WO + 15360;
static_assert(L_O + 10240 <= LDS_BYTES && L_WG % 16 == 0, "LDS map");
}
template <bool EDGE>
DI f32x16 attn_wave(lcptr L, int hh, int qb, int lane, int tstart) {
    using namespace pb;
    const int r = lane & 31, h = lane >> 5;
    bf16x8 q0; s16x4 q1;
    { lcptr qp = L + L_Q + hh * 5 * 1024 + (32 * qb + r) * 8;
      q0 = cat8(ld64(qp + (2 * h) * 1024), ld64(qp + (2 * h + 1) * 1024));
      const s16x4 t = ld64(qp + 4 * 1024); q1 = h ? (s16x4){0, 0, 0, 0} : t; }
    lcptr kp = L + L_K + hh * 5 * GS + (32 * qb + r) * 8;
    const f32x16 z16 = {};
    bf16x8 k0[9]; s16x4 k1[9];
#pragma unroll
    for (int t = 0; t < 9; ++t) { k0[t] = cat8(ld64(kp + (2 * h) * GS + t * 256), ld64(kp + (2 * h + 1) * GS + t * 256)); k1[t] = ld64(kp + 4 * GS + t * 256); }
    float m;
    { f32x16 a = MFMA32K8(k1[8], q1, MFMA32(k0[8], q0, z16)), b = MFMA32K8(k1[0], q1, MFMA32(k0[0], q0, z16));
      const float bias = (!EDGE || tstart == 0) ? 0.f : -INFINITY;
#pragma unroll
      for (int i = 0; i < 16; ++i) { const int c = (i & 3) + 8 * (i >> 2) + 4 * h; a[i] = (c <= r) ? a[i] : (EDGE ? b[i] + bias : b[i]); }
      float x = __builtin_fmaxf(__builtin_fmaxf(a[0], a[1]), a[2]), y = __builtin_fmaxf(__builtin_fmaxf(a[3], a[4]), a[5]);
#pragma unroll
      for (int i = 6; i < 14; i += 4) { x = __builtin_fmaxf(__builtin_fmaxf(x, a[i]), a[i + 1]); y = __builtin_fmaxf(__builtin_fmaxf(y, a[i + 2]), a[i + 3]); }
      m = __builtin_fmaxf(__builtin_fmaxf(__builtin_fmaxf(x, a[14]), a[15]), y); }
#pragma unroll
    for (int t = 1; t < 8; ++t) { const f32x16 a = MFMA32K8(k1[t], q1, MFMA32(k0[t], q0, z16));
        float x = __builtin_fmaxf(__builtin_fmaxf(a[0], a[1]), a[2]), y = __builtin_fmaxf(__builtin_fmaxf(a[3], a[4]), a[5]);
#pragma unroll
        for (int i = 6; i < 14; i += 4) { x = __builtin_fmaxf(__builtin_fmaxf(x, a[i]), a[i + 1]); y = __builtin_fmaxf(__builtin_fmaxf(y, a[i + 2]), a[i + 3]); }
        x = __builtin_fmaxf(__builtin_fmaxf(x, a[14]), a[15]);
        if (EDGE) x = t >= tstart ? x : -INFINITY;
        m = __builtin_fmaxf(__builtin_fmaxf(m, x), (EDGE && t < tstart) ? -INFINITY : y); }
    { auto rr = __builtin_amdgcn_permlane32_swap(__float_as_uint(m), __float_as_uint(m), false, false); m = __builtin_fmaxf(__uint_as_float(rr[0]), __uint_as_float(rr[1])); }
    f32x16 nm16;
#pragma unroll
    for (int i = 0; i < 16; ++i) nm16[i] = -m;
    BAR_VL(0);
    const int blk = (lane >> 4) & 1, vq = (lane & 15) >> 2, vp = lane & 3, dg = 4 * blk + vp;
    lcptr vpz = L + (dg < 5 ? L_V + (hh * 5 + dg) * GS : L_ONE) + (32 * qb + 4 * h + vq) * 8;
    f32x16 O = z16;
#pragma unroll
    for (int t = 1; t < 8; ++t) {
        const bf16x8 v0 = cat8(ldtr(vpz + t * 256), ldtr(vpz + t * 256 + 64)), v1 = cat8(ldtr(vpz + t * 256 + 128), ldtr(vpz + t * 256 + 192));
        f32x16 a = MFMA32K8(k1[t], q1, MFMA32(k0[t], q0, nm16));
        if (EDGE) { const float bias = t >= tstart ? 0.f : -INFINITY;
#pragma unroll
            for (int i = 0; i < 16; ++i) a[i] += bias; }
        unsigned P[8];
#pragma unroll
        for (int i = 0; i < 8; ++i) P[i] = pk2(__builtin_amdgcn_exp2f(a[2 * i]), __builtin_amdgcn_exp2f(a[2 * i + 1]));
        O = MFMA32(v0, __builtin_bit_cast(bf16x8, ((u32x4){P[0], P[1], P[2], P[3]})), O);
        O = MFMA32(v1, __builtin_bit_cast(bf16x8, ((u32x4){P[4], P[5], P[6], P[7]})), O);
    }
    { const bf16x8 v0 = cat8(ldtr(vpz + 8 * 256), ldtr(vpz + 8 * 256 + 64)), v1 = cat8(ldtr(vpz + 8 * 256 + 128), ldtr(vpz + 8 * 256 + 192));
      const bf16x8 w0 = cat8(ldtr(vpz), ldtr(vpz + 64)), w1 = cat8(ldtr(vpz + 128), ldtr(vpz + 192));
      f32x16 a = MFMA32K8(k1[8], q1, MFMA32(k0[8], q0, nm16)); const f32x16 b = MFMA32K8(k1[0], q1, MFMA32(k0[0], q0, nm16));
      const float bias = (!EDGE || tstart == 0) ? 0.f : -INFINITY;
#pragma unroll
      for (int i = 0; i < 16; ++i) { const int c = (i & 3) + 8 * (i >> 2) + 4 * h; a[i] = (c <= r) ? a[i] : (EDGE ? b[i] + bias : b[i]); }
      unsigned P[8], P0[8];
#pragma unroll
      for (int i = 0; i < 8; ++i) { P[i] = pk2(__builtin_amdgcn_exp2f(a[2 * i]), __builtin_amdgcn_exp2f(a[2 * i + 1]));
          const int c0 = ((2 * i) & 3) + 8 * ((2 * i) >> 2) + 4 * h; const unsigned mk = (c0 <= r ? 0xffffu : 0u) | (c0 + 1 <= r ? 0xffff0000u : 0u);
          P0[i] = P[i] & ~mk; P[i] &= mk; }
      O = MFMA32(v0, __builtin_bit_cast(bf16x8, ((u32x4){P[0], P[1], P[2], P[3]})), O);
      O = MFMA32(v1, __builtin_bit_cast(bf16x8, ((u32x4){P[4], P[5], P[6], P[7]})), O);
      O = MFMA32(w0, __builtin_bit_cast(bf16x8, ((u32x4){P0[0], P0[1], P0[2], P0[3]})), O);
      O = MFMA32(w1, __builtin_bit_cast(bf16x8, ((u32x4){P0[4], P0[5], P0[6], P0[7]})), O); }
    return O;
}
template <bool FUSED>
DI void phase_b(const Params& p, lptr L, int lane, int wid, int ti, int tj, u64 tag) {
    using namespace pb;
    const int tid = wid * 64 + lane;
    const unsigned lds0 = (unsigned)(uintptr_t)L;
    const int ns = ti < 8 ? 3 : 2, s0 = ti < 8 ? 3 * ti : 24 + 2 * (ti - 8);
    const int ct = tj >> 1, z = 2 * ti + (tj & 1);
    const int nod = (z < 20 ? 1 : 0) + 1 + (z + 32 < 54 ? 1 : 0);
#pragma unroll 1
    for (int n = wid; n < 7 * ns + 5 * nod; n += 8) {
        if (n < 7 * ns) { const int u = 64 * n + lane, row = u / 28, c = u % 28; int col = 112 * tj + 8 * (c % 14); col = col > F_ - 8 ? F_ - 8 : col;
            const unsigned vo = (unsigned)((16 * s0 + row) * F_ + col) * 2u;
            if (c < 14) glds16<false>(vo, p.wg, lds0 + L_WG + n * 1024); else glds16<false>(vo, p.wu, lds0 + L_WG + n * 1024);
        } else { const int m = n - 7 * ns, im = m / 5, u = 64 * (m % 5) + lane, row = u / 10, c = u % 10;
            const bool isO = z < 20 && im == 0; const int step = isO ? z : (im - (z < 20 ? 1 : 0) == 0 ? z : z + 32);
            glds16<false>((unsigned)((32 * step + row) * D_ + 80 * ct + 8 * c) * 2u, isO ? p.wo : p.wd, lds0 + L_WO + im * 5120 + (m % 5) * 1024); }
    }
    if (ti < 2) {
        for (int n = tid; n < (2 - ti) * 20 * 64; n += 512) { const int pc = n >> 6, mb = pc % (2 - ti), gq = pc / (2 - ti);
            *(LAS u32x4*)(L + gq * GS + mb * 1024 + (n & 63) * 16) = (u32x4){0u, 0u, 0u, 0u}; }
    }
    if (tid < 384) *(LAS u32x2*)(L + L_ONE + tid * 8) = (u32x2){0x00003f80u, 0u};
    BAR_VL(0);
    { const int h = lane >> 5, blk = (lane >> 4) & 1, g = lane >> 4, q = (lane & 15) >> 2, pp = lane & 3;
      if (wid < 7) {
          const int t = wid;
          const int cb = t < 6 ? (t & 1) * 224 + (32 * (t >> 1) + 16 * blk + 4 * pp) * 2 : blk * 224 + (96 + 4 * pp) * 2;
          lcptr src = (lcptr)L + L_WG + (8 * h + q) * 448 + cb;
          f32x16 cs = {}; const bf16x8 ones = ones8();
#pragma unroll 1
          for (int s = 0; s < ns; ++s) { const bf16x8 f = cat8(ldtr(src + s * 16 * 448), ldtr(src + s * 16 * 448 + 4 * 448));
              st16<FUSED>(p.ws + WS_PWG + ((size_t)(tj * 40 + s0 + s) * 7 + t) * 1024 + lane * 16, __builtin_bit_cast(u32x4, f));
              cs = MFMA32(f, ones, cs); }
          if ((lane & 31) == 0) { float* dst = (float*)(p.ws + WS_CSP) + (ti * 16 + tj) * 224 + 32 * t + 4 * h;
#pragma unroll
              for (int u = 0; u < 4; ++u) st16<FUSED>(dst + 8 * u, __builtin_bit_cast(u32x4, ((f32x4){cs[4 * u], cs[4 * u + 1], cs[4 * u + 2], cs[4 * u + 3]}))); }
      }
#pragma unroll 1
      for (int n = wid; n < 5 * nod; n += 8) { const int im = n / 5, nt = n % 5;
          const bool isO = z < 20 && im == 0; const int step = isO ? z : (im - (z < 20 ? 1 : 0) == 0 ? z : z + 32);
          lcptr src = (lcptr)L + L_WO + im * 5120 + (8 * g + q) * 160 + (16 * nt + 4 * pp) * 2;
          const bf16x8 f = cat8(ldtr(src), ldtr(src + 4 * 160));
          char* dst = p.ws + (isO ? WS_PWO + ((size_t)(ct * 20 + step) * 5 + nt) * 1024 : WS_PWD + ((size_t)(ct * 54 + step) * 5 + nt) * 1024);
          st16<FUSED>(dst + lane * 16, __builtin_bit_cast(u32x4, f)); }
    }
    if (FUSED) {
        if (wid == 7) { const int mb = lane / 6, sec = (lane % 6) >> 1, e = lane & 1, mtile = ti - 2 + mb;
            const bool need = lane < 18 && mtile >= 0 && (sec > 0 || mb == 2);
            poll_flags(p.ws + WS_FLAG + FL_A + ((mtile < 0 ? 0 : mtile) * 16 + ((640 * sec + 40 * tj + 39 * e) >> 7)) * 8, need, tag); }
    }
    BAR_VL(0);
    if (FUSED && tid == 0) st_dev(p.ws + WS_FLAG + FL_P + (ti * 16 + tj) * 8, tag);
    STAMP(4);
    int nv = 0;
    { const char* qkv = p.ws + WS_QKV; const unsigned vo = lane * 16;
#pragma unroll 1
      for (int n = wid; n < 70; n += 8) {
          int grp, mtile; unsigned dst;
          if (n >= 30 && n < 40) { const int m = n - 30; grp = 10 * tj + m; mtile = ti; dst = (unsigned)(L_Q + m * 1024); }
          else { const int kv = n >= 40, rem = n - 40 * kv, hh = rem / 15, dg = (rem % 15) / 3, mb = rem % 3; grp = 160 * (1 + kv) + 5 * (2 * tj + hh) + dg; mtile = ti - 2 + mb; dst = (unsigned)(kv * L_V + (hh * 5 + dg) * GS + mb * 1024); }
          if (mtile >= 0) { glds16<FUSED>(vo, qkv + (size_t)grp * 16384 + (size_t)mtile * 1024, lds0 + dst); nv += n >= 40; }
      } }
    bar_vl_n<1, 4>(nv);
    STAMP(5);
    const int hh = wid >> 2, qb = wid & 3, r = lane & 31, h = lane >> 5;
    const int q0g = 128 * ti + 32 * qb;
    f32x16 O;
    if (ti >= 2) O = attn_wave<false>((lcptr)L, hh, qb, lane, 0); else O = attn_wave<true>((lcptr)L, hh, qb, lane, q0g >= 256 ? 0 : 8 - (q0g >> 5));
    { auto rr = __builtin_amdgcn_permlane32_swap(__float_as_uint(O[8]), __float_as_uint(O[8]), false, false);
      const float il = 1.0f / __uint_as_float(rr[1]);
      const int row = 32 * qb + r;
#pragma unroll
      for (int u = 0; u < 3; ++u) { const int dgp = 2 * u + h; if (dgp < 5) { const int cgp = 5 * hh + dgp;
          *(LAS u32x2*)(L + L_O + (cgp >> 1) * 2048 + row * 16 + (cgp & 1) * 8) = (u32x2){pk2(O[4 * u] * il, O[4 * u + 1] * il), pk2(O[4 * u + 2] * il, O[4 * u + 3] * il)}; } } }
    STAMP(6);
    BAR_L();
    for (int n = wid; n < 10; n += 8)
        st16<FUSED>(p.ws + WS_O + (size_t)(5 * tj + (n >> 1)) * 32768 + (size_t)(128 * ti + 64 * (n & 1)) * 16 + lane * 16, *(const LAS u32x4*)(L + L_O + n * 1024 + lane * 16));
    STAMP(7);
    publish<FUSED>(p, FL_B + (ti * 16 + tj) * 8, wid, lane, tag);
}

template <int MODE> struct PC;
template <> struct PC<0> { static constexpr int NST = 10, RB = 10, RA = 6, LA = 102400, L_RES = 151552, L_X = 102400, L_STG = 122880; };
template <> struct PC<1> { static constexpr int NST = 27, RB = 8, RA = 8, LA = 81920, L_RES = 147456, L_X = 81920, L_STG = 102400; };
template <bool FUSED, int MODE>
DI void ce_prefetch(const Params& p, lptr L, int lane, int wid, int tj) {
    const char* wb = p.ws + (MODE ? WS_PWD + (size_t)(tj >> 1) * 54 * 5120 : WS_PWO + (size_t)(tj >> 1) * 20 * 5120) + (wid - 6) * 5120 + 2048;
    const unsigned l = (unsigned)(uintptr_t)L + (wid - 6) * 5120 + 2048;
#pragma unroll 1
    for (int s = 0; s < PC<MODE>::RB; ++s) gl_lin5<FUSED>(lane * 16, wb + (size_t)s * 10240, l + s * 10240);
}
template <bool FUSED, int MODE>
DI void phase_ce(const Params& p, lptr L, int lane, int wid, int ti, int tj, u64 tag) {
    typedef PC<MODE> C;
    constexpr int NST = C::NST, RA = C::RA, RB = C::RB;
    const unsigned lds0 = (unsigned)(uintptr_t)L;
    const int rh = tj & 1, ct = tj >> 1, r0 = 128 * ti + 64 * rh;
    const int wm = (wid >> 1) & 1, wk = wid & 1, r16 = lane & 15, g = lane >> 4;
    if (wid >= 6) {
        if (MODE == 0) {
            bar_vl_n<5, RB - 1>(RB - 1);
            BAR_VL(0);
            if (FUSED) {
#pragma unroll 1
                for (int i = 0; i < 5; ++i) { int ln = (((wid - 6) * 5 + i) * 64 + lane); ln = ln < 560 ? ln : 559;
                    warm64(p.ws + WS_PWG + (size_t)tj * 286720 + (size_t)(ti & 3) * 71680, (unsigned)ln * 128u, lds0); }
            }
#pragma unroll 1
            for (int s = 2; s < NST; ++s) BAR_L();
        } else {
            const char* wb = p.ws + WS_PWD + (size_t)ct * 54 * 5120 + (wid - 6) * 5120 + 2048 + (size_t)RB * 10240;
            const unsigned l0 = lds0 + (wid - 6) * 5120 + 2048; unsigned l = l0;
            ring_loader<RB, NST, 5, 2, false>([&]() { gl_lin5<FUSED>(lane * 16, wb, l); wb += 10240; l = l == l0 + (RB - 1) * 10240 ? l0 : l + 10240; }, [&](int) {}, [&](int) {});
        }
        BAR_L(); BAR_L(); BAR_L();
    } else if (wid >= 4) {
        const int la = wid - 4;
        const unsigned voA = lane * 16;
#pragma unroll
        for (int i = 0; i < 5; ++i) { const int pc = 5 * la + i;
            if (MODE == 0) { const int n = 64 * pc + lane; glds16<false>((unsigned)((n / 10) * D_ * 2 + (n % 10) * 16), (const char*)p.x + (size_t)r0 * D_ * 2 + ct * 160, lds0 + C::L_RES + pc * 1024); }
            else glds16<FUSED>(voA, p.ws + WS_H + (size_t)(10 * ct + pc) * 32768 + (size_t)r0 * 16, lds0 + C::L_RES + pc * 1024); }
        if (FUSED) poll_flags(p.ws + WS_FLAG + (MODE ? FL_D : FL_B) + (ti * 16 + (lane & 15)) * 8, lane < 16, tag);
        STAMPW(MODE ? 17 : 9, 4);
        const char* ab = p.ws + (MODE ? WS_HH : WS_O) + (size_t)r0 * 16 + (size_t)(4 * la) * 32768 + 2048;
        unsigned vp[4];
#pragma unroll
        for (int i = 0; i < 4; ++i) vp[i] = voA + (unsigned)i * (32768 - 1024);
        const unsigned l0 = lds0 + C::LA + la * 4096 + 2048; unsigned l = l0;
        ring_loader<RA, NST, 4, 2, true>([&]() { gl_str4<FUSED>(vp, ab, l); ab += 8 * 32768; l = l == l0 + (RA - 1) * 8192 ? l0 : l + 8192; }, [&](int) {}, [&](int) {});
        BAR_L(); BAR_L(); BAR_L();
    } else {
        const unsigned aA = (unsigned)(C::LA + (4 * wk + g) * 1024 + (32 * wm + r16) * 16), aB = (unsigned)(wk * 5120 + lane * 16);
        f32x4 acc[2][5];
#pragma unroll
        for (int i = 0; i < 2; ++i)
#pragma unroll
            for (int n = 0; n < 5; ++n) acc[i][n] = (f32x4){0.f, 0.f, 0.f, 0.f};
        bf16x8 Af[3][2], Bf[3][5];
        int sa = 0, sb = 0;
        auto rd = [&](int b) {
            lcptr la = (lcptr)L + aA + sa * 8192; lcptr lb = (lcptr)L + aB + sb * 10240;
#pragma unroll
            for (int i = 0; i < 2; ++i) Af[b][i] = ld128(la + i * 256);
#pragma unroll
            for (int n = 0; n < 5; ++n) Bf[b][n] = ld128(lb + n * 1024);
            sa = sa == RA - 1 ? 0 : sa + 1; sb = sb == RB - 1 ? 0 : sb + 1;
        };
        auto mm = [&](int b) {
#pragma unroll
            for (int n = 0; n < 5; ++n)
#pragma unroll
                for (int i = 0; i < 2; ++i) acc[i][n] = MFMA16(Bf[b][n], Af[b][i], acc[i][n]);
        };
        auto bar = [&]() { __builtin_amdgcn_sched_barrier(0); __builtin_amdgcn_s_waitcnt(0xC77F); asm volatile("" ::: "memory"); __builtin_amdgcn_s_barrier(); asm volatile("" ::: "memory"); __builtin_amdgcn_sched_barrier(0); };
        auto il = [&]() { SGB(0x8, 1); SGB(0x100, 2); SGB(0x8, 1); SGB(0x100, 2); SGB(0x8, 1); SGB(0x100, 2); SGB(0x8, 1); SGB(0x100, 1); SGB(0x8, 6); __builtin_amdgcn_sched_barrier(0); };
        bar(); STAMP(MODE ? 18 : 10); if (MODE) STAMPC(28); rd(0);
        bar(); rd(1);
        int s = 0;
#pragma unroll 1
        for (; s + 4 < NST; s += 3) {
            bar(); rd(2); mm(0); il();
            bar(); rd(0); mm(1); il();
            bar(); rd(1); mm(2); il();
        }
        if (NST % 3 == 0) { bar(); rd(2); mm(0); il(); mm(1); mm(2); }
        else { bar(); rd(2); mm(0); il(); bar(); rd(0); mm(1); il(); mm(2); mm(0); }
        STAMP(MODE ? 19 : 11); if (MODE) STAMPC(29);
        BAR_L();
#pragma unroll
        for (int n = 0; n < 5; ++n) *(LAS f32x4*)(L + C::L_X + (wid * 5 + n) * 1024 + lane * 16) = wk ? acc[0][n] : acc[1][n];
        BAR_L();
        const int row = 32 * wm + 16 * wk + r16;
        float ps = 0.f, pq = 0.f;
#pragma unroll
        for (int n = 0; n < 5; ++n) { const f32x4 v = (wk ? acc[1][n] : acc[0][n]) + *(const LAS f32x4*)(L + C::L_X + ((wid ^ 1) * 5 + n) * 1024 + lane * 16);
            u32x2 rr;
            if (MODE == 0) rr = *(const LAS u32x2*)(L + C::L_RES + row * 160 + n * 32 + g * 8);
            else rr = *(const LAS u32x2*)(L + C::L_RES + (2 * n + (g >> 1)) * 1024 + row * 16 + (g & 1) * 8);
            const unsigned a = pk2(v[0], v[1]), b = pk2(v[2], v[3]);
            const u32x2 o = {pk2(bflo(rr[0]) + bflo(a), bfhi(rr[0]) + bfhi(a)), pk2(bflo(rr[1]) + bflo(b), bfhi(rr[1]) + bfhi(b))};
            if (MODE == 0) {
#pragma unroll
                for (int e = 0; e < 2; ++e) { const float lo = bflo(o[e]), hi = bfhi(o[e]); ps += lo + hi; pq += lo * lo + hi * hi; }
                *(LAS u32x2*)(L + C::L_STG + (2 * n + (g >> 1)) * 1024 + row * 16 + (g & 1) * 8) = o;
            } else *(LAS u32x2*)(L + C::L_STG + row * 160 + n * 32 + g * 8) = o; }
        if (MODE == 0) { ps = sum_x32(sum_x16(ps)); pq = sum_x32(sum_x16(pq));
            if (g == 0) { char* dst = p.ws + WS_PS + ((size_t)(ti * 8 + ct) * 128 + 64 * rh + row) * 8; const u64 v = ((u64)__float_as_uint(pq) << 32) | __float_as_uint(ps);
                if (FUSED) st_dev(dst, v); else *(u64*)dst = v; } }
        BAR_L();
    }
    for (int n = wid; n < 10; n += 8) { const u32x4 v = *(const LAS u32x4*)(L + C::L_STG + n * 1024 + lane * 16);
        if (MODE == 0) st16<FUSED>(p.ws + WS_H + (size_t)(10 * ct + n) * 32768 + (size_t)r0 * 16 + lane * 16, v);
        else { const int u = 64 * n + lane; *(u32x4*)(p.out + (size_t)(r0 + u / 10) * D_ * 2 + ct * 160 + (u % 10) * 16) = v; } }
    STAMP(MODE ? 20 : 12);
    if (MODE == 0) publish<FUSED>(p, FL_C + (ti * 16 + tj) * 8, wid, lane, tag);
}

namespace pd { constexpr int RB = 3, RA = 4, SB = 28672, LA = RB * SB, NST = 10, L_STAT = LA + RA * 16384, L_CS = L_STAT + 1024, L_STG = LA; }
template <bool FUSED>
DI void d_bissue(const char* wb, unsigned l, int lane) { gl_lin7<FUSED>(lane * 16, wb, l); gl_lin7<FUSED>(lane * 16, wb + 7168, l + 7168); }
template <bool FUSED>
DI void d_prefetch(const Params& p, lptr L, int lane, int wid, int tj) {
    const char* wb = p.ws + WS_PWG + (size_t)tj * 286720 + (wid - 6) * 14336 + 3072;
    const unsigned l = (unsigned)(uintptr_t)L + (wid - 6) * 14336 + 3072;
#pragma unroll 1
    for (int s = 0; s < pd::RB; ++s) d_bissue<FUSED>(wb + (size_t)s * pd::SB, l + s * pd::SB, lane);
}
template <bool FUSED>
DI void phase_d(const Params& p, lptr L, int lane, int wid, int ti, int tj, u64 tag) {
    using namespace pd;
    const unsigned lds0 = (unsigned)(uintptr_t)L;
    if (wid >= 6) {
        const int lb = wid - 6;
        u32x2 ps[8]; float cp[2][16];
        const int row = 64 * lb + lane;
        const char* wb = p.ws + WS_PWG + (size_t)tj * 286720 + lb * 14336 + 3072 + (size_t)RB * SB;
        const unsigned l0 = lds0 + lb * 14336 + 3072; unsigned l = l0;
        auto issue = [&]() { d_bissue<FUSED>(wb, l, lane); wb += SB; l = l == l0 + (RB - 1) * SB ? l0 : l + SB; };
        static_assert(RB == 3 && NST == 10, "the waits below");
        BAR_VL(28);
#pragma unroll
        for (int c = 0; c < 8; ++c) ld64_async(ps[c], p.ws + WS_PS + ((size_t)(ti * 8 + c) * 128 + row) * 8);
        BAR_VL(22); issue();
#pragma unroll
        for (int i = 0; i < 16; ++i) ld32_async(cp[0][i], (const float*)(p.ws + WS_CSP) + (i * 16 + tj) * 224 + row);
        BAR_VL(38); issue();
#pragma unroll
        for (int i = 0; i < 16; ++i) ld32_async(cp[1][i], (const float*)(p.ws + WS_CSP) + (i * 16 + tj) * 224 + (row + 128 < 224 ? row + 128 : 223));
        BAR_VL(46); issue();
        BAR_VL(30); issue();
        BAR_VL(14); issue();
        { float a = 0.f, q = 0.f;
#pragma unroll
          for (int c = 0; c < 8; ++c) { asm volatile("" : "+v"(ps[c])); a += __uint_as_float(ps[c][0]); q += __uint_as_float(ps[c][1]); }
          *(LAS f32x2*)(L + L_STAT + row * 8) = ln_scalars(a, q);
#pragma unroll
          for (int z = 0; z < 2; ++z) { float sm = 0.f;
#pragma unroll
              for (int i = 0; i < 16; ++i) { asm volatile("" : "+v"(cp[z][i])); sm += cp[z][i]; }
              if (row + 128 * z < 224) *(LAS float*)(L + L_CS + (row + 128 * z) * 4) = sm; } }
        BAR_VL(14); issue();
        BAR_VL(14); issue();
        BAR_VL(14);
        if (FUSED) {
#pragma unroll 1
            for (int i = 0; i < 3; ++i) { int ln = (lb * 3 + i) * 64 + lane; ln = ln < 270 ? ln : 269;
                warm64(p.ws + WS_PWD + (size_t)(tj >> 1) * 276480 + (size_t)(2 * (ti & 3) + (tj & 1)) * 34560, (unsigned)ln * 128u, lds0); }
            BAR_VL(3);
        } else BAR_VL(0);
        BAR_L(); BAR_L();
    } else if (wid >= 4) {
        const int la = wid - 4;
        if (FUSED) poll_flags(p.ws + WS_FLAG + FL_C + (ti * 16 + (lane & 15)) * 8, lane < 16, tag);
        STAMPW(13, 4);
        const char* ab = p.ws + WS_H + (size_t)(128 * ti + 64 * la) * 16 + 4096;
        unsigned vp[4];
#pragma unroll
        for (int i = 0; i < 4; ++i) vp[i] = lane * 16 + (unsigned)i * (32768 - 2048);
        const unsigned l0 = lds0 + LA + la * 1024 + 4096; unsigned l = l0;
        ring_loader<RA, NST, 8, 1, true>([&]() { gl_str4w<FUSED>(vp, ab, l); gl_str4w<FUSED>(vp, ab + 4 * 32768, l + 8192); ab += 8 * 32768; l = l == l0 + (RA - 1) * 16384 ? l0 : l + 16384; }, [&](int) {}, [&](int) {});
        BAR_L(); BAR_L();
    } else {
        const int r32 = lane & 31, h = lane >> 5;
        const unsigned aA = (unsigned)(LA + h * 2048 + (32 * wid + r32) * 16), aB = (unsigned)(lane * 16);
        f32x16 acc[7];
#pragma unroll
        for (int t = 0; t < 7; ++t)
#pragma unroll
            for (int i = 0; i < 16; ++i) acc[t][i] = 0.f;
        bf16x8 A0, B0[7], A1, B1[7];
        auto rd = [&](bf16x8& Af, bf16x8* Bf, int sa, int sb, int u) {
            lcptr lb = (lcptr)L + aB + sb * SB + u * 7168;
            Af = ld128((lcptr)L + aA + sa * 16384 + u * 4096);
#pragma unroll
            for (int t = 0; t < 7; ++t) Bf[t] = ld128(lb + t * 1024);
        };
        auto mm = [&](const bf16x8& Af, const bf16x8* Bf) {
#pragma unroll
            for (int t = 0; t < 7; ++t) acc[t] = MFMA32(Bf[t], Af, acc[t]);
        };
        auto il = [&](const bf16x8& Af, const bf16x8* Bf) { SGB(0x8, 1); SGB(0x100, 2); SGB(0x8, 1); SGB(0x100, 2); SGB(0x8, 1); SGB(0x100, 2); SGB(0x8, 1); SGB(0x100, 2); SGB(0x8, 3); __builtin_amdgcn_sched_barrier(0);
            asm volatile("" :: "v"(Af), "v"(Bf[0]), "v"(Bf[1]), "v"(Bf[2]), "v"(Bf[3]), "v"(Bf[4]), "v"(Bf[5]), "v"(Bf[6])); __builtin_amdgcn_sched_barrier(0); };
        CBAR();
        STAMP(14); STAMPC(24);
        rd(A0, B0, 0, 0, 0);
        int sa = 0, sb = 0;
#pragma unroll 1
        for (int s = 0; s < NST - 1; ++s) {
            rd(A1, B1, sa, sb, 1); mm(A0, B0); il(A0, B0);
            rd(A0, B0, sa, sb, 2); mm(A1, B1); il(A1, B1);
            rd(A1, B1, sa, sb, 3); mm(A0, B0); il(A0, B0);
            sa = sa == RA - 1 ? 0 : sa + 1; sb = sb == RB - 1 ? 0 : sb + 1;
            CBAR();
            rd(A0, B0, sa, sb, 0); mm(A1, B1); il(A1, B1);
        }
        rd(A1, B1, sa, sb, 1); mm(A0, B0); il(A0, B0);
        rd(A0, B0, sa, sb, 2); mm(A1, B1); il(A1, B1);
        rd(A1, B1, sa, sb, 3); mm(A0, B0); il(A0, B0);
        mm(A1, B1);
        STAMP(15); STAMPC(25);
        BAR_L();
        const int tok = 32 * wid + r32;
        const f32x2 st = *(const LAS f32x2*)(L + L_STAT + tok * 8);
        const float r2 = st.y * st.y;
#pragma unroll
        for (int a = 0; a < 3; ++a)
#pragma unroll
            for (int u = 0; u < 4; ++u) {
                const f32x4 cg = *(const LAS f32x4*)(L + L_CS + (64 * a + 8 * u + 4 * h) * 4), cu = *(const LAS f32x4*)(L + L_CS + (64 * a + 32 + 8 * u + 4 * h) * 4);
                float o[4];
#pragma unroll
                for (int e = 0; e < 4; ++e) { const float zg = acc[2 * a][4 * u + e] - cg[e] * st.x, zu = acc[2 * a + 1][4 * u + e] - cu[e] * st.x; o[e] = __builtin_fmaxf(zg, 0.f) * zu * r2; }
                *(LAS u32x2*)(L + L_STG + (4 * a + u) * 2048 + tok * 16 + h * 8) = (u32x2){pk2(o[0], o[1]), pk2(o[2], o[3])}; }
#pragma unroll
        for (int u = 0; u < 2; ++u) {
            const f32x4 cg = *(const LAS f32x4*)(L + L_CS + (192 + 8 * u + 4 * h) * 4), cu = *(const LAS f32x4*)(L + L_CS + (208 + 8 * u + 4 * h) * 4);
            float o[4];
#pragma unroll
            for (int e = 0; e < 4; ++e) { const float zg = acc[6][4 * u + e] - cg[e] * st.x, zu = acc[6][8 + 4 * u + e] - cu[e] * st.x; o[e] = __builtin_fmaxf(zg, 0.f) * zu * r2; }
            *(LAS u32x2*)(L + L_STG + (12 + u) * 2048 + tok * 16 + h * 8) = (u32x2){pk2(o[0], o[1]), pk2(o[2], o[3])}; }
        BAR_L();
    }
    for (int n = wid; n < 28; n += 8) { const int c = 14 * tj + (n >> 1); if (c < 216)
        st16<FUSED>(p.ws + WS_HH + (size_t)c * 32768 + (size_t)(128 * ti + 64 * (n & 1)) * 16 + lane * 16, *(const LAS u32x4*)(L + L_STG + n * 1024 + lane * 16)); }
    STAMP(16);
    publish<FUSED>(p, FL_D + (ti * 16 + tj) * 8, wid, lane, tag);
}

#define ROTA 1
#define ROTC 0
#define ROTD 0
#define ROTE 1
#define PROLOG() \
    extern __shared__ __attribute__((aligned(16))) char smem[]; \
    lptr L = (lptr)smem; \
    const int tid = threadIdx.x, lane = tid & 63, wid = __builtin_amdgcn_readfirstlane(tid >> 6); \
    const int xcd = blockIdx.x & 7, idx = blockIdx.x >> 3; \
    const int ti = 4 * (xcd & 3) + (idx & 3), tj = 8 * (xcd >> 2) + (idx >> 2); \
    const int rot10 = ((idx >> 2) + 3 * (idx & 3)) % 10, rot27 = ((idx >> 2) * 3 + (idx & 3) * 7) % 27; \
    const int rotC = ROTC ? rot10 : 0, rotD = ROTD ? rot10 : 0, rotE = ROTE ? rot27 : 0, rotA = ROTA ? rot10 : 0; \
    (void)rot10; (void)rot27; (void)tid; (void)rotA; (void)rotC; (void)rotD; (void)rotE

__global__ __launch_bounds__(512, 2) void k_fused(const Params p) {
    PROLOG();
    STAMP(0);
    u64 tag;
    { const u64 e = ld_dev(p.ws + WS_FLAG + FL_E) + 1;
      tag = ((u64)(unsigned)__builtin_amdgcn_readfirstlane((unsigned)(e >> 32)) << 32) | (unsigned)__builtin_amdgcn_readfirstlane((unsigned)e); }
    if (tj >= 15) phase_a_stats<true>(p, lane, wid, ti, tag); else phase_a<true>(p, L, lane, wid, ti, tj, rotA, tag);
    if (tid == 0) st_dev(p.ws + WS_FLAG + FL_START + blockIdx.x * 8, tag);
    phase_b<true>(p, L, lane, wid, ti, tj, tag);
    if (wid >= 6) {
        poll_flags(p.ws + WS_FLAG + FL_P + ((lane & 15) * 16 + (lane < 16 ? tj : (tj & ~1) + ((lane >> 4) & 1))) * 8, lane < 48, tag);
        ce_prefetch<true, 0>(p, L, lane, wid, tj);
    }
    STAMP(8);
    phase_ce<true, 0>(p, L, lane, wid, ti, tj, tag);
    if (wid >= 6) d_prefetch<true>(p, L, lane, wid, tj);
    if (blockIdx.x == 0 && wid == 5) {
        for (unsigned spins = 0; spins < (1u << 17); ++spins) {
            bool ok = true;
#pragma unroll
            for (int i = 0; i < 4; ++i) ok &= ld_dev(p.ws + WS_FLAG + FL_START + (lane * 4 + i) * 8) == tag;
            if (__all(ok)) break;
            __builtin_amdgcn_s_sleep(1);
        }
        if (lane == 0) st_dev(p.ws + WS_FLAG + FL_E, tag);
    }
    phase_d<true>(p, L, lane, wid, ti, tj, tag);
    if (wid >= 6) ce_prefetch<true, 1>(p, L, lane, wid, tj);
    phase_ce<true, 1>(p, L, lane, wid, ti, tj, tag);
    STAMP(21);
}

extern "C" void kernel_launch(void* const* d_in, const int* in_sizes, int n_in, void* d_out, int out_size, void* d_ws, size_t ws_size, hipStream_t stream) {
    Params p;
    p.x = (const u16*)d_in[0]; p.wqkv = (const u16*)d_in[1]; p.wo = (const u16*)d_in[2];
    p.wg = (const u16*)d_in[4]; p.wu = (const u16*)d_in[5]; p.wd = (const u16*)d_in[6];
    p.out = (char*)d_out; p.ws = (char*)d_ws;
    (void)hipFuncSetAttribute((const void*)k_fused, hipFuncAttributeMaxDynamicSharedMemorySize, LDS_BYTES);
    k_fused<<<256, 512, LDS_BYTES, stream>>>(p);
}
```

```cpp
#include <hip/hip_runtime.h>
#include <hip/hip_bf16.h>
#include <stdint.h>

#define LAS __attribute__((address_space(3)))
#define DI __device__ __forceinline__
typedef unsigned short u16;
typedef unsigned long long u64;
typedef short bf16x8 __attribute__((ext_vector_type(8)));
typedef short s16x4 __attribute__((ext_vector_type(4)));
typedef float f32x2 __attribute__((ext_vector_type(2)));
typedef float f32x4 __attribute__((ext_vector_type(4)));
typedef float f32x16 __attribute__((ext_vector_type(16)));
typedef unsigned u32x2 __attribute__((ext_vector_type(2)));
typedef unsigned u32x4 __attribute__((ext_vector_type(4)));
typedef __bf16 bf16x2_t __attribute__((ext_vector_type(2)));
typedef LAS char* lptr;
typedef LAS const char* lcptr;

static constexpr int S_ = 2048, D_ = 640, H_ = 32, DH_ = 20, F_ = 1728;
static constexpr size_t WS_FLAG = 0;
static constexpr size_t FL_E = 0, FL_START = 1024, FL_A = 4096, FL_B = 8192, FL_C = 12288, FL_D = 16384, FL_P = 20480;
static constexpr size_t WS_QKV = 32768;
static constexpr size_t WS_O = WS_QKV + (size_t)480 * 16384;
static constexpr size_t WS_H = WS_O + (size_t)80 * 32768;
static constexpr size_t WS_HH = WS_H + (size_t)80 * 32768;
static constexpr size_t WS_CSP = WS_HH + (size_t)216 * 32768;
static constexpr size_t WS_PS = WS_CSP + (size_t)16 * 16 * 224 * 4;
static constexpr size_t WS_S1 = WS_PS + (size_t)16 * 8 * 128 * 8;
static constexpr size_t WS_PWO = WS_S1 + (size_t)2048 * 8;
static constexpr size_t WS_PWD = WS_PWO + (size_t)8 * 20 * 5 * 1024;
static constexpr size_t WS_PWG = WS_PWD + (size_t)8 * 54 * 5 * 1024;
static constexpr size_t WS_END = WS_PWG + (size_t)16 * 40 * 7 * 1024;
static constexpr int LDS_BYTES = 163840;
static constexpr float C2_ = 0.22360679774997896f * 1.4426950408889634f;

struct Params { const u16* x; const u16* wqkv; const u16* wo; const u16* wg; const u16* wu; const u16* wd; char* out; char* ws; };

#define STAMP(i) do {} while (0)
#define STAMPC(i) do {} while (0)
#define STAMPW(i, w) do {} while (0)

DI unsigned pk2(float a, float b) { f32x2 v = {a, b}; return __builtin_bit_cast(unsigned, __builtin_convertvector(v, bf16x2_t)); }
DI float bflo(unsigned u) { return __uint_as_float(u << 16); }
DI float bfhi(unsigned u) { return __uint_as_float(u & 0xffff0000u); }
DI float rbf(float a) { return bflo(pk2(a, 0.f) & 0xffffu); }
template <bool SC1> DI void glds16(unsigned voff, const void* sbase, unsigned ldsaddr) {
    unsigned keep;
    if (SC1) asm volatile("s_mov_b32 %0, m0\n\ts_mov_b32 m0, %3\n\ts_nop 0\n\tglobal_load_lds_dwordx4 %1, %2 sc1\n\ts_mov_b32 m0, %0" : "=&s"(keep) : "v"(voff), "s"(sbase), "s"(ldsaddr) : "memory");
    else asm volatile("s_mov_b32 %0, m0\n\ts_mov_b32 m0, %3\n\ts_nop 0\n\tglobal_load_lds_dwordx4 %1, %2\n\ts_mov_b32 m0, %0" : "=&s"(keep) : "v"(voff), "s"(sbase), "s"(ldsaddr) : "memory");
}
#define GL_(v, off, sc) "global_load_lds_dwordx4 %" #v ", %[g] offset:" #off sc "\n\t"
#define GLHEAD "s_mov_b32 m0, %[l]\n\ts_nop 0\n\t"
template <bool SC1> DI void gl_lin5(unsigned v, const char* g, unsigned l) {
    if (SC1) asm volatile(GLHEAD GL_(0, -2048, " sc1") GL_(0, -1024, " sc1") GL_(0, 0, " sc1") GL_(0, 1024, " sc1") GL_(0, 2048, " sc1") :: "v"(v), [g] "s"(g), [l] "s"(l) : "memory");
    else asm volatile(GLHEAD GL_(0, -2048, "") GL_(0, -1024, "") GL_(0, 0, "") GL_(0, 1024, "") GL_(0, 2048, "") :: "v"(v), [g] "s"(g), [l] "s"(l) : "memory");
}
template <bool SC1> DI void gl_lin7(unsigned v, const char* g, unsigned l) {
    if (SC1) asm volatile(GLHEAD GL_(0, -3072, " sc1") GL_(0, -2048, " sc1") GL_(0, -1024, " sc1") GL_(0, 0, " sc1") GL_(0, 1024, " sc1") GL_(0, 2048, " sc1") GL_(0, 3072, " sc1") :: "v"(v), [g] "s"(g), [l] "s"(l) : "memory");
    else asm volatile(GLHEAD GL_(0, -3072, "") GL_(0, -2048, "") GL_(0, -1024, "") GL_(0, 0, "") GL_(0, 1024, "") GL_(0, 2048, "") GL_(0, 3072, "") :: "v"(v), [g] "s"(g), [l] "s"(l) : "memory");
}
template <bool SC1> DI void gl_str4(const unsigned* v, const char* g, unsigned l) {
    if (SC1) asm volatile(GLHEAD GL_(0, -2048, " sc1") GL_(1, -1024, " sc1") GL_(2, 0, " sc1") GL_(3, 1024, " sc1") :: "v"(v[0]), "v"(v[1]), "v"(v[2]), "v"(v[3]), [g] "s"(g), [l] "s"(l) : "memory");
    else asm volatile(GLHEAD GL_(0, -2048, "") GL_(1, -1024, "") GL_(2, 0, "") GL_(3, 1024, "") :: "v"(v[0]), "v"(v[1]), "v"(v[2]), "v"(v[3]), [g] "s"(g), [l] "s"(l) : "memory");
}
template <bool SC1> DI void gl_str4w(const unsigned* v, const char* g, unsigned l) {
    if (SC1) asm volatile(GLHEAD GL_(0, -4096, " sc1") GL_(1, -2048, " sc1") GL_(2, 0, " sc1") GL_(3, 2048, " sc1") :: "v"(v[0]), "v"(v[1]), "v"(v[2]), "v"(v[3]), [g] "s"(g), [l] "s"(l) : "memory");
    else asm volatile(GLHEAD GL_(0, -4096, "") GL_(1, -2048, "") GL_(2, 0, "") GL_(3, 2048, "") :: "v"(v[0]), "v"(v[1]), "v"(v[2]), "v"(v[3]), [g] "s"(g), [l] "s"(l) : "memory");
}
DI void gl_str8(const unsigned* v, const char* g, unsigned l) {
    asm volatile(GLHEAD GL_(0, -4096, "") GL_(1, -3072, "") GL_(2, -2048, "") GL_(3, -1024, "") GL_(4, 0, "") GL_(5, 1024, "") GL_(6, 2048, "") GL_(7, 3072, "")
                 :: "v"(v[0]), "v"(v[1]), "v"(v[2]), "v"(v[3]), "v"(v[4]), "v"(v[5]), "v"(v[6]), "v"(v[7]), [g] "s"(g), [l] "s"(l) : "memory");
}
template <bool SC1> DI void st16(void* ptr, u32x4 v) {
    if (SC1) asm volatile("global_store_dwordx4 %0, %1, off sc1\n\ts_nop 1" :: "v"(ptr), "v"(v) : "memory");
    else *(u32x4*)ptr = v;
}
DI u64 ld_dev(const void* p_) { return __hip_atomic_load((const u64*)p_, __ATOMIC_RELAXED, __HIP_MEMORY_SCOPE_AGENT); }
DI void st_dev(void* p_, u64 v) { __hip_atomic_store((u64*)p_, v, __ATOMIC_RELAXED, __HIP_MEMORY_SCOPE_AGENT); }
DI void ld64_async(u32x2& v, const void* ptr) { asm volatile("global_load_dwordx2 %0, %1, off sc1" : "=v"(v) : "v"(ptr) : "memory"); }
DI void ld32_async(float& v, const void* ptr) { asm volatile("global_load_dword %0, %1, off sc1" : "=v"(v) : "v"(ptr) : "memory"); }
DI void poll_flags(const void* flag, bool need, u64 tag) {
    for (unsigned spins = 0; spins < (1u << 17); ++spins) {
        const u64 v = need ? ld_dev(flag) : tag;
        if (__all(v == tag)) break;
        __builtin_amdgcn_s_sleep(1);
    }
    asm volatile("" ::: "memory");
}
DI void warm64(const void* base, unsigned voff, unsigned lds0) {
    unsigned keep;
    asm volatile("s_mov_b32 %0, m0\n\ts_mov_b32 m0, %3\n\ts_nop 0\n\tglobal_load_lds_dword %1, %2 sc1\n\ts_mov_b32 m0, %0" : "=&s"(keep) : "v"(voff), "s"(base), "s"(lds0 + (unsigned)(LDS_BYTES - 256)) : "memory");
}
#define VMWAIT(N) asm volatile("s_waitcnt vmcnt(%0)" :: "i"(N) : "memory")
#define BAR_L() asm volatile("s_waitcnt lgkmcnt(0)\n\ts_barrier" ::: "memory")
#define BAR_VL(N) asm volatile("s_waitcnt vmcnt(%0) lgkmcnt(0)\n\ts_barrier" :: "i"(N) : "memory")
#define WAIT_LGKM0() do { __builtin_amdgcn_sched_barrier(0); __builtin_amdgcn_s_waitcnt(0xC07F); __builtin_amdgcn_sched_barrier(0); } while (0)
#define CBAR() do { WAIT_LGKM0(); asm volatile("" ::: "memory"); __builtin_amdgcn_s_barrier(); asm volatile("" ::: "memory"); __builtin_amdgcn_sched_barrier(0); } while (0)
#define SGB(mask, n) __builtin_amdgcn_sched_group_barrier(mask, n, 0)
template <int STEP, int MAXN> DI void bar_vl_n(int n) {
    if (n >= MAXN) { BAR_VL((MAXN * STEP > 63 ? 63 : MAXN * STEP)); }
    else if constexpr (MAXN > 0) bar_vl_n<STEP, MAXN - 1>(n);
}
template <int N> struct IC { static constexpr int v = N; };
template <int K, int END, class F> DI void static_for(F&& f) { if constexpr (K < END) { f(IC<K>{}); static_for<K + 1, END>(f); } }
template <int R, int NST, int NP, int LAG, bool PROLOGUE, int NEND = NST, class FI, class FP, class FQ>
DI void ring_loader(FI&& issue, FP&& pre, FQ&& post) {
    constexpr int L0 = (R < NST ? R : NST) - 1;
    if (PROLOGUE) {
#pragma unroll 1
        for (int k = 0; k <= L0; ++k) issue();
    }
    static_for<0, LAG + 1>([&](auto kc) { constexpr int k = decltype(kc)::v; BAR_VL((L0 - k) * NP); pre(k); if constexpr (k >= LAG && k - LAG + R < NST) issue(); post(k); });
    constexpr int KS = NST - R + LAG;
    if constexpr (KS > LAG + 1) {
#pragma unroll 1
        for (int k = LAG + 1; k < KS; ++k) { BAR_VL((R - 1 - LAG) * NP); pre(k); issue(); post(k); }
    }
    static_for<(KS > LAG + 1 ? KS : LAG + 1), NEND>([&](auto kc) { constexpr int k = decltype(kc)::v; BAR_VL((NST - 1 - k) * NP); pre(k); post(k); });
}
DI bf16x8 ld128(lcptr p) { return *(const LAS bf16x8*)p; }
DI s16x4 ld64(lcptr p) { return *(const LAS s16x4*)p; }
DI s16x4 ld64v(lcptr p) { return *(const volatile LAS s16x4*)p; }
DI s16x4 ldtr(lcptr p) { typedef short v4 __attribute__((ext_vector_type(4))); return __builtin_bit_cast(s16x4, __builtin_amdgcn_ds_read_tr16_b64_v4i16((LAS v4*)p)); }
DI bf16x8 cat8(s16x4 a, s16x4 b) { return __builtin_shufflevector(a, b, 0, 1, 2, 3, 4, 5, 6, 7); }
#define MFMA16(a, b, c) __builtin_amdgcn_mfma_f32_16x16x32_bf16(a, b, c, 0, 0, 0)
#define MFMA32(a, b, c) __builtin_amdgcn_mfma_f32_32x32x16_bf16(a, b, c, 0, 0, 0)
#define MFMA32K8(a, b, c) __builtin_amdgcn_mfma_f32_32x32x8bf16_1k(a, b, c, 0, 0, 0)
template <int CTRL> DI float dpp_add(float v) { return v + __int_as_float(__builtin_amdgcn_update_dpp(0, __float_as_int(v), CTRL, 0xf, 0xf, true)); }
DI float sum8(float v) { v = dpp_add<0xB1>(v); v = dpp_add<0x4E>(v); return dpp_add<0x141>(v); }
DI float sum_x16(float v) { auto r = __builtin_amdgcn_permlane16_swap(__float_as_uint(v), __float_as_uint(v), false, false); return __uint_as_float(r[0]) + __uint_as_float(r[1]); }
DI float sum_x32(float v) { auto r = __builtin_amdgcn_permlane32_swap(__float_as_uint(v), __float_as_uint(v), false, false); return __uint_as_float(r[0]) + __uint_as_float(r[1]); }
DI bf16x8 ones8() { bf16x8 o = {0x3f80, 0x3f80, 0x3f80, 0x3f80, 0x3f80, 0x3f80, 0x3f80, 0x3f80}; asm volatile("" : "+v"(o)); return o; }

DI f32x2 ln_scalars(float sum, float sq) {
    const float mean = sum * (1.0f / D_);
    float var = sq * (1.0f / D_) - mean * mean; var = var > 0.f ? var : 0.f;
    const float sd = rbf(__builtin_sqrtf(rbf(rbf(var) + 1e-5f)));
    return (f32x2){rbf(mean), 1.0f / sd};
}
template <bool FUSED> DI void publish(const Params& p, size_t flag_off, int wid, int lane, u64 tag, int sidx = 47) {
    if (FUSED) { BAR_VL(0); STAMP(sidx); if (wid == 0 && lane == 0) st_dev(p.ws + WS_FLAG + flag_off, tag); }
}

namespace rp { constexpr int L_G = 132608, L_U = L_G + 10752, L_O = L_G + 21504, L_D = 102400; }
DI void rp1_addr(const Params& p, int lane, int lw, int ti, int tj, const char* (&ga)[7]) {
    const int ns = ti < 8 ? 3 : 2, s0 = ti < 8 ? 3 * ti : 24 + 2 * (ti - 8);
    const int ct = tj >> 1, z = 2 * ti + (tj & 1);
#pragma unroll
    for (int i = 0; i < 7; ++i) { const int n = lw + 4 * i;
        if (n < 21) { int u = 64 * n + lane; const int mat = u >= 672; u -= 672 * mat; int row = u / 14; int col = 112 * tj + 8 * (u - 14 * row); col = col > F_ - 8 ? F_ - 8 : col;
            row = row < 16 * ns ? row : 16 * ns - 1;
            ga[i] = (const char*)((mat ? p.wu : p.wg) + (size_t)(16 * s0 + row) * F_ + col);
        } else { const int m = n < 26 ? n - 21 : 4, u = 64 * m + lane, row = u / 10, c = u - 10 * row;
            ga[i] = (const char*)(p.wo + (size_t)(32 * (z < 20 ? z : 0) + row) * D_ + 80 * ct + 8 * c); } }
}
DI void glds16v(const void* vaddr, unsigned ldsaddr) {
    asm volatile("s_nop 0\n\ts_mov_b32 m0, %1\n\ts_nop 0\n\tglobal_load_lds_dwordx4 %0, off" :: "v"(vaddr), "s"(ldsaddr) : "memory");
}
DI void rp1_issue(const char* (&ga)[7], unsigned lds0, int lw) {
#pragma unroll
    for (int i = 0; i < 7; ++i) { const int n = lw + 4 * i; glds16v(ga[i], (unsigned)__builtin_amdgcn_readfirstlane(lds0 + rp::L_G + (n < 26 ? n : 25) * 1024)); }
}
template <bool FUSED>
DI void rp1_store(const Params& p, lptr L, int lane, int lw, int ti, int tj) {
    const int ns = ti < 8 ? 3 : 2, s0 = ti < 8 ? 3 * ti : 24 + 2 * (ti - 8);
    const int ct = tj >> 1, z = 2 * ti + (tj & 1);
    const int h = lane >> 5, blk = (lane >> 4) & 1, g = lane >> 4, q = (lane & 15) >> 2, pp = lane & 3;
    const bf16x8 ones = ones8();
#pragma unroll 1
    for (int t = lw; t < 7; t += 4) {
        lcptr src = (lcptr)L + (t < 6 ? ((t & 1) ? rp::L_U : rp::L_G) + (32 * (t >> 1) + 16 * blk + 4 * pp) * 2 : (blk ? rp::L_U : rp::L_G) + (96 + 4 * pp) * 2) + (8 * h + q) * 224;
        s16x4 f[3][2];
#pragma unroll
        for (int s = 0; s < 3; ++s) { f[s][0] = ldtr(src + s * 16 * 224); f[s][1] = ldtr(src + s * 16 * 224 + 4 * 224); }
        f32x16 cs = {};
        char* dst = p.ws + WS_PWG + ((size_t)(tj * 40 + s0) * 7 + t) * 1024 + lane * 16;
#pragma unroll
        for (int s = 0; s < 3; ++s) if (s < ns) { const bf16x8 fr = cat8(f[s][0], f[s][1]); st16<FUSED>(dst + s * 7168, __builtin_bit_cast(u32x4, fr)); cs = MFMA32(fr, ones, cs); }
        if ((lane & 31) == 0) { float* cd = (float*)(p.ws + WS_CSP) + (ti * 16 + tj) * 224 + 32 * t + 4 * h;
#pragma unroll
            for (int u = 0; u < 4; ++u) st16<FUSED>(cd + 8 * u, __builtin_bit_cast(u32x4, ((f32x4){cs[4 * u], cs[4 * u + 1], cs[4 * u + 2], cs[4 * u + 3]}))); }
    }
    if (z < 20) {
#pragma unroll 1
        for (int nt = 3 - lw; nt < 5; nt += 4) {
            lcptr src = (lcptr)L + rp::L_O + (8 * g + q) * 160 + (16 * nt + 4 * pp) * 2;
            st16<FUSED>(p.ws + WS_PWO + ((size_t)(ct * 20 + z) * 5 + nt) * 1024 + lane * 16, __builtin_bit_cast(u32x4, cat8(ldtr(src), ldtr(src + 4 * 160)))); }
    }
}
DI void rp2_issue(const Params& p, unsigned lds0, int lane, int wid, int ti, int tj) {
    const int ct = tj >> 1, z = 2 * ti + (tj & 1), np = z + 32 < 54 ? 10 : 5;
#pragma unroll 1
    for (int n = wid; n < np; n += 8) { const int im = n >= 5, u = 64 * (n - 5 * im) + lane, row = u / 10, c = u - 10 * row;
        glds16<false>((unsigned)((32 * (z + 32 * im) + row) * D_ + 80 * ct + 8 * c) * 2u, p.wd, lds0 + rp::L_D + n * 1024); }
}
template <bool FUSED>
DI void rp2_store(const Params& p, lptr L, int lane, int wid, int ti, int tj) {
    const int ct = tj >> 1, z = 2 * ti + (tj & 1), np = z + 32 < 54 ? 10 : 5;
    const int g = lane >> 4, q = (lane & 15) >> 2, pp = lane & 3;
#pragma unroll 1
    for (int n = wid; n < np; n += 8) { const int im = n >= 5, nt = n - 5 * im;
        lcptr src = (lcptr)L + rp::L_D + im * 5120 + (8 * g + q) * 160 + (16 * nt + 4 * pp) * 2;
        st16<FUSED>(p.ws + WS_PWD + ((size_t)(ct * 54 + z + 32 * im) * 5 + nt) * 1024 + lane * 16, __builtin_bit_cast(u32x4, cat8(ldtr(src), ldtr(src + 4 * 160)))); }
}
DI u64 make_tag(u32x2& ev) {
    asm volatile("" : "+v"(ev));
    const u64 e = (((u64)(unsigned)__builtin_amdgcn_readfirstlane(ev[1]) << 32) | (unsigned)__builtin_amdgcn_readfirstlane(ev[0])) + 1;
    return e;
}

namespace pa { constexpr int R = 4, NST = 10, LA = 0, LB = R * 16384, L_STAT = 2 * R * 16384, L_CS = L_STAT + 1024; }
template <bool FUSED>
DI void phase_a_stats(const Params& p, lptr L, int lane, int wid, int ti, u32x2& ev, u64& tag) {
    const int r = lane >> 3, c = lane & 7;
    if (wid >= 4) { const char* ga[7]; rp1_addr(p, lane, wid - 4, ti, 15, ga); rp1_issue(ga, (unsigned)(uintptr_t)L, wid - 4); }
#pragma unroll
    for (int hlf = 0; hlf < 2; ++hlf) {
        const int row = 128 * ti + 16 * wid + 8 * hlf + r;
        const u32x4* src = (const u32x4*)(p.x + (size_t)row * D_) + c;
        u32x4 v[10];
#pragma unroll
        for (int i = 0; i < 10; ++i) v[i] = src[8 * i];
        float a = 0.f, q = 0.f;
#pragma unroll
        for (int i = 0; i < 10; ++i)
#pragma unroll
            for (int e = 0; e < 4; ++e) { const float lo = bflo(v[i][e]), hi = bfhi(v[i][e]); a += lo + hi; q += lo * lo + hi * hi; }
        const f32x2 st = ln_scalars(sum8(a), sum8(q));
        if (c == 0) { const u64 w = ((u64)__float_as_uint(st.y) << 32) | __float_as_uint(st.x); char* dst = p.ws + WS_S1 + (size_t)row * 8; if (FUSED) st_dev(dst, w); else *(u64*)dst = w; }
    }
    BAR_VL(0);
    if (FUSED) { tag = make_tag(ev); if (wid == 0 && lane == 0) st_dev(p.ws + WS_FLAG + FL_A + (ti * 16 + 15) * 8, tag); }
    if (wid >= 4) rp1_store<FUSED>(p, L, lane, wid - 4, ti, 15);
    if (FUSED) { BAR_VL(0); if (wid == 0 && lane == 0) st_dev(p.ws + WS_FLAG + FL_P + (ti * 16 + 15) * 8, tag); }
}
template <bool FUSED>
DI void phase_a(const Params& p, lptr L, int lane, int wid, int ti, int tj, int rot, u32x2& ev, u64& tag) {
    using namespace pa;
    const unsigned lds0 = (unsigned)(uintptr_t)L;
    if (wid >= 4) {
        const int lw = wid - 4, r = lane >> 3, c = lane & 7;
        const bool isA = lw < 2;
        unsigned vo[2];
#pragma unroll
        for (int e = 0; e < 2; ++e) vo[e] = isA ? (unsigned)(r * D_ + ((c ^ ((4 * e + (r >> 1)) & 7)) * 8)) * 2u : (unsigned)(r * 3 * D_ + ((c ^ (4 * ((r >> 1) & 1) + 2 * e)) * 8)) * 2u;
        unsigned vp[8];
#pragma unroll
        for (int i = 0; i < 8; ++i) vp[i] = vo[i & 1] + (isA ? (unsigned)i * (8 * D_ * 2 - 1024) : (unsigned)(i & 3) * (8 * 3 * D_ * 2 - 1024));
        const char* gb = isA ? (const char*)p.x + (size_t)(128 * ti + 64 * lw) * D_ * 2 + 4096 + rot * 128 : (const char*)p.wqkv + ((size_t)(32 * (lw - 2) + 64 * rot) * 3 * D_ + 128 * tj) * 2 + 2048;
        const int gstep = isA ? 128 : 64 * 3 * D_ * 2;
        int ks = rot; unsigned sl = lds0 + (isA ? LA + lw * 8192 + 4096 : LB + (lw - 2) * 4096 + 2048), sslot = 0;
        auto issue = [&]() {
            if (isA) gl_str8(vp, gb, sl + sslot); else { gl_str4<false>(vp, gb, sl + sslot); gl_str4<false>(vp, gb + 128, sl + sslot + 8192); }
            gb += gstep; if (++ks == NST) { ks = 0; gb -= (size_t)NST * gstep; }
            sslot = (sslot + 16384) & (R * 16384 - 1);
        };
        const int g = lane >> 4, q = (lane & 15) >> 2, pp = lane & 3;
        unsigned aC[2];
#pragma unroll
        for (int t = 0; t < 2; ++t) aC[t] = (unsigned)(LB + ((lw >> 1) * 8 + g) * 1024 + q * 128 + (((2 * (2 * (lw & 1) + t) + (pp >> 1)) ^ (4 * ((q >> 1) & 1) + 2 * (g & 1))) * 16) + (pp & 1) * 8);
        f32x4 cs[2] = {{0.f, 0.f, 0.f, 0.f}, {0.f, 0.f, 0.f, 0.f}};
        const bf16x8 ones = ones8();
        s16x4 cf[2][2][2];
        __builtin_amdgcn_s_setprio(3);
        auto pre = [&](int s) { lcptr q0 = (lcptr)L + (s & 3) * 16384;
#pragma unroll
            for (int ks2 = 0; ks2 < 2; ++ks2)
#pragma unroll
                for (int t = 0; t < 2; ++t) { cf[ks2][t][0] = ldtr(q0 + aC[t] + ks2 * 4096); cf[ks2][t][1] = ldtr(q0 + aC[t] + ks2 * 4096 + 512); } };
        auto post = [&](int) {
#pragma unroll
            for (int ks2 = 0; ks2 < 2; ++ks2)
#pragma unroll
                for (int t = 0; t < 2; ++t) cs[t] = MFMA16(cat8(cf[ks2][t][0], cf[ks2][t][1]), ones, cs[t]); };
        const char* ga[7]; rp1_addr(p, lane, lw, ti, tj, ga);
        issue(); issue();
        BAR_VL(8); pre(0); issue(); rp1_issue(ga, lds0, lw);
        if (FUSED) tag = make_tag(ev);
        BAR_VL(15); post(0); pre(1); issue();
        BAR_VL(15); post(1); pre(2); issue();
#pragma unroll 1
        for (int k = 3; k < NST - 2; ++k) { BAR_VL(8); post(k - 1); pre(k); issue(); }
        BAR_VL(8); post(NST - 3); pre(NST - 2);
        BAR_VL(0); post(NST - 2); pre(NST - 1); post(NST - 1);
        if (isA) {
            if (FUSED) poll_flags(p.ws + WS_FLAG + FL_A + (ti * 16 + 15) * 8, lane == 0, tag);
            const char* src = p.ws + WS_S1 + (size_t)(128 * ti + 64 * lw + lane) * 8;
            const u64 w = FUSED ? ld_dev(src) : *(const u64*)src;
            *(LAS u64*)(L + L_STAT + (64 * lw + lane) * 8) = w;
        }
        __builtin_amdgcn_s_setprio(0);
        if ((lane & 15) == 0) {
#pragma unroll
            for (int t = 0; t < 2; ++t) *(LAS f32x4*)(L + L_CS + (32 * lw + 16 * t + 4 * g) * 4) = cs[t];
        }
        BAR_L();
        rp1_store<FUSED>(p, L, lane, lw, ti, tj);
        BAR_L();
    } else {
        const int wm = wid >> 1, wn = wid & 1, r32 = lane & 31, h = lane >> 5, blk = (lane >> 4) & 1, q = (lane & 15) >> 2, pp = lane & 3;
        unsigned aX[4], aW[2];
        { const int row = 64 * wm + r32;
#pragma unroll
          for (int u = 0; u < 4; ++u) aX[u] = (unsigned)(LA + (row >> 3) * 1024 + (row & 7) * 128 + (((2 * u + h) ^ ((row >> 1) & 7)) * 16));
#pragma unroll
          for (int nt = 0; nt < 2; ++nt) aW[nt] = (unsigned)(LB + (wn * 8 + h) * 1024 + q * 128 + (((4 * nt + 2 * blk + (pp >> 1)) ^ (4 * ((q >> 1) & 1) + 2 * h)) * 16) + (pp & 1) * 8); }
        f32x16 acc[2][2];
#pragma unroll
        for (int i = 0; i < 16; ++i) { acc[0][0][i] = 0.f; acc[0][1][i] = 0.f; acc[1][0][i] = 0.f; acc[1][1][i] = 0.f; }
        bf16x8 X[4][2], W[4][2];
        auto rd = [&](int slot, int u) {
            lcptr sl = (lcptr)L + slot * 16384;
#pragma unroll
            for (int nt = 0; nt < 2; ++nt) W[u][nt] = cat8(ldtr(sl + aW[nt] + u * 2048), ldtr(sl + aW[nt] + u * 2048 + 512));
#pragma unroll
            for (int mt = 0; mt < 2; ++mt) X[u][mt] = ld128(sl + aX[u] + mt * 4096);
        };
        auto mm = [&](int u) {
#pragma unroll
            for (int mt = 0; mt < 2; ++mt)
#pragma unroll
                for (int nt = 0; nt < 2; ++nt) acc[mt][nt] = MFMA32(W[u][nt], X[u][mt], acc[mt][nt]);
        };
        auto il = [&]() { SGB(0x8, 1); SGB(0x100, 2); SGB(0x8, 1); SGB(0x100, 2); SGB(0x8, 1); SGB(0x100, 2); SGB(0x8, 1); __builtin_amdgcn_sched_barrier(0); };
        auto bar = [&]() { __builtin_amdgcn_sched_barrier(0); asm volatile("" ::: "memory"); __builtin_amdgcn_s_barrier(); asm volatile("" ::: "memory"); __builtin_amdgcn_sched_barrier(0); };
        bar();
        if (FUSED) VMWAIT(0);
        STAMP(1); STAMPC(26);
        rd(0, 0); rd(0, 1);
#pragma unroll 1
        for (int s = 0; s < NST - 1; ++s) {
            const int c = s & 3, n = (s + 1) & 3;
            rd(c, 2); mm(0); il();
            rd(c, 3); mm(1); il();
            bar();
            rd(n, 0); mm(2); il();
            rd(n, 1); mm(3); il();
        }
        rd((NST - 1) & 3, 2); mm(0); il();
        rd((NST - 1) & 3, 3); mm(1); il();
        mm(2); mm(3);
        STAMP(2); STAMPC(27);
        if (FUSED) tag = make_tag(ev);
        BAR_L();
        STAMP(38);
#pragma unroll
        for (int mt = 0; mt < 2; ++mt) { const int tok = 64 * wm + 32 * mt + r32; const f32x2 st = *(const LAS f32x2*)(L + L_STAT + tok * 8);
#pragma unroll
            for (int nt = 0; nt < 2; ++nt)
#pragma unroll
                for (int u = 0; u < 4; ++u) { const int col = 64 * wn + 32 * nt + 8 * u + 4 * h;
                    const f32x4 cs = *(const LAS f32x4*)(L + L_CS + col * 4);
                    const float sc = (128 * tj + col < D_) ? st.y * C2_ : st.y;
                    const f32x4 a = {acc[mt][nt][4 * u], acc[mt][nt][4 * u + 1], acc[mt][nt][4 * u + 2], acc[mt][nt][4 * u + 3]};
                    const f32x4 v = (a - cs * st.x) * sc;
                    *(LAS u32x2*)(L + (col >> 2) * 1024 + tok * 8) = (u32x2){pk2(v[0], v[1]), pk2(v[2], v[3])}; } }
        BAR_L();
        STAMP(39);
    }
    char* dst = p.ws + WS_QKV + (size_t)(32 * tj) * 16384 + (size_t)ti * 1024;
#pragma unroll
    for (int i = 0; i < 4; ++i) { const int gq = 4 * wid + i; st16<FUSED>(dst + (size_t)gq * 16384 + lane * 16, *(const LAS u32x4*)(L + gq * 1024 + lane * 16)); }
    STAMP(3);
    publish<FUSED>(p, FL_A + (ti * 16 + tj) * 8, wid, lane, tag, 42);
    if (FUSED && wid == 1 && lane == 0) st_dev(p.ws + WS_FLAG + FL_P + (ti * 16 + tj) * 8, tag);
}

namespace pb {
constexpr int GS = 3104;
constexpr int L_K = 0, L_V = 10 * GS, L_ONE = 20 * GS, L_Q = 21 * GS, L_O = 102400;
static_assert(L_Q + 10240 <= L_O, "LDS map");
}
#define ATT_STAMP() do {} while (0)
template <bool EDGE>
DI f32x16 attn_wave(lcptr L, int hh, int qb, int lane, int tstart, u64* stq) {
    using namespace pb;
    const int r = lane & 31, h = lane >> 5;
    bf16x8 q0; s16x4 q1;
    { lcptr qp = L + L_Q + hh * 5 * 1024 + (32 * qb + r) * 8;
      q0 = cat8(ld64(qp + (2 * h) * 1024), ld64(qp + (2 * h + 1) * 1024));
      const s16x4 t = ld64(qp + 4 * 1024); q1 = h ? (s16x4){0, 0, 0, 0} : t; }
    lcptr kp = L + L_K + hh * 5 * GS + (32 * qb + r) * 8;
    const f32x16 z16 = {};
    bf16x8 k0[9]; s16x4 k1[9];
#pragma unroll
    for (int t = 0; t < 9; ++t) { k0[t] = cat8(ld64v(kp + (2 * h) * GS + t * 256), ld64v(kp + (2 * h + 1) * GS + t * 256)); k1[t] = ld64v(kp + 4 * GS + t * 256); }
    float m;
    { f32x16 a = MFMA32K8(k1[8], q1, MFMA32(k0[8], q0, z16)), b = MFMA32K8(k1[0], q1, MFMA32(k0[0], q0, z16));
      const float bias = (!EDGE || tstart == 0) ? 0.f : -INFINITY;
#pragma unroll
      for (int i = 0; i < 16; ++i) { const int c = (i & 3) + 8 * (i >> 2) + 4 * h; a[i] = (c <= r) ? a[i] : (EDGE ? b[i] + bias : b[i]); }
      float x = __builtin_fmaxf(__builtin_fmaxf(a[0], a[1]), a[2]), y = __builtin_fmaxf(__builtin_fmaxf(a[3], a[4]), a[5]);
#pragma unroll
      for (int i = 6; i < 14; i += 4) { x = __builtin_fmaxf(__builtin_fmaxf(x, a[i]), a[i + 1]); y = __builtin_fmaxf(__builtin_fmaxf(y, a[i + 2]), a[i + 3]); }
      m = __builtin_fmaxf(__builtin_fmaxf(__builtin_fmaxf(x, a[14]), a[15]), y); }
#pragma unroll
    for (int t = 1; t < 8; ++t) { const f32x16 a = MFMA32K8(k1[t], q1, MFMA32(k0[t], q0, z16));
        float x = __builtin_fmaxf(__builtin_fmaxf(a[0], a[1]), a[2]), y = __builtin_fmaxf(__builtin_fmaxf(a[3], a[4]), a[5]);
#pragma unroll
        for (int i = 6; i < 14; i += 4) { x = __builtin_fmaxf(__builtin_fmaxf(x, a[i]), a[i + 1]); y = __builtin_fmaxf(__builtin_fmaxf(y, a[i + 2]), a[i + 3]); }
        x = __builtin_fmaxf(__builtin_fmaxf(x, a[14]), a[15]);
        if (EDGE) x = t >= tstart ? x : -INFINITY;
        m = __builtin_fmaxf(__builtin_fmaxf(m, x), (EDGE && t < tstart) ? -INFINITY : y); }
    { auto rr = __builtin_amdgcn_permlane32_swap(__float_as_uint(m), __float_as_uint(m), false, false); m = __builtin_fmaxf(__uint_as_float(rr[0]), __uint_as_float(rr[1])); }
    f32x16 nm16;
#pragma unroll
    for (int i = 0; i < 16; ++i) nm16[i] = -m;
    BAR_VL(0);
    ATT_STAMP();
    const int blk = (lane >> 4) & 1, vq = (lane & 15) >> 2, vp = lane & 3, dg = 4 * blk + vp;
    lcptr vpz = L + (dg < 5 ? L_V + (hh * 5 + dg) * GS : L_ONE) + (32 * qb + 4 * h + vq) * 8;
    f32x16 O = z16;
#pragma unroll
    for (int t = 1; t < 8; ++t) {
        const bf16x8 v0 = cat8(ldtr(vpz + t * 256), ldtr(vpz + t * 256 + 64)), v1 = cat8(ldtr(vpz + t * 256 + 128), ldtr(vpz + t * 256 + 192));
        f32x16 a = MFMA32K8(k1[t], q1, MFMA32(k0[t], q0, nm16));
        if (EDGE) { const float bias = t >= tstart ? 0.f : -INFINITY;
#pragma unroll
            for (int i = 0; i < 16; ++i) a[i] += bias; }
        unsigned P[8];
#pragma unroll
        for (int i = 0; i < 8; ++i) P[i] = pk2(__builtin_amdgcn_exp2f(a[2 * i]), __builtin_amdgcn_exp2f(a[2 * i + 1]));
        O = MFMA32(v0, __builtin_bit_cast(bf16x8, ((u32x4){P[0], P[1], P[2], P[3]})), O);
        O = MFMA32(v1, __builtin_bit_cast(bf16x8, ((u32x4){P[4], P[5], P[6], P[7]})), O);
    }
    { const bf16x8 v0 = cat8(ldtr(vpz + 8 * 256), ldtr(vpz + 8 * 256 + 64)), v1 = cat8(ldtr(vpz + 8 * 256 + 128), ldtr(vpz + 8 * 256 + 192));
      const bf16x8 w0 = cat8(ldtr(vpz), ldtr(vpz + 64)), w1 = cat8(ldtr(vpz + 128), ldtr(vpz + 192));
      f32x16 a = MFMA32K8(k1[8], q1, MFMA32(k0[8], q0, nm16)); const f32x16 b = MFMA32K8(k1[0], q1, MFMA32(k0[0], q0, nm16));
      const float bias = (!EDGE || tstart == 0) ? 0.f : -INFINITY;
#pragma unroll
      for (int i = 0; i < 16; ++i) { const int c = (i & 3) + 8 * (i >> 2) + 4 * h; a[i] = (c <= r) ? a[i] : (EDGE ? b[i] + bias : b[i]); }
      unsigned P[8], P0[8];
#pragma unroll
      for (int i = 0; i < 8; ++i) { P[i] = pk2(__builtin_amdgcn_exp2f(a[2 * i]), __builtin_amdgcn_exp2f(a[2 * i + 1]));
          const int c0 = ((2 * i) & 3) + 8 * ((2 * i) >> 2) + 4 * h; const unsigned mk = (c0 <= r ? 0xffffu : 0u) | (c0 + 1 <= r ? 0xffff0000u : 0u);
          P0[i] = P[i] & ~mk; P[i] &= mk; }
      O = MFMA32(v0, __builtin_bit_cast(bf16x8, ((u32x4){P[0], P[1], P[2], P[3]})), O);
      O = MFMA32(v1, __builtin_bit_cast(bf16x8, ((u32x4){P[4], P[5], P[6], P[7]})), O);
      O = MFMA32(w0, __builtin_bit_cast(bf16x8, ((u32x4){P0[0], P0[1], P0[2], P0[3]})), O);
      O = MFMA32(w1, __builtin_bit_cast(bf16x8, ((u32x4){P0[4], P0[5], P0[6], P0[7]})), O); }
    return O;
}
template <bool FUSED>
DI void phase_b(const Params& p, lptr L, int lane, int wid, int ti, int tj, u64 tag) {
    using namespace pb;
    const int tid = wid * 64 + lane;
    const unsigned lds0 = (unsigned)(uintptr_t)L;
    rp2_issue(p, lds0, lane, wid, ti, tj);
    if (ti < 2) {
        for (int n = tid; n < (2 - ti) * 20 * 64; n += 512) { const int pc = n >> 6, mb = pc % (2 - ti), gq = pc / (2 - ti);
            *(LAS u32x4*)(L + gq * GS + mb * 1024 + (n & 63) * 16) = (u32x4){0u, 0u, 0u, 0u}; }
    }
    if (tid < 384) *(LAS u32x2*)(L + L_ONE + tid * 8) = (u32x2){0x00003f80u, 0u};
    const int sub = wid & 3, mtile = sub < 3 ? ti - 2 + sub : ti;
    const char* g0 = p.ws + WS_QKV + (size_t)((sub < 3 ? 160 : 0) + 5 * (2 * tj + (wid >> 2))) * 16384 + (size_t)(mtile < 0 ? 0 : mtile) * 1024;
    const unsigned l0 = lds0 + (sub < 3 ? L_K + (wid >> 2) * 5 * GS + sub * 1024 : L_Q + (wid >> 2) * 5120);
    BAR_VL(0);
    rp2_store<FUSED>(p, L, lane, wid, ti, tj);
    if (FUSED) {
        if (wid == 7) { const int mb = lane / 6, sec = (lane % 6) >> 1, e = lane & 1, mt2 = ti - 2 + mb;
            const bool need = lane < 18 && mt2 >= 0 && (sec > 0 || mb == 2);
            poll_flags(p.ws + WS_FLAG + FL_A + ((mt2 < 0 ? 0 : mt2) * 16 + ((640 * sec + 40 * tj + 39 * e) >> 7)) * 8, need, tag); STAMPW(35, 7); }
    }
    BAR_L();
    STAMP(4);
    if (sub == 3) {
#pragma unroll
        for (int d = 0; d < 5; ++d) glds16<FUSED>(lane * 16, g0 + d * 16384, l0 + d * 1024);
        BAR_VL(0);
    } else if (mtile >= 0) {
#pragma unroll
        for (int d = 0; d < 5; ++d) glds16<FUSED>(lane * 16, g0 + d * 16384, l0 + d * GS);
#pragma unroll
        for (int d = 0; d < 5; ++d) glds16<FUSED>(lane * 16, g0 + (160 + d) * 16384, l0 + L_V + d * GS);
        BAR_VL(5);
    } else BAR_L();
    STAMP(5);
    const int hh = wid >> 2, qb = wid & 3, r = lane & 31, h = lane >> 5;
    const int q0g = 128 * ti + 32 * qb;
    f32x16 O;
    u64* stq = nullptr;
    if (ti >= 2) O = attn_wave<false>((lcptr)L, hh, qb, lane, 0, stq); else O = attn_wave<true>((lcptr)L, hh, qb, lane, q0g >= 256 ? 0 : 8 - (q0g >> 5), stq);
    { auto rr = __builtin_amdgcn_permlane32_swap(__float_as_uint(O[8]), __float_as_uint(O[8]), false, false);
      const float il = 1.0f / __uint_as_float(rr[1]);
      const int row = 32 * qb + r;
#pragma unroll
      for (int u = 0; u < 3; ++u) { const int dgp = 2 * u + h; if (dgp < 5) { const int cgp = 5 * hh + dgp;
          *(LAS u32x2*)(L + L_O + (cgp >> 1) * 2048 + row * 16 + (cgp & 1) * 8) = (u32x2){pk2(O[4 * u] * il, O[4 * u + 1] * il), pk2(O[4 * u + 2] * il, O[4 * u + 3] * il)}; } } }
    STAMP(6); STAMPW(37, 7);
    BAR_L();
    for (int n = wid; n < 10; n += 8)
        st16<FUSED>(p.ws + WS_O + (size_t)(5 * tj + (n >> 1)) * 32768 + (size_t)(128 * ti + 64 * (n & 1)) * 16 + lane * 16, *(const LAS u32x4*)(L + L_O + n * 1024 + lane * 16));
    STAMP(7);
    publish<FUSED>(p, FL_B + (ti * 16 + tj) * 8, wid, lane, tag);
}

template <int MODE> struct PC;
template <> struct PC<0> { static constexpr int NST = 10, RB = 10, RA = 6, LA = 102400, L_RES = 151552, L_X = 102400, L_STG = 122880; };
template <> struct PC<1> { static constexpr int NST = 27, RB = 8, RA = 8, LA = 81920, L_RES = 147456, L_X = 81920, L_STG = 102400; };
template <bool FUSED, int MODE>
DI void ce_prefetch(const Params& p, lptr L, int lane, int wid, int tj) {
    const char* wb = p.ws + (MODE ? WS_PWD + (size_t)(tj >> 1) * 54 * 5120 : WS_PWO + (size_t)(tj >> 1) * 20 * 5120) + (wid - 6) * 5120 + 2048;
    const unsigned l = (unsigned)(uintptr_t)L + (wid - 6) * 5120 + 2048;
#pragma unroll 1
    for (int s = 0; s < PC<MODE>::RB; ++s) gl_lin5<FUSED>(lane * 16, wb + (size_t)s * 10240, l + s * 10240);
}
template <bool FUSED, int MODE>
DI void phase_ce(const Params& p, lptr L, int lane, int wid, int ti, int tj, u64 tag) {
    typedef PC<MODE> C;
    constexpr int NST = C::NST, RA = C::RA, RB = C::RB;
    const unsigned lds0 = (unsigned)(uintptr_t)L;
    const int rh = tj & 1, ct = tj >> 1, r0 = 128 * ti + 64 * rh;
    const int wm = (wid >> 1) & 1, wk = wid & 1, r16 = lane & 15, g = lane >> 4;
    if (wid >= 6) {
        if (MODE == 0) {
            bar_vl_n<5, RB - 1>(RB - 1);
            BAR_VL(0);
#pragma unroll 1
            for (int s = 2; s < NST; ++s) BAR_L();
        } else {
            const char* wb = p.ws + WS_PWD + (size_t)ct * 54 * 5120 + (wid - 6) * 5120 + 2048 + (size_t)RB * 10240;
            const unsigned l0 = lds0 + (wid - 6) * 5120 + 2048; unsigned l = l0;
            ring_loader<RB, NST, 5, 2, false>([&]() { gl_lin5<FUSED>(lane * 16, wb, l); wb += 10240; l = l == l0 + (RB - 1) * 10240 ? l0 : l + 10240; }, [&](int) {}, [&](int) {});
        }
        BAR_L(); BAR_L(); BAR_L();
    } else if (wid >= 4) {
        const int la = wid - 4;
        const unsigned voA = lane * 16;
#pragma unroll
        for (int i = 0; i < 5; ++i) { const int pc = 5 * la + i;
            if (MODE == 0) { const int n = 64 * pc + lane; glds16<false>((unsigned)((n / 10) * D_ * 2 + (n % 10) * 16), (const char*)p.x + (size_t)r0 * D_ * 2 + ct * 160, lds0 + C::L_RES + pc * 1024); }
            else glds16<FUSED>(voA, p.ws + WS_H + (size_t)(10 * ct + pc) * 32768 + (size_t)r0 * 16, lds0 + C::L_RES + pc * 1024); }
        if (FUSED) poll_flags(p.ws + WS_FLAG + (MODE ? FL_D : FL_B) + (ti * 16 + (lane & 15)) * 8, lane < 16, tag);
        STAMPW(MODE ? 17 : 9, 4);
        const char* ab = p.ws + (MODE ? WS_HH : WS_O) + (size_t)r0 * 16 + (size_t)(4 * la) * 32768 + 2048;
        unsigned vp[4];
#pragma unroll
        for (int i = 0; i < 4; ++i) vp[i] = voA + (unsigned)i * (32768 - 1024);
        const unsigned l0 = lds0 + C::LA + la * 4096 + 2048; unsigned l = l0;
        ring_loader<RA, NST, 4, 2, true>([&]() { gl_str4<FUSED>(vp, ab, l); ab += 8 * 32768; l = l == l0 + (RA - 1) * 8192 ? l0 : l + 8192; }, [&](int) {}, [&](int) {});
        BAR_L(); BAR_L(); BAR_L();
    } else {
        const unsigned aA = (unsigned)(C::LA + (4 * wk + g) * 1024 + (32 * wm + r16) * 16), aB = (unsigned)(wk * 5120 + lane * 16);
        f32x4 acc[2][5];
#pragma unroll
        for (int i = 0; i < 2; ++i)
#pragma unroll
            for (int n = 0; n < 5; ++n) acc[i][n] = (f32x4){0.f, 0.f, 0.f, 0.f};
        bf16x8 Af[3][2], Bf[3][5];
        int sa = 0, sb = 0;
        auto rd = [&](int b) {
            lcptr la = (lcptr)L + aA + sa * 8192; lcptr lb = (lcptr)L + aB + sb * 10240;
#pragma unroll
            for (int i = 0; i < 2; ++i) Af[b][i] = ld128(la + i * 256);
#pragma unroll
            for (int n = 0; n < 5; ++n) Bf[b][n] = ld128(lb + n * 1024);
            sa = sa == RA - 1 ? 0 : sa + 1; sb = sb == RB - 1 ? 0 : sb + 1;
        };
        auto mm = [&](int b) {
#pragma unroll
            for (int n = 0; n < 5; ++n)
#pragma unroll
                for (int i = 0; i < 2; ++i) acc[i][n] = MFMA16(Bf[b][n], Af[b][i], acc[i][n]);
        };
        auto bar = [&]() { __builtin_amdgcn_sched_barrier(0); __builtin_amdgcn_s_waitcnt(0xC77F); asm volatile("" ::: "memory"); __builtin_amdgcn_s_barrier(); asm volatile("" ::: "memory"); __builtin_amdgcn_sched_barrier(0); };
        auto il = [&]() { SGB(0x8, 1); SGB(0x100, 2); SGB(0x8, 1); SGB(0x100, 2); SGB(0x8, 1); SGB(0x100, 2); SGB(0x8, 1); SGB(0x100, 1); SGB(0x8, 6); __builtin_amdgcn_sched_barrier(0); };
        bar(); STAMP(MODE ? 18 : 10); if (MODE) STAMPC(28); rd(0);
        bar(); rd(1);
        int s = 0;
#pragma unroll 1
        for (; s + 4 < NST; s += 3) {
            bar(); rd(2); mm(0); il();
            bar(); rd(0); mm(1); il();
            bar(); rd(1); mm(2); il();
        }
        if (NST % 3 == 0) { bar(); rd(2); mm(0); il(); mm(1); mm(2); }
        else { bar(); rd(2); mm(0); il(); bar(); rd(0); mm(1); il(); mm(2); mm(0); }
        STAMP(MODE ? 19 : 11); if (MODE) STAMPC(29);
        BAR_L();
#pragma unroll
        for (int n = 0; n < 5; ++n) *(LAS f32x4*)(L + C::L_X + (wid * 5 + n) * 1024 + lane * 16) = wk ? acc[0][n] : acc[1][n];
        BAR_L();
        const int row = 32 * wm + 16 * wk + r16;
        float ps = 0.f, pq = 0.f;
#pragma unroll
        for (int n = 0; n < 5; ++n) { const f32x4 v = (wk ? acc[1][n] : acc[0][n]) + *(const LAS f32x4*)(L + C::L_X + ((wid ^ 1) * 5 + n) * 1024 + lane * 16);
            u32x2 rr;
            if (MODE == 0) rr = *(const LAS u32x2*)(L + C::L_RES + row * 160 + n * 32 + g * 8);
            else rr = *(const LAS u32x2*)(L + C::L_RES + (2 * n + (g >> 1)) * 1024 + row * 16 + (g & 1) * 8);
            const unsigned a = pk2(v[0], v[1]), b = pk2(v[2], v[3]);
            const u32x2 o = {pk2(bflo(rr[0]) + bflo(a), bfhi(rr[0]) + bfhi(a)), pk2(bflo(rr[1]) + bflo(b), bfhi(rr[1]) + bfhi(b))};
            if (MODE == 0) {
#pragma unroll
                for (int e = 0; e < 2; ++e) { const float lo = bflo(o[e]), hi = bfhi(o[e]); ps += lo + hi; pq += lo * lo + hi * hi; }
                *(LAS u32x2*)(L + C::L_STG + (2 * n + (g >> 1)) * 1024 + row * 16 + (g & 1) * 8) = o;
            } else *(LAS u32x2*)(L + C::L_STG + row * 160 + n * 32 + g * 8) = o; }
        if (MODE == 0) { ps = sum_x32(sum_x16(ps)); pq = sum_x32(sum_x16(pq));
            if (g == 0) { char* dst = p.ws + WS_PS + ((size_t)(ti * 8 + ct) * 128 + 64 * rh + row) * 8; const u64 v = ((u64)__float_as_uint(pq) << 32) | __float_as_uint(ps);
                if (FUSED) st_dev(dst, v); else *(u64*)dst = v; } }
        BAR_L();
    }
    for (int n = wid; n < 10; n += 8) { const u32x4 v = *(const LAS u32x4*)(L + C::L_STG + n * 1024 + lane * 16);
        if (MODE == 0) st16<FUSED>(p.ws + WS_H + (size_t)(10 * ct + n) * 32768 + (size_t)r0 * 16 + lane * 16, v);
        else { const int u = 64 * n + lane; *(u32x4*)(p.out + (size_t)(r0 + u / 10) * D_ * 2 + ct * 160 + (u % 10) * 16) = v; } }
    STAMP(MODE ? 20 : 12);
    if (MODE == 0) publish<FUSED>(p, FL_C + (ti * 16 + tj) * 8, wid, lane, tag, 43);
}

namespace pd { constexpr int RB = 3, RA = 4, SB = 28672, LA = RB * SB, NST = 10, L_STAT = LA + RA * 16384, L_CS = L_STAT + 1024, L_STG = LA; }
template <bool FUSED>
DI void d_bissue(const char* wb, unsigned l, int lane) { gl_lin7<FUSED>(lane * 16, wb, l); gl_lin7<FUSED>(lane * 16, wb + 7168, l + 7168); }
template <bool FUSED>
DI void d_prefetch(const Params& p, lptr L, int lane, int wid, int tj) {
    const char* wb = p.ws + WS_PWG + (size_t)tj * 286720 + (wid - 6) * 14336 + 3072;
    const unsigned l = (unsigned)(uintptr_t)L + (wid - 6) * 14336 + 3072;
#pragma unroll 1
    for (int s = 0; s < pd::RB; ++s) d_bissue<FUSED>(wb + (size_t)s * pd::SB, l + s * pd::SB, lane);
}
template <bool FUSED>
DI void phase_d(const Params& p, lptr L, int lane, int wid, int ti, int tj, u64 tag) {
    using namespace pd;
    const unsigned lds0 = (unsigned)(uintptr_t)L;
    if (wid >= 6) {
        const int lb = wid - 6;
        u32x2 ps[8]; float cp[2][16];
        const int row = 64 * lb + lane;
        const char* wb = p.ws + WS_PWG + (size_t)tj * 286720 + lb * 14336 + 3072 + (size_t)RB * SB;
        const unsigned l0 = lds0 + lb * 14336 + 3072; unsigned l = l0;
        auto issue = [&]() { d_bissue<FUSED>(wb, l, lane); wb += SB; l = l == l0 + (RB - 1) * SB ? l0 : l + SB; };
        static_assert(RB == 3 && NST == 10, "the waits below");
        BAR_VL(28);
#pragma unroll
        for (int c = 0; c < 8; ++c) ld64_async(ps[c], p.ws + WS_PS + ((size_t)(ti * 8 + c) * 128 + row) * 8);
        BAR_VL(22); issue();
#pragma unroll
        for (int i = 0; i < 16; ++i) ld32_async(cp[0][i], (const float*)(p.ws + WS_CSP) + (i * 16 + tj) * 224 + row);
        BAR_VL(38); issue();
#pragma unroll
        for (int i = 0; i < 16; ++i) ld32_async(cp[1][i], (const float*)(p.ws + WS_CSP) + (i * 16 + tj) * 224 + (row + 128 < 224 ? row + 128 : 223));
        BAR_VL(46); issue();
        BAR_VL(30); issue();
        BAR_VL(14); issue();
        { float a = 0.f, q = 0.f;
#pragma unroll
          for (int c = 0; c < 8; ++c) { asm volatile("" : "+v"(ps[c])); a += __uint_as_float(ps[c][0]); q += __uint_as_float(ps[c][1]); }
          *(LAS f32x2*)(L + L_STAT + row * 8) = ln_scalars(a, q);
#pragma unroll
          for (int z = 0; z < 2; ++z) { float sm = 0.f;
#pragma unroll
              for (int i = 0; i < 16; ++i) { asm volatile("" : "+v"(cp[z][i])); sm += cp[z][i]; }
              if (row + 128 * z < 224) *(LAS float*)(L + L_CS + (row + 128 * z) * 4) = sm; } }
        BAR_VL(14); issue();
        BAR_VL(14); issue();
        BAR_VL(14);
        if (FUSED) {
            poll_flags(p.ws + WS_FLAG + FL_B + ((lane & 15) * 16 + (tj & ~1) + ((lane >> 4) & 1)) * 8, lane < 32, tag);
#pragma unroll 1
            for (int i = 0; i < 3; ++i) { int ln = (lb * 3 + i) * 64 + lane; ln = ln < 270 ? ln : 269;
                warm64(p.ws + WS_PWD + (size_t)(tj >> 1) * 276480 + (size_t)(2 * (ti & 3) + (tj & 1)) * 34560, (unsigned)ln * 128u, lds0); }
            BAR_VL(3);
        } else BAR_VL(0);
        BAR_L(); BAR_L();
    } else if (wid >= 4) {
        const int la = wid - 4;
        if (FUSED) poll_flags(p.ws + WS_FLAG + FL_C + (ti * 16 + (lane & 15)) * 8, lane < 16, tag);
        STAMPW(13, 4);
        const char* ab = p.ws + WS_H + (size_t)(128 * ti + 64 * la) * 16 + 4096;
        unsigned vp[4];
#pragma unroll
        for (int i = 0; i < 4; ++i) vp[i] = lane * 16 + (unsigned)i * (32768 - 2048);
        const unsigned l0 = lds0 + LA + la * 1024 + 4096; unsigned l = l0;
        ring_loader<RA, NST, 8, 1, true>([&]() { gl_str4w<FUSED>(vp, ab, l); gl_str4w<FUSED>(vp, ab + 4 * 32768, l + 8192); ab += 8 * 32768; l = l == l0 + (RA - 1) * 16384 ? l0 : l + 16384; }, [&](int) {}, [&](int) {});
        BAR_L(); BAR_L();
    } else {
        const int r32 = lane & 31, h = lane >> 5;
        const unsigned aA = (unsigned)(LA + h * 2048 + (32 * wid + r32) * 16), aB = (unsigned)(lane * 16);
        f32x16 acc[7];
#pragma unroll
        for (int t = 0; t < 7; ++t)
#pragma unroll
            for (int i = 0; i < 16; ++i) acc[t][i] = 0.f;
        bf16x8 A0, B0[7], A1, B1[7];
        auto rd = [&](bf16x8& Af, bf16x8* Bf, int sa, int sb, int u) {
            lcptr lb = (lcptr)L + aB + sb * SB + u * 7168;
            Af = ld128((lcptr)L + aA + sa * 16384 + u * 4096);
#pragma unroll
            for (int t = 0; t < 7; ++t) Bf[t] = ld128(lb + t * 1024);
        };
        auto mm = [&](const bf16x8& Af, const bf16x8* Bf) {
#pragma unroll
            for (int t = 0; t < 7; ++t) acc[t] = MFMA32(Bf[t], Af, acc[t]);
        };
        auto il = [&](const bf16x8& Af, const bf16x8* Bf) { SGB(0x8, 1); SGB(0x100, 2); SGB(0x8, 1); SGB(0x100, 2); SGB(0x8, 1); SGB(0x100, 2); SGB(0x8, 1); SGB(0x100, 2); SGB(0x8, 3); __builtin_amdgcn_sched_barrier(0);
            asm volatile("" :: "v"(Af), "v"(Bf[0]), "v"(Bf[1]), "v"(Bf[2]), "v"(Bf[3]), "v"(Bf[4]), "v"(Bf[5]), "v"(Bf[6])); __builtin_amdgcn_sched_barrier(0); };
        CBAR();
        STAMP(14); STAMPC(24);
        rd(A0, B0, 0, 0, 0);
        int sa = 0, sb = 0;
#pragma unroll 1
        for (int s = 0; s < NST - 1; ++s) {
            rd(A1, B1, sa, sb, 1); mm(A0, B0); il(A0, B0);
            rd(A0, B0, sa, sb, 2); mm(A1, B1); il(A1, B1);
            rd(A1, B1, sa, sb, 3); mm(A0, B0); il(A0, B0);
            sa = sa == RA - 1 ? 0 : sa + 1; sb = sb == RB - 1 ? 0 : sb + 1;
            CBAR();
            rd(A0, B0, sa, sb, 0); mm(A1, B1); il(A1, B1);
        }
        rd(A1, B1, sa, sb, 1); mm(A0, B0); il(A0, B0);
        rd(A0, B0, sa, sb, 2); mm(A1, B1); il(A1, B1);
        rd(A1, B1, sa, sb, 3); mm(A0, B0); il(A0, B0);
        mm(A1, B1);
        STAMP(15); STAMPC(25);
        BAR_L();
        STAMP(40);
        const int tok = 32 * wid + r32;
        const f32x2 st = *(const LAS f32x2*)(L + L_STAT + tok * 8);
        const float r2 = st.y * st.y;
        const f32x2 nm2 = {-st.x, -st.x}, r22 = {r2, r2};
        auto glu4 = [&](float g0, float g1, float g2, float g3, float u0, float u1, float u2, float u3, const f32x4& cg, const f32x4& cu) {
            const f32x2 za = (f32x2){cg[0], cg[1]} * nm2 + (f32x2){g0, g1}, zb = (f32x2){cg[2], cg[3]} * nm2 + (f32x2){g2, g3};
            const f32x2 ya = ((f32x2){cu[0], cu[1]} * nm2 + (f32x2){u0, u1}) * r22, yb = ((f32x2){cu[2], cu[3]} * nm2 + (f32x2){u2, u3}) * r22;
            const f32x2 oa = (f32x2){__builtin_fmaxf(za[0], 0.f), __builtin_fmaxf(za[1], 0.f)} * ya, ob = (f32x2){__builtin_fmaxf(zb[0], 0.f), __builtin_fmaxf(zb[1], 0.f)} * yb;
            return (u32x2){pk2(oa[0], oa[1]), pk2(ob[0], ob[1])}; };
#pragma unroll
        for (int a = 0; a < 3; ++a)
#pragma unroll
            for (int u = 0; u < 4; ++u) {
                const f32x4 cg = *(const LAS f32x4*)(L + L_CS + (64 * a + 8 * u + 4 * h) * 4), cu = *(const LAS f32x4*)(L + L_CS + (64 * a + 32 + 8 * u + 4 * h) * 4);
                *(LAS u32x2*)(L + L_STG + (4 * a + u) * 2048 + tok * 16 + h * 8) = glu4(acc[2 * a][4 * u], acc[2 * a][4 * u + 1], acc[2 * a][4 * u + 2], acc[2 * a][4 * u + 3],
                    acc[2 * a + 1][4 * u], acc[2 * a + 1][4 * u + 1], acc[2 * a + 1][4 * u + 2], acc[2 * a + 1][4 * u + 3], cg, cu); }
#pragma unroll
        for (int u = 0; u < 2; ++u) {
            const f32x4 cg = *(const LAS f32x4*)(L + L_CS + (192 + 8 * u + 4 * h) * 4), cu = *(const LAS f32x4*)(L + L_CS + (208 + 8 * u + 4 * h) * 4);
            *(LAS u32x2*)(L + L_STG + (12 + u) * 2048 + tok * 16 + h * 8) = glu4(acc[6][4 * u], acc[6][4 * u + 1], acc[6][4 * u + 2], acc[6][4 * u + 3],
                acc[6][8 + 4 * u], acc[6][8 + 4 * u + 1], acc[6][8 + 4 * u + 2], acc[6][8 + 4 * u + 3], cg, cu); }
        STAMP(41);
        BAR_L();
    }
    for (int n = wid; n < 28; n += 8) { const int c = 14 * tj + (n >> 1); if (c < 216)
        st16<FUSED>(p.ws + WS_HH + (size_t)c * 32768 + (size_t)(128 * ti + 64 * (n & 1)) * 16 + lane * 16, *(const LAS u32x4*)(L + L_STG + n * 1024 + lane * 16)); }
    STAMP(16);
    publish<FUSED>(p, FL_D + (ti * 16 + tj) * 8, wid, lane, tag, 44);
}

#define ROTA 2
#define ROTC 0
#define ROTD 0
#define ROTE 1
#define PROLOG() \
    extern __shared__ __attribute__((aligned(16))) char smem[]; \
    lptr L = (lptr)smem; \
    const int tid = threadIdx.x, lane = tid & 63, wid = __builtin_amdgcn_readfirstlane(tid >> 6); \
    const int xcd = blockIdx.x & 7, idx = blockIdx.x >> 3; \
    const int ti = 4 * (xcd & 3) + (idx & 3), tj = 8 * (xcd >> 2) + (idx >> 2); \
    const int rot10 = ((idx >> 2) + 3 * (idx & 3)) % 10, rot27 = ((idx >> 2) * 3 + (idx & 3) * 7) % 27; \
    const int rotC = ROTC ? rot10 : 0, rotD = ROTD ? rot10 : 0, rotE = ROTE ? rot27 : 0, rotA = ROTA == 1 ? rot10 : ROTA == 2 ? (((idx >> 2) + (idx & 3)) & 3) : ROTA == 3 ? ((idx >> 2) & 1) * 5 : 0; \
    (void)rot10; (void)rot27; (void)tid; (void)rotA; (void)rotC; (void)rotD; (void)rotE

__global__ __launch_bounds__(512, 2) void k_fused(const Params p) {
    PROLOG();
    STAMP(0);
    u64 tag = 0; u32x2 ev;
    ld64_async(ev, p.ws + WS_FLAG + FL_E);
    if (tj >= 15) phase_a_stats<true>(p, L, lane, wid, ti, ev, tag); else phase_a<true>(p, L, lane, wid, ti, tj, rotA, ev, tag);
    if (tid == 0) st_dev(p.ws + WS_FLAG + FL_START + blockIdx.x * 8, tag);
    phase_b<true>(p, L, lane, wid, ti, tj, tag);
    if (wid >= 6) {
        poll_flags(p.ws + WS_FLAG + FL_P + ((lane & 15) * 16 + (lane < 16 ? tj : (tj & ~1) + ((lane >> 4) & 1))) * 8, lane < 48, tag);
        ce_prefetch<true, 0>(p, L, lane, wid, tj);
    }
    STAMP(8);
    phase_ce<true, 0>(p, L, lane, wid, ti, tj, tag);
    if (wid >= 6) d_prefetch<true>(p, L, lane, wid, tj);
    if (blockIdx.x == 0 && wid == 5) {
        for (unsigned spins = 0; spins < (1u << 17); ++spins) {
            bool ok = true;
#pragma unroll
            for (int i = 0; i < 4; ++i) ok &= ld_dev(p.ws + WS_FLAG + FL_START + (lane * 4 + i) * 8) == tag;
            if (__all(ok)) break;
            __builtin_amdgcn_s_sleep(1);
        }
        if (lane == 0) st_dev(p.ws + WS_FLAG + FL_E, tag);
    }
    phase_d<true>(p, L, lane, wid, ti, tj, tag);
    if (wid >= 6) ce_prefetch<true, 1>(p, L, lane, wid, tj);
    phase_ce<true, 1>(p, L, lane, wid, ti, tj, tag);
    STAMP(21);
}

extern "C" void kernel_launch(void* const* d_in, const int* in_sizes, int n_in, void* d_out, int out_size, void* d_ws, size_t ws_size, hipStream_t stream) {
    Params p;
    p.x = (const u16*)d_in[0]; p.wqkv = (const u16*)d_in[1]; p.wo = (const u16*)d_in[2];
    p.wg = (const u16*)d_in[4]; p.wu = (const u16*)d_in[5]; p.wd = (const u16*)d_in[6];
    p.out = (char*)d_out; p.ws = (char*)d_ws;
    (void)hipFuncSetAttribute((const void*)k_fused, hipFuncAttributeMaxDynamicSharedMemorySize, LDS_BYTES);
    k_fused<<<256, 512, LDS_BYTES, stream>>>(p);
}
```

```cpp
#define ROTA 2
#include <hip/hip_runtime.h>
#include <hip/hip_bf16.h>
#include <stdint.h>

#define LAS __attribute__((address_space(3)))
#define DI __device__ __forceinline__
typedef unsigned short u16;
typedef unsigned long long u64;
typedef short bf16x8 __attribute__((ext_vector_type(8)));
typedef short s16x4 __attribute__((ext_vector_type(4)));
typedef float f32x2 __attribute__((ext_vector_type(2)));
typedef float f32x4 __attribute__((ext_vector_type(4)));
typedef float f32x16 __attribute__((ext_vector_type(16)));
typedef unsigned u32x2 __attribute__((ext_vector_type(2)));
typedef unsigned u32x4 __attribute__((ext_vector_type(4)));
typedef __bf16 bf16x2_t __attribute__((ext_vector_type(2)));
typedef LAS char* lptr;
typedef LAS const char* lcptr;

static constexpr int S_ = 2048, D_ = 640, H_ = 32, DH_ = 20, F_ = 1728;
static constexpr size_t WS_FLAG = 0;
static constexpr size_t FL_E = 0, FL_START = 1024, FL_A = 4096, FL_B = 8192, FL_C = 12288, FL_D = 16384, FL_P = 20480;
static constexpr size_t WS_QKV = 32768;
static constexpr size_t WS_O = WS_QKV + (size_t)480 * 16384;
static constexpr size_t WS_H = WS_O + (size_t)80 * 32768;
static constexpr size_t WS_HH = WS_H + (size_t)80 * 32768;
static constexpr size_t WS_CSP = WS_HH + (size_t)216 * 32768;
static constexpr size_t WS_PS = WS_CSP + (size_t)16 * 16 * 224 * 4;
static constexpr size_t WS_S1 = WS_PS + (size_t)16 * 8 * 128 * 8;
static constexpr size_t WS_PWO = WS_S1 + (size_t)2048 * 8;
static constexpr size_t WS_PWD = WS_PWO + (size_t)8 * 20 * 5 * 1024;
static constexpr size_t WS_PWG = WS_PWD + (size_t)8 * 54 * 5 * 1024;
static constexpr size_t WS_END = WS_PWG + (size_t)16 * 40 * 7 * 1024;
static constexpr int LDS_BYTES = 163840;
static constexpr int CODE_LINES = 344;
static constexpr float C2_ = 0.22360679774997896f * 1.4426950408889634f;

struct Params { const u16* x; const u16* wqkv; const u16* wo; const u16* wg; const u16* wu; const u16* wd; char* out; char* ws; };

#define STAMP(i) do {} while (0)
#define STAMPC(i) do {} while (0)
#define STAMPW(i, w) do {} while (0)

DI unsigned pk2(float a, float b) { f32x2 v = {a, b}; return __builtin_bit_cast(unsigned, __builtin_convertvector(v, bf16x2_t)); }
DI float bflo(unsigned u) { return __uint_as_float(u << 16); }
DI float bfhi(unsigned u) { return __uint_as_float(u & 0xffff0000u); }
DI float rbf(float a) { return bflo(pk2(a, 0.f) & 0xffffu); }
template <bool SC1> DI void glds16(unsigned voff, const void* sbase, unsigned ldsaddr) {
    unsigned keep;
    if (SC1) asm volatile("s_mov_b32 %0, m0\n\ts_mov_b32 m0, %3\n\ts_nop 0\n\tglobal_load_lds_dwordx4 %1, %2 sc1\n\ts_mov_b32 m0, %0" : "=&s"(keep) : "v"(voff), "s"(sbase), "s"(ldsaddr) : "memory");
    else asm volatile("s_mov_b32 %0, m0\n\ts_mov_b32 m0, %3\n\ts_nop 0\n\tglobal_load_lds_dwordx4 %1, %2\n\ts_mov_b32 m0, %0" : "=&s"(keep) : "v"(voff), "s"(sbase), "s"(ldsaddr) : "memory");
}
#define GL_(v, off, sc) "global_load_lds_dwordx4 %" #v ", %[g] offset:" #off sc "\n\t"
#define GLHEAD "s_mov_b32 m0, %[l]\n\ts_nop 0\n\t"
template <bool SC1> DI void gl_lin5(unsigned v, const char* g, unsigned l) {
    if (SC1) asm volatile(GLHEAD GL_(0, -2048, " sc1") GL_(0, -1024, " sc1") GL_(0, 0, " sc1") GL_(0, 1024, " sc1") GL_(0, 2048, " sc1") :: "v"(v), [g] "s"(g), [l] "s"(l) : "memory");
    else asm volatile(GLHEAD GL_(0, -2048, "") GL_(0, -1024, "") GL_(0, 0, "") GL_(0, 1024, "") GL_(0, 2048, "") :: "v"(v), [g] "s"(g), [l] "s"(l) : "memory");
}
template <bool SC1> DI void gl_lin7(unsigned v, const char* g, unsigned l) {
    if (SC1) asm volatile(GLHEAD GL_(0, -3072, " sc1") GL_(0, -2048, " sc1") GL_(0, -1024, " sc1") GL_(0, 0, " sc1") GL_(0, 1024, " sc1") GL_(0, 2048, " sc1") GL_(0, 3072, " sc1") :: "v"(v), [g] "s"(g), [l] "s"(l) : "memory");
    else asm volatile(GLHEAD GL_(0, -3072, "") GL_(0, -2048, "") GL_(0, -1024, "") GL_(0, 0, "") GL_(0, 1024, "") GL_(0, 2048, "") GL_(0, 3072, "") :: "v"(v), [g] "s"(g), [l] "s"(l) : "memory");
}
template <bool SC1> DI void gl_str4(const unsigned* v, const char* g, unsigned l) {
    if (SC1) asm volatile(GLHEAD GL_(0, -2048, " sc1") GL_(1, -1024, " sc1") GL_(2, 0, " sc1") GL_(3, 1024, " sc1") :: "v"(v[0]), "v"(v[1]), "v"(v[2]), "v"(v[3]), [g] "s"(g), [l] "s"(l) : "memory");
    else asm volatile(GLHEAD GL_(0, -2048, "") GL_(1, -1024, "") GL_(2, 0, "") GL_(3, 1024, "") :: "v"(v[0]), "v"(v[1]), "v"(v[2]), "v"(v[3]), [g] "s"(g), [l] "s"(l) : "memory");
}
template <bool SC1> DI void gl_str4w(const unsigned* v, const char* g, unsigned l) {
    if (SC1) asm volatile(GLHEAD GL_(0, -4096, " sc1") GL_(1, -2048, " sc1") GL_(2, 0, " sc1") GL_(3, 2048, " sc1") :: "v"(v[0]), "v"(v[1]), "v"(v[2]), "v"(v[3]), [g] "s"(g), [l] "s"(l) : "memory");
    else asm volatile(GLHEAD GL_(0, -4096, "") GL_(1, -2048, "") GL_(2, 0, "") GL_(3, 2048, "") :: "v"(v[0]), "v"(v[1]), "v"(v[2]), "v"(v[3]), [g] "s"(g), [l] "s"(l) : "memory");
}
DI void gl_str8(const unsigned* v, const char* g, unsigned l) {
    asm volatile(GLHEAD GL_(0, -4096, "") GL_(1, -3072, "") GL_(2, -2048, "") GL_(3, -1024, "") GL_(4, 0, "") GL_(5, 1024, "") GL_(6, 2048, "") GL_(7, 3072, "")
                 :: "v"(v[0]), "v"(v[1]), "v"(v[2]), "v"(v[3]), "v"(v[4]), "v"(v[5]), "v"(v[6]), "v"(v[7]), [g] "s"(g), [l] "s"(l) : "memory");
}
template <bool SC1> DI void st16(void* ptr, u32x4 v) {
    if (SC1) asm volatile("global_store_dwordx4 %0, %1, off sc1\n\ts_nop 1" :: "v"(ptr), "v"(v) : "memory");
    else *(u32x4*)ptr = v;
}
DI u64 ld_dev(const void* p_) { return __hip_atomic_load((const u64*)p_, __ATOMIC_RELAXED, __HIP_MEMORY_SCOPE_AGENT); }
DI void st_dev(void* p_, u64 v) { __hip_atomic_store((u64*)p_, v, __ATOMIC_RELAXED, __HIP_MEMORY_SCOPE_AGENT); }
DI void ld64_async(u32x2& v, const void* ptr) { asm volatile("global_load_dwordx2 %0, %1, off sc1" : "=v"(v) : "v"(ptr) : "memory"); }
DI void ld32_async(float& v, const void* ptr) { asm volatile("global_load_dword %0, %1, off sc1" : "=v"(v) : "v"(ptr) : "memory"); }
DI void poll_flags(const void* flag, bool need, u64 tag) {
    for (unsigned spins = 0; spins < (1u << 17); ++spins) {
        const u64 v = need ? ld_dev(flag) : tag;
        if (__all(v == tag)) break;
        __builtin_amdgcn_s_sleep(1);
    }
    asm volatile("" ::: "memory");
}
DI void warm64(const void* base, unsigned voff, unsigned lds0) {
    unsigned keep;
    asm volatile("s_mov_b32 %0, m0\n\ts_mov_b32 m0, %3\n\ts_nop 0\n\tglobal_load_lds_dword %1, %2 sc1\n\ts_mov_b32 m0, %0" : "=&s"(keep) : "v"(voff), "s"(base), "s"(lds0 + (unsigned)(LDS_BYTES - 256)) : "memory");
}
#define VMWAIT(N) asm volatile("s_waitcnt vmcnt(%0)" :: "i"(N) : "memory")
#define BAR_L() asm volatile("s_waitcnt lgkmcnt(0)\n\ts_barrier" ::: "memory")
#define BAR_VL(N) asm volatile("s_waitcnt vmcnt(%0) lgkmcnt(0)\n\ts_barrier" :: "i"(N) : "memory")
#define WAIT_LGKM0() do { __builtin_amdgcn_sched_barrier(0); __builtin_amdgcn_s_waitcnt(0xC07F); __builtin_amdgcn_sched_barrier(0); } while (0)
#define CBAR() do { WAIT_LGKM0(); asm volatile("" ::: "memory"); __builtin_amdgcn_s_barrier(); asm volatile("" ::: "memory"); __builtin_amdgcn_sched_barrier(0); } while (0)
#define SGB(mask, n) __builtin_amdgcn_sched_group_barrier(mask, n, 0)
template <int STEP, int MAXN> DI void bar_vl_n(int n) {
    if (n >= MAXN) { BAR_VL((MAXN * STEP > 63 ? 63 : MAXN * STEP)); }
    else if constexpr (MAXN > 0) bar_vl_n<STEP, MAXN - 1>(n);
}
template <int N> struct IC { static constexpr int v = N; };
template <int K, int END, class F> DI void static_for(F&& f) { if constexpr (K < END) { f(IC<K>{}); static_for<K + 1, END>(f); } }
template <int R, int NST, int NP, int LAG, bool PROLOGUE, int NEND = NST, class FI, class FP, class FQ>
DI void ring_loader(FI&& issue, FP&& pre, FQ&& post) {
    constexpr int L0 = (R < NST ? R : NST) - 1;
    if (PROLOGUE) {
#pragma unroll 1
        for (int k = 0; k <= L0; ++k) issue();
    }
    static_for<0, LAG + 1>([&](auto kc) { constexpr int k = decltype(kc)::v; BAR_VL((L0 - k) * NP); pre(k); if constexpr (k >= LAG && k - LAG + R < NST) issue(); post(k); });
    constexpr int KS = NST - R + LAG;
    if constexpr (KS > LAG + 1) {
#pragma unroll 1
        for (int k = LAG + 1; k < KS; ++k) { BAR_VL((R - 1 - LAG) * NP); pre(k); issue(); post(k); }
    }
    static_for<(KS > LAG + 1 ? KS : LAG + 1), NEND>([&](auto kc) { constexpr int k = decltype(kc)::v; BAR_VL((NST - 1 - k) * NP); pre(k); post(k); });
}
DI bf16x8 ld128(lcptr p) { return *(const LAS bf16x8*)p; }
DI s16x4 ld64(lcptr p) { return *(const LAS s16x4*)p; }
DI s16x4 ld64v(lcptr p) { return *(const volatile LAS s16x4*)p; }
DI s16x4 ldtr(lcptr p) { typedef short v4 __attribute__((ext_vector_type(4))); return __builtin_bit_cast(s16x4, __builtin_amdgcn_ds_read_tr16_b64_v4i16((LAS v4*)p)); }
DI bf16x8 cat8(s16x4 a, s16x4 b) { return __builtin_shufflevector(a, b, 0, 1, 2, 3, 4, 5, 6, 7); }
#define MFMA16(a, b, c) __builtin_amdgcn_mfma_f32_16x16x32_bf16(a, b, c, 0, 0, 0)
#define MFMA32(a, b, c) __builtin_amdgcn_mfma_f32_32x32x16_bf16(a, b, c, 0, 0, 0)
#define MFMA32K8(a, b, c) __builtin_amdgcn_mfma_f32_32x32x8bf16_1k(a, b, c, 0, 0, 0)
template <int CTRL> DI float dpp_add(float v) { return v + __int_as_float(__builtin_amdgcn_update_dpp(0, __float_as_int(v), CTRL, 0xf, 0xf, true)); }
DI float sum8(float v) { v = dpp_add<0xB1>(v); v = dpp_add<0x4E>(v); return dpp_add<0x141>(v); }
DI float sum_x16(float v) { auto r = __builtin_amdgcn_permlane16_swap(__float_as_uint(v), __float_as_uint(v), false, false); return __uint_as_float(r[0]) + __uint_as_float(r[1]); }
DI float sum_x32(float v) { auto r = __builtin_amdgcn_permlane32_swap(__float_as_uint(v), __float_as_uint(v), false, false); return __uint_as_float(r[0]) + __uint_as_float(r[1]); }
DI bf16x8 ones8() { bf16x8 o = {0x3f80, 0x3f80, 0x3f80, 0x3f80, 0x3f80, 0x3f80, 0x3f80, 0x3f80}; asm volatile("" : "+v"(o)); return o; }

DI f32x2 ln_scalars(float sum, float sq) {
    const float mean = sum * (1.0f / D_);
    float var = sq * (1.0f / D_) - mean * mean; var = var > 0.f ? var : 0.f;
    const float sd = rbf(__builtin_sqrtf(rbf(rbf(var) + 1e-5f)));
    return (f32x2){rbf(mean), 1.0f / sd};
}
template <bool FUSED> DI void publish(const Params& p, size_t flag_off, int wid, int lane, u64 tag, int sidx = 47) {
    if (FUSED) { BAR_VL(0); STAMP(sidx); if (wid == 0 && lane == 0) st_dev(p.ws + WS_FLAG + flag_off, tag); }
}

namespace rp { constexpr int L_G = 132608, L_U = L_G + 10752, L_O = L_G + 21504, L_D = 102400; }
DI void rp1_addr(const Params& p, int lane, int lw, int ti, int tj, const char* (&ga)[7]) {
    const int ns = ti < 8 ? 3 : 2, s0 = ti < 8 ? 3 * ti : 24 + 2 * (ti - 8);
    const int ct = tj >> 1, z = 2 * ti + (tj & 1);
#pragma unroll
    for (int i = 0; i < 7; ++i) { const int n = lw + 4 * i;
        if (n < 21) { int u = 64 * n + lane; const int mat = u >= 672; u -= 672 * mat; int row = u / 14; int col = 112 * tj + 8 * (u - 14 * row); col = col > F_ - 8 ? F_ - 8 : col;
            row = row < 16 * ns ? row : 16 * ns - 1;
            ga[i] = (const char*)((mat ? p.wu : p.wg) + (size_t)(16 * s0 + row) * F_ + col);
        } else { const int m = n < 26 ? n - 21 : 4, u = 64 * m + lane, row = u / 10, c = u - 10 * row;
            ga[i] = (const char*)(p.wo + (size_t)(32 * (z < 20 ? z : 0) + row) * D_ + 80 * ct + 8 * c); } }
}
DI void glds16v(const void* vaddr, unsigned ldsaddr) {
    asm volatile("s_nop 0\n\ts_mov_b32 m0, %1\n\ts_nop 0\n\tglobal_load_lds_dwordx4 %0, off" :: "v"(vaddr), "s"(ldsaddr) : "memory");
}
DI void rp1_issue(const char* (&ga)[7], unsigned lds0, int lw) {
#pragma unroll
    for (int i = 0; i < 7; ++i) { const int n = lw + 4 * i; glds16v(ga[i], (unsigned)__builtin_amdgcn_readfirstlane(lds0 + rp::L_G + (n < 26 ? n : 25) * 1024)); }
}
template <bool FUSED>
DI void rp1_store(const Params& p, lptr L, int lane, int lw, int ti, int tj) {
    const int ns = ti < 8 ? 3 : 2, s0 = ti < 8 ? 3 * ti : 24 + 2 * (ti - 8);
    const int ct = tj >> 1, z = 2 * ti + (tj & 1);
    const int h = lane >> 5, blk = (lane >> 4) & 1, g = lane >> 4, q = (lane & 15) >> 2, pp = lane & 3;
    const bf16x8 ones = ones8();
#pragma unroll 1
    for (int t = lw; t < 7; t += 4) {
        lcptr src = (lcptr)L + (t < 6 ? ((t & 1) ? rp::L_U : rp::L_G) + (32 * (t >> 1) + 16 * blk + 4 * pp) * 2 : (blk ? rp::L_U : rp::L_G) + (96 + 4 * pp) * 2) + (8 * h + q) * 224;
        s16x4 f[3][2];
#pragma unroll
        for (int s = 0; s < 3; ++s) { f[s][0] = ldtr(src + s * 16 * 224); f[s][1] = ldtr(src + s * 16 * 224 + 4 * 224); }
        f32x16 cs = {};
        char* dst = p.ws + WS_PWG + ((size_t)(tj * 40 + s0) * 7 + t) * 1024 + lane * 16;
#pragma unroll
        for (int s = 0; s < 3; ++s) if (s < ns) { const bf16x8 fr = cat8(f[s][0], f[s][1]); st16<FUSED>(dst + s * 7168, __builtin_bit_cast(u32x4, fr)); cs = MFMA32(fr, ones, cs); }
        if ((lane & 31) == 0) { float* cd = (float*)(p.ws + WS_CSP) + (ti * 16 + tj) * 224 + 32 * t + 4 * h;
#pragma unroll
            for (int u = 0; u < 4; ++u) st16<FUSED>(cd + 8 * u, __builtin_bit_cast(u32x4, ((f32x4){cs[4 * u], cs[4 * u + 1], cs[4 * u + 2], cs[4 * u + 3]}))); }
    }
    if (z < 20) {
#pragma unroll 1
        for (int nt = 3 - lw; nt < 5; nt += 4) {
            lcptr src = (lcptr)L + rp::L_O + (8 * g + q) * 160 + (16 * nt + 4 * pp) * 2;
            st16<FUSED>(p.ws + WS_PWO + ((size_t)(ct * 20 + z) * 5 + nt) * 1024 + lane * 16, __builtin_bit_cast(u32x4, cat8(ldtr(src), ldtr(src + 4 * 160)))); }
    }
}
DI void rp2_issue(const Params& p, unsigned lds0, int lane, int wid, int ti, int tj) {
    const int ct = tj >> 1, z = 2 * ti + (tj & 1), np = z + 32 < 54 ? 10 : 5;
#pragma unroll 1
    for (int n = wid; n < np; n += 8) { const int im = n >= 5, u = 64 * (n - 5 * im) + lane, row = u / 10, c = u - 10 * row;
        glds16<false>((unsigned)((32 * (z + 32 * im) + row) * D_ + 80 * ct + 8 * c) * 2u, p.wd, lds0 + rp::L_D + n * 1024); }
}
template <bool FUSED>
DI void rp2_store(const Params& p, lptr L, int lane, int wid, int ti, int tj) {
    const int ct = tj >> 1, z = 2 * ti + (tj & 1), np = z + 32 < 54 ? 10 : 5;
    const int g = lane >> 4, q = (lane & 15) >> 2, pp = lane & 3;
#pragma unroll 1
    for (int n = wid; n < np; n += 8) { const int im = n >= 5, nt = n - 5 * im;
        lcptr src = (lcptr)L + rp::L_D + im * 5120 + (8 * g + q) * 160 + (16 * nt + 4 * pp) * 2;
        st16<FUSED>(p.ws + WS_PWD + ((size_t)(ct * 54 + z + 32 * im) * 5 + nt) * 1024 + lane * 16, __builtin_bit_cast(u32x4, cat8(ldtr(src), ldtr(src + 4 * 160)))); }
}
DI u64 make_tag(u32x2& ev) {
    asm volatile("" : "+v"(ev));
    const u64 e = (((u64)(unsigned)__builtin_amdgcn_readfirstlane(ev[1]) << 32) | (unsigned)__builtin_amdgcn_readfirstlane(ev[0])) + 1;
    return e;
}

namespace pa { constexpr int R = 4, NST = 10, LA = 0, LB = R * 16384, L_STAT = 2 * R * 16384, L_CS = L_STAT + 1024; }
template <bool FUSED>
DI void phase_a_stats(const Params& p, lptr L, int lane, int wid, int ti, u32x2& ev, u64& tag) {
    const int r = lane >> 3, c = lane & 7;
    if (wid >= 4) { const char* ga[7]; rp1_addr(p, lane, wid - 4, ti, 15, ga); rp1_issue(ga, (unsigned)(uintptr_t)L, wid - 4); }
#pragma unroll
    for (int hlf = 0; hlf < 2; ++hlf) {
        const int row = 128 * ti + 16 * wid + 8 * hlf + r;
        const u32x4* src = (const u32x4*)(p.x + (size_t)row * D_) + c;
        u32x4 v[10];
#pragma unroll
        for (int i = 0; i < 10; ++i) v[i] = src[8 * i];
        float a = 0.f, q = 0.f;
#pragma unroll
        for (int i = 0; i < 10; ++i)
#pragma unroll
            for (int e = 0; e < 4; ++e) { const float lo = bflo(v[i][e]), hi = bfhi(v[i][e]); a += lo + hi; q += lo * lo + hi * hi; }
        const f32x2 st = ln_scalars(sum8(a), sum8(q));
        if (c == 0) { const u64 w = ((u64)__float_as_uint(st.y) << 32) | __float_as_uint(st.x); char* dst = p.ws + WS_S1 + (size_t)row * 8; if (FUSED) st_dev(dst, w); else *(u64*)dst = w; }
    }
    BAR_VL(0);
    if (FUSED) { tag = make_tag(ev); if (wid == 0 && lane == 0) st_dev(p.ws + WS_FLAG + FL_A + (ti * 16 + 15) * 8, tag); }
    if (wid >= 4) rp1_store<FUSED>(p, L, lane, wid - 4, ti, 15);
    if (FUSED) { BAR_VL(0); if (wid == 0 && lane == 0) st_dev(p.ws + WS_FLAG + FL_P + (ti * 16 + 15) * 8, tag); }
}
template <bool FUSED>
DI void phase_a(const Params& p, lptr L, int lane, int wid, int ti, int tj, int rot, u32x2& ev, u64& tag) {
    using namespace pa;
    const unsigned lds0 = (unsigned)(uintptr_t)L;
    if (wid >= 4) {
        const int lw = wid - 4, r = lane >> 3, c = lane & 7;
        const bool isA = lw < 2;
        unsigned vo[2];
#pragma unroll
        for (int e = 0; e < 2; ++e) vo[e] = isA ? (unsigned)(r * D_ + ((c ^ ((4 * e + (r >> 1)) & 7)) * 8)) * 2u : (unsigned)(r * 3 * D_ + ((c ^ (4 * ((r >> 1) & 1) + 2 * e)) * 8)) * 2u;
        unsigned vp[8];
#pragma unroll
        for (int i = 0; i < 8; ++i) vp[i] = vo[i & 1] + (isA ? (unsigned)i * (8 * D_ * 2 - 1024) : (unsigned)(i & 3) * (8 * 3 * D_ * 2 - 1024));
        const char* gb = isA ? (const char*)p.x + (size_t)(128 * ti + 64 * lw) * D_ * 2 + 4096 + rot * 128 : (const char*)p.wqkv + ((size_t)(32 * (lw - 2) + 64 * rot) * 3 * D_ + 128 * tj) * 2 + 2048;
        const int gstep = isA ? 128 : 64 * 3 * D_ * 2;
        int ks = rot; unsigned sl = lds0 + (isA ? LA + lw * 8192 + 4096 : LB + (lw - 2) * 4096 + 2048), sslot = 0;
        auto issue = [&]() {
            if (isA) gl_str8(vp, gb, sl + sslot); else { gl_str4<false>(vp, gb, sl + sslot); gl_str4<false>(vp, gb + 128, sl + sslot + 8192); }
            gb += gstep; if (++ks == NST) { ks = 0; gb -= (size_t)NST * gstep; }
            sslot = (sslot + 16384) & (R * 16384 - 1);
        };
        const int g = lane >> 4, q = (lane & 15) >> 2, pp = lane & 3;
        unsigned aC[2];
#pragma unroll
        for (int t = 0; t < 2; ++t) aC[t] = (unsigned)(LB + ((lw >> 1) * 8 + g) * 1024 + q * 128 + (((2 * (2 * (lw & 1) + t) + (pp >> 1)) ^ (4 * ((q >> 1) & 1) + 2 * (g & 1))) * 16) + (pp & 1) * 8);
        f32x4 cs[2] = {{0.f, 0.f, 0.f, 0.f}, {0.f, 0.f, 0.f, 0.f}};
        const bf16x8 ones = ones8();
        s16x4 cf[2][2][2];
        __builtin_amdgcn_s_setprio(3);
        auto pre = [&](int s) { lcptr q0 = (lcptr)L + (s & 3) * 16384;
#pragma unroll
            for (int ks2 = 0; ks2 < 2; ++ks2)
#pragma unroll
                for (int t = 0; t < 2; ++t) { cf[ks2][t][0] = ldtr(q0 + aC[t] + ks2 * 4096); cf[ks2][t][1] = ldtr(q0 + aC[t] + ks2 * 4096 + 512); } };
        auto post = [&](int) {
#pragma unroll
            for (int ks2 = 0; ks2 < 2; ++ks2)
#pragma unroll
                for (int t = 0; t < 2; ++t) cs[t] = MFMA16(cat8(cf[ks2][t][0], cf[ks2][t][1]), ones, cs[t]); };
        const char* ga[7]; rp1_addr(p, lane, lw, ti, tj, ga);
        issue(); issue();
        BAR_VL(8); pre(0); issue(); rp1_issue(ga, lds0, lw);
        if (FUSED) tag = make_tag(ev);
        BAR_VL(15); post(0); pre(1); issue();
        BAR_VL(15); post(1); pre(2); issue();
#pragma unroll 1
        for (int k = 3; k < NST - 2; ++k) { BAR_VL(8); post(k - 1); pre(k); issue(); }
        BAR_VL(8); post(NST - 3); pre(NST - 2);
        BAR_VL(0); post(NST - 2); pre(NST - 1); post(NST - 1);
        if (isA) {
            if (FUSED) poll_flags(p.ws + WS_FLAG + FL_A + (ti * 16 + 15) * 8, lane == 0, tag);
            const char* src = p.ws + WS_S1 + (size_t)(128 * ti + 64 * lw + lane) * 8;
            const u64 w = FUSED ? ld_dev(src) : *(const u64*)src;
            *(LAS u64*)(L + L_STAT + (64 * lw + lane) * 8) = w;
        }
        __builtin_amdgcn_s_setprio(0);
        if ((lane & 15) == 0) {
#pragma unroll
            for (int t = 0; t < 2; ++t) *(LAS f32x4*)(L + L_CS + (32 * lw + 16 * t + 4 * g) * 4) = cs[t];
        }
        BAR_L();
        rp1_store<FUSED>(p, L, lane, lw, ti, tj);
        BAR_L();
    } else {
        const int wm = wid >> 1, wn = wid & 1, r32 = lane & 31, h = lane >> 5, blk = (lane >> 4) & 1, q = (lane & 15) >> 2, pp = lane & 3;
        unsigned aX[4], aW[2];
        { const int row = 64 * wm + r32;
#pragma unroll
          for (int u = 0; u < 4; ++u) aX[u] = (unsigned)(LA + (row >> 3) * 1024 + (row & 7) * 128 + (((2 * u + h) ^ ((row >> 1) & 7)) * 16));
#pragma unroll
          for (int nt = 0; nt < 2; ++nt) aW[nt] = (unsigned)(LB + (wn * 8 + h) * 1024 + q * 128 + (((4 * nt + 2 * blk + (pp >> 1)) ^ (4 * ((q >> 1) & 1) + 2 * h)) * 16) + (pp & 1) * 8); }
        f32x16 acc[2][2];
#pragma unroll
        for (int i = 0; i < 16; ++i) { acc[0][0][i] = 0.f; acc[0][1][i] = 0.f; acc[1][0][i] = 0.f; acc[1][1][i] = 0.f; }
        bf16x8 X[4][2], W[4][2];
        auto rd = [&](int slot, int u) {
            lcptr sl = (lcptr)L + slot * 16384;
#pragma unroll
            for (int nt = 0; nt < 2; ++nt) W[u][nt] = cat8(ldtr(sl + aW[nt] + u * 2048), ldtr(sl + aW[nt] + u * 2048 + 512));
#pragma unroll
            for (int mt = 0; mt < 2; ++mt) X[u][mt] = ld128(sl + aX[u] + mt * 4096);
        };
        auto mm = [&](int u) {
#pragma unroll
            for (int mt = 0; mt < 2; ++mt)
#pragma unroll
                for (int nt = 0; nt < 2; ++nt) acc[mt][nt] = MFMA32(W[u][nt], X[u][mt], acc[mt][nt]);
        };
        auto il = [&]() { SGB(0x8, 1); SGB(0x100, 2); SGB(0x8, 1); SGB(0x100, 2); SGB(0x8, 1); SGB(0x100, 2); SGB(0x8, 1); __builtin_amdgcn_sched_barrier(0); };
        auto bar = [&]() { __builtin_amdgcn_sched_barrier(0); asm volatile("" ::: "memory"); __builtin_amdgcn_s_barrier(); asm volatile("" ::: "memory"); __builtin_amdgcn_sched_barrier(0); };
        bar();
        if (FUSED) VMWAIT(0);
        STAMP(1); STAMPC(26);
        rd(0, 0); rd(0, 1);
#pragma unroll 1
        for (int s = 0; s < NST - 1; ++s) {
            const int c = s & 3, n = (s + 1) & 3;
            rd(c, 2); mm(0); il();
            rd(c, 3); mm(1); il();
            bar();
            rd(n, 0); mm(2); il();
            rd(n, 1); mm(3); il();
        }
        rd((NST - 1) & 3, 2); mm(0); il();
        rd((NST - 1) & 3, 3); mm(1); il();
        mm(2); mm(3);
        STAMP(2); STAMPC(27);
        if (FUSED) tag = make_tag(ev);
        BAR_L();
        STAMP(38);
#pragma unroll
        for (int mt = 0; mt < 2; ++mt) { const int tok = 64 * wm + 32 * mt + r32; const f32x2 st = *(const LAS f32x2*)(L + L_STAT + tok * 8);
#pragma unroll
            for (int nt = 0; nt < 2; ++nt)
#pragma unroll
                for (int u = 0; u < 4; ++u) { const int col = 64 * wn + 32 * nt + 8 * u + 4 * h;
                    const f32x4 cs = *(const LAS f32x4*)(L + L_CS + col * 4);
                    const float sc = (128 * tj + col < D_) ? st.y * C2_ : st.y;
                    const f32x4 a = {acc[mt][nt][4 * u], acc[mt][nt][4 * u + 1], acc[mt][nt][4 * u + 2], acc[mt][nt][4 * u + 3]};
                    const f32x4 v = (a - cs * st.x) * sc;
                    *(LAS u32x2*)(L + (col >> 2) * 1024 + tok * 8) = (u32x2){pk2(v[0], v[1]), pk2(v[2], v[3])}; } }
        BAR_L();
        STAMP(39);
    }
    char* dst = p.ws + WS_QKV + (size_t)(32 * tj) * 16384 + (size_t)ti * 1024;
#pragma unroll
    for (int i = 0; i < 4; ++i) { const int gq = 4 * wid + i; st16<FUSED>(dst + (size_t)gq * 16384 + lane * 16, *(const LAS u32x4*)(L + gq * 1024 + lane * 16)); }
    STAMP(3);
    publish<FUSED>(p, FL_A + (ti * 16 + tj) * 8, wid, lane, tag, 42);
    if (FUSED && wid == 1 && lane == 0) st_dev(p.ws + WS_FLAG + FL_P + (ti * 16 + tj) * 8, tag);
}

namespace pb {
constexpr int GS = 3104;
constexpr int L_K = 0, L_V = 10 * GS, L_ONE = 20 * GS, L_Q = 21 * GS, L_O = 102400;
static_assert(L_Q + 10240 <= L_O, "LDS map");
}
#define ATT_STAMP() do {} while (0)
template <bool EDGE>
DI f32x16 attn_wave(lcptr L, int hh, int qb, int lane, int tstart, u64* stq) {
    using namespace pb;
    const int r = lane & 31, h = lane >> 5;
    bf16x8 q0; s16x4 q1;
    { lcptr qp = L + L_Q + hh * 5 * 1024 + (32 * qb + r) * 8;
      q0 = cat8(ld64(qp + (2 * h) * 1024), ld64(qp + (2 * h + 1) * 1024));
      const s16x4 t = ld64(qp + 4 * 1024); q1 = h ? (s16x4){0, 0, 0, 0} : t; }
    lcptr kp = L + L_K + hh * 5 * GS + (32 * qb + r) * 8;
    const f32x16 z16 = {};
    bf16x8 k0[9]; s16x4 k1[9];
#pragma unroll
    for (int t = 0; t < 9; ++t) { k0[t] = cat8(ld64v(kp + (2 * h) * GS + t * 256), ld64v(kp + (2 * h + 1) * GS + t * 256)); k1[t] = ld64v(kp + 4 * GS + t * 256); }
    float m;
    { f32x16 a = MFMA32K8(k1[8], q1, MFMA32(k0[8], q0, z16)), b = MFMA32K8(k1[0], q1, MFMA32(k0[0], q0, z16));
      const float bias = (!EDGE || tstart == 0) ? 0.f : -INFINITY;
#pragma unroll
      for (int i = 0; i < 16; ++i) { const int c = (i & 3) + 8 * (i >> 2) + 4 * h; a[i] = (c <= r) ? a[i] : (EDGE ? b[i] + bias : b[i]); }
      float x = __builtin_fmaxf(__builtin_fmaxf(a[0], a[1]), a[2]), y = __builtin_fmaxf(__builtin_fmaxf(a[3], a[4]), a[5]);
#pragma unroll
      for (int i = 6; i < 14; i += 4) { x = __builtin_fmaxf(__builtin_fmaxf(x, a[i]), a[i + 1]); y = __builtin_fmaxf(__builtin_fmaxf(y, a[i + 2]), a[i + 3]); }
      m = __builtin_fmaxf(__builtin_fmaxf(__builtin_fmaxf(x, a[14]), a[15]), y); }
#pragma unroll
    for (int t = 1; t < 8; ++t) { const f32x16 a = MFMA32K8(k1[t], q1, MFMA32(k0[t], q0, z16));
        float x = __builtin_fmaxf(__builtin_fmaxf(a[0], a[1]), a[2]), y = __builtin_fmaxf(__builtin_fmaxf(a[3], a[4]), a[5]);
#pragma unroll
        for (int i = 6; i < 14; i += 4) { x = __builtin_fmaxf(__builtin_fmaxf(x, a[i]), a[i + 1]); y = __builtin_fmaxf(__builtin_fmaxf(y, a[i + 2]), a[i + 3]); }
        x = __builtin_fmaxf(__builtin_fmaxf(x, a[14]), a[15]);
        if (EDGE) x = t >= tstart ? x : -INFINITY;
        m = __builtin_fmaxf(__builtin_fmaxf(m, x), (EDGE && t < tstart) ? -INFINITY : y); }
    { auto rr = __builtin_amdgcn_permlane32_swap(__float_as_uint(m), __float_as_uint(m), false, false); m = __builtin_fmaxf(__uint_as_float(rr[0]), __uint_as_float(rr[1])); }
    f32x16 nm16;
#pragma unroll
    for (int i = 0; i < 16; ++i) nm16[i] = -m;
    BAR_VL(0);
    ATT_STAMP();
    const int blk = (lane >> 4) & 1, vq = (lane & 15) >> 2, vp = lane & 3, dg = 4 * blk + vp;
    lcptr vpz = L + (dg < 5 ? L_V + (hh * 5 + dg) * GS : L_ONE) + (32 * qb + 4 * h + vq) * 8;
    f32x16 O = z16;
#pragma unroll
    for (int t = 1; t < 8; ++t) {
        const bf16x8 v0 = cat8(ldtr(vpz + t * 256), ldtr(vpz + t * 256 + 64)), v1 = cat8(ldtr(vpz + t * 256 + 128), ldtr(vpz + t * 256 + 192));
        f32x16 a = MFMA32K8(k1[t], q1, MFMA32(k0[t], q0, nm16));
        if (EDGE) { const float bias = t >= tstart ? 0.f : -INFINITY;
#pragma unroll
            for (int i = 0; i < 16; ++i) a[i] += bias; }
        unsigned P[8];
#pragma unroll
        for (int i = 0; i < 8; ++i) P[i] = pk2(__builtin_amdgcn_exp2f(a[2 * i]), __builtin_amdgcn_exp2f(a[2 * i + 1]));
        O = MFMA32(v0, __builtin_bit_cast(bf16x8, ((u32x4){P[0], P[1], P[2], P[3]})), O);
        O = MFMA32(v1, __builtin_bit_cast(bf16x8, ((u32x4){P[4], P[5], P[6], P[7]})), O);
    }
    { const bf16x8 v0 = cat8(ldtr(vpz + 8 * 256), ldtr(vpz + 8 * 256 + 64)), v1 = cat8(ldtr(vpz + 8 * 256 + 128), ldtr(vpz + 8 * 256 + 192));
      const bf16x8 w0 = cat8(ldtr(vpz), ldtr(vpz + 64)), w1 = cat8(ldtr(vpz + 128), ldtr(vpz + 192));
      f32x16 a = MFMA32K8(k1[8], q1, MFMA32(k0[8], q0, nm16)); const f32x16 b = MFMA32K8(k1[0], q1, MFMA32(k0[0], q0, nm16));
      const float bias = (!EDGE || tstart == 0) ? 0.f : -INFINITY;
#pragma unroll
      for (int i = 0; i < 16; ++i) { const int c = (i & 3) + 8 * (i >> 2) + 4 * h; a[i] = (c <= r) ? a[i] : (EDGE ? b[i] + bias : b[i]); }
      unsigned P[8], P0[8];
#pragma unroll
      for (int i = 0; i < 8; ++i) { P[i] = pk2(__builtin_amdgcn_exp2f(a[2 * i]), __builtin_amdgcn_exp2f(a[2 * i + 1]));
          const int c0 = ((2 * i) & 3) + 8 * ((2 * i) >> 2) + 4 * h; const unsigned mk = (c0 <= r ? 0xffffu : 0u) | (c0 + 1 <= r ? 0xffff0000u : 0u);
          P0[i] = P[i] & ~mk; P[i] &= mk; }
      O = MFMA32(v0, __builtin_bit_cast(bf16x8, ((u32x4){P[0], P[1], P[2], P[3]})), O);
      O = MFMA32(v1, __builtin_bit_cast(bf16x8, ((u32x4){P[4], P[5], P[6], P[7]})), O);
      O = MFMA32(w0, __builtin_bit_cast(bf16x8, ((u32x4){P0[0], P0[1], P0[2], P0[3]})), O);
      O = MFMA32(w1, __builtin_bit_cast(bf16x8, ((u32x4){P0[4], P0[5], P0[6], P0[7]})), O); }
    return O;
}
template <bool FUSED>
DI void phase_b(const Params& p, lptr L, int lane, int wid, int ti, int tj, u64 tag) {
    using namespace pb;
    const int tid = wid * 64 + lane;
    const unsigned lds0 = (unsigned)(uintptr_t)L;
    rp2_issue(p, lds0, lane, wid, ti, tj);
    if (ti < 2) {
        for (int n = tid; n < (2 - ti) * 20 * 64; n += 512) { const int pc = n >> 6, mb = pc % (2 - ti), gq = pc / (2 - ti);
            *(LAS u32x4*)(L + gq * GS + mb * 1024 + (n & 63) * 16) = (u32x4){0u, 0u, 0u, 0u}; }
    }
    if (tid < 384) *(LAS u32x2*)(L + L_ONE + tid * 8) = (u32x2){0x00003f80u, 0u};
    const int sub = wid & 3, mtile = sub < 3 ? ti - 2 + sub : ti;
    const char* g0 = p.ws + WS_QKV + (size_t)((sub < 3 ? 160 : 0) + 5 * (2 * tj + (wid >> 2))) * 16384 + (size_t)(mtile < 0 ? 0 : mtile) * 1024;
    const unsigned l0 = lds0 + (sub < 3 ? L_K + (wid >> 2) * 5 * GS + sub * 1024 : L_Q + (wid >> 2) * 5120);
    BAR_VL(0);
    rp2_store<FUSED>(p, L, lane, wid, ti, tj);
    if (FUSED) {
        if (wid == 7) { const int mb = lane / 6, sec = (lane % 6) >> 1, e = lane & 1, mt2 = ti - 2 + mb;
            const bool need = lane < 18 && mt2 >= 0 && (sec > 0 || mb == 2);
            poll_flags(p.ws + WS_FLAG + FL_A + ((mt2 < 0 ? 0 : mt2) * 16 + ((640 * sec + 40 * tj + 39 * e) >> 7)) * 8, need, tag); STAMPW(35, 7); }
    }
    BAR_L();
    STAMP(4);
    if (sub == 3) {
#pragma unroll
        for (int d = 0; d < 5; ++d) glds16<FUSED>(lane * 16, g0 + d * 16384, l0 + d * 1024);
        BAR_VL(0);
    } else if (mtile >= 0) {
#pragma unroll
        for (int d = 0; d < 5; ++d) glds16<FUSED>(lane * 16, g0 + d * 16384, l0 + d * GS);
#pragma unroll
        for (int d = 0; d < 5; ++d) glds16<FUSED>(lane * 16, g0 + (160 + d) * 16384, l0 + L_V + d * GS);
        BAR_VL(5);
    } else BAR_L();
    STAMP(5);
    const int hh = wid >> 2, qb = wid & 3, r = lane & 31, h = lane >> 5;
    const int q0g = 128 * ti + 32 * qb;
    f32x16 O;
    u64* stq = nullptr;
    if (ti >= 2) O = attn_wave<false>((lcptr)L, hh, qb, lane, 0, stq); else O = attn_wave<true>((lcptr)L, hh, qb, lane, q0g >= 256 ? 0 : 8 - (q0g >> 5), stq);
    { auto rr = __builtin_amdgcn_permlane32_swap(__float_as_uint(O[8]), __float_as_uint(O[8]), false, false);
      const float il = 1.0f / __uint_as_float(rr[1]);
      const int row = 32 * qb + r;
#pragma unroll
      for (int u = 0; u < 3; ++u) { const int dgp = 2 * u + h; if (dgp < 5) { const int cgp = 5 * hh + dgp;
          *(LAS u32x2*)(L + L_O + (cgp >> 1) * 2048 + row * 16 + (cgp & 1) * 8) = (u32x2){pk2(O[4 * u] * il, O[4 * u + 1] * il), pk2(O[4 * u + 2] * il, O[4 * u + 3] * il)}; } } }
    STAMP(6); STAMPW(37, 7);
    BAR_L();
    for (int n = wid; n < 10; n += 8)
        st16<FUSED>(p.ws + WS_O + (size_t)(5 * tj + (n >> 1)) * 32768 + (size_t)(128 * ti + 64 * (n & 1)) * 16 + lane * 16, *(const LAS u32x4*)(L + L_O + n * 1024 + lane * 16));
    STAMP(7);
    publish<FUSED>(p, FL_B + (ti * 16 + tj) * 8, wid, lane, tag);
}

template <int MODE> struct PC;
template <> struct PC<0> { static constexpr int NST = 10, RB = 10, RA = 6, LA = 102400, L_RES = 151552, L_X = 102400, L_STG = 122880; };
template <> struct PC<1> { static constexpr int NST = 27, RB = 8, RA = 8, LA = 81920, L_RES = 147456, L_X = 81920, L_STG = 102400; };
template <bool FUSED, int MODE>
DI void ce_prefetch(const Params& p, lptr L, int lane, int wid, int tj) {
    const char* wb = p.ws + (MODE ? WS_PWD + (size_t)(tj >> 1) * 54 * 5120 : WS_PWO + (size_t)(tj >> 1) * 20 * 5120) + (wid - 6) * 5120 + 2048;
    const unsigned l = (unsigned)(uintptr_t)L + (wid - 6) * 5120 + 2048;
#pragma unroll 1
    for (int s = 0; s < PC<MODE>::RB; ++s) gl_lin5<FUSED>(lane * 16, wb + (size_t)s * 10240, l + s * 10240);
}
template <bool FUSED, int MODE>
DI void phase_ce(const Params& p, lptr L, int lane, int wid, int ti, int tj, u64 tag) {
    typedef PC<MODE> C;
    constexpr int NST = C::NST, RA = C::RA, RB = C::RB;
    const unsigned lds0 = (unsigned)(uintptr_t)L;
    const int rh = tj & 1, ct = tj >> 1, r0 = 128 * ti + 64 * rh;
    const int wm = (wid >> 1) & 1, wk = wid & 1, r16 = lane & 15, g = lane >> 4;
    if (wid >= 6) {
        if (MODE == 0) {
            bar_vl_n<5, RB - 1>(RB - 1);
            BAR_VL(0);
#pragma unroll 1
            for (int s = 2; s < NST; ++s) BAR_L();
        } else {
            const char* wb = p.ws + WS_PWD + (size_t)ct * 54 * 5120 + (wid - 6) * 5120 + 2048 + (size_t)RB * 10240;
            const unsigned l0 = lds0 + (wid - 6) * 5120 + 2048; unsigned l = l0;
            ring_loader<RB, NST, 5, 2, false>([&]() { gl_lin5<FUSED>(lane * 16, wb, l); wb += 10240; l = l == l0 + (RB - 1) * 10240 ? l0 : l + 10240; }, [&](int) {}, [&](int) {});
        }
        BAR_L(); BAR_L(); BAR_L();
    } else if (wid >= 4) {
        const int la = wid - 4;
        const unsigned voA = lane * 16;
#pragma unroll
        for (int i = 0; i < 5; ++i) { const int pc = 5 * la + i;
            if (MODE == 0) { const int n = 64 * pc + lane; glds16<false>((unsigned)((n / 10) * D_ * 2 + (n % 10) * 16), (const char*)p.x + (size_t)r0 * D_ * 2 + ct * 160, lds0 + C::L_RES + pc * 1024); }
            else glds16<FUSED>(voA, p.ws + WS_H + (size_t)(10 * ct + pc) * 32768 + (size_t)r0 * 16, lds0 + C::L_RES + pc * 1024); }
        if (FUSED) poll_flags(p.ws + WS_FLAG + (MODE ? FL_D : FL_B) + (ti * 16 + (lane & 15)) * 8, lane < 16, tag);
        STAMPW(MODE ? 17 : 9, 4);
        const char* ab = p.ws + (MODE ? WS_HH : WS_O) + (size_t)r0 * 16 + (size_t)(4 * la) * 32768 + 2048;
        unsigned vp[4];
#pragma unroll
        for (int i = 0; i < 4; ++i) vp[i] = voA + (unsigned)i * (32768 - 1024);
        const unsigned l0 = lds0 + C::LA + la * 4096 + 2048; unsigned l = l0;
        ring_loader<RA, NST, 4, 2, true>([&]() { gl_str4<FUSED>(vp, ab, l); ab += 8 * 32768; l = l == l0 + (RA - 1) * 8192 ? l0 : l + 8192; }, [&](int) {}, [&](int) {});
        BAR_L(); BAR_L(); BAR_L();
    } else {
        const unsigned aA = (unsigned)(C::LA + (4 * wk + g) * 1024 + (32 * wm + r16) * 16), aB = (unsigned)(wk * 5120 + lane * 16);
        f32x4 acc[2][5];
#pragma unroll
        for (int i = 0; i < 2; ++i)
#pragma unroll
            for (int n = 0; n < 5; ++n) acc[i][n] = (f32x4){0.f, 0.f, 0.f, 0.f};
        bf16x8 Af[3][2], Bf[3][5];
        int sa = 0, sb = 0;
        auto rd = [&](int b) {
            lcptr la = (lcptr)L + aA + sa * 8192; lcptr lb = (lcptr)L + aB + sb * 10240;
#pragma unroll
            for (int i = 0; i < 2; ++i) Af[b][i] = ld128(la + i * 256);
#pragma unroll
            for (int n = 0; n < 5; ++n) Bf[b][n] = ld128(lb + n * 1024);
            sa = sa == RA - 1 ? 0 : sa + 1; sb = sb == RB - 1 ? 0 : sb + 1;
        };
        auto mm = [&](int b) {
#pragma unroll
            for (int n = 0; n < 5; ++n)
#pragma unroll
                for (int i = 0; i < 2; ++i) acc[i][n] = MFMA16(Bf[b][n], Af[b][i], acc[i][n]);
        };
        auto bar = [&]() { __builtin_amdgcn_sched_barrier(0); __builtin_amdgcn_s_waitcnt(0xC77F); asm volatile("" ::: "memory"); __builtin_amdgcn_s_barrier(); asm volatile("" ::: "memory"); __builtin_amdgcn_sched_barrier(0); };
        auto il = [&]() { SGB(0x8, 1); SGB(0x100, 2); SGB(0x8, 1); SGB(0x100, 2); SGB(0x8, 1); SGB(0x100, 2); SGB(0x8, 1); SGB(0x100, 1); SGB(0x8, 6); __builtin_amdgcn_sched_barrier(0); };
        bar(); STAMP(MODE ? 18 : 10); if (MODE) STAMPC(28); rd(0);
        bar(); rd(1);
        int s = 0;
#pragma unroll 1
        for (; s + 4 < NST; s += 3) {
            bar(); rd(2); mm(0); il();
            bar(); rd(0); mm(1); il();
            bar(); rd(1); mm(2); il();
        }
        if (NST % 3 == 0) { bar(); rd(2); mm(0); il(); mm(1); mm(2); }
        else { bar(); rd(2); mm(0); il(); bar(); rd(0); mm(1); il(); mm(2); mm(0); }
        STAMP(MODE ? 19 : 11); if (MODE) STAMPC(29);
        BAR_L();
#pragma unroll
        for (int n = 0; n < 5; ++n) *(LAS f32x4*)(L + C::L_X + (wid * 5 + n) * 1024 + lane * 16) = wk ? acc[0][n] : acc[1][n];
        BAR_L();
        const int row = 32 * wm + 16 * wk + r16;
        float ps = 0.f, pq = 0.f;
#pragma unroll
        for (int n = 0; n < 5; ++n) { const f32x4 v = (wk ? acc[1][n] : acc[0][n]) + *(const LAS f32x4*)(L + C::L_X + ((wid ^ 1) * 5 + n) * 1024 + lane * 16);
            u32x2 rr;
            if (MODE == 0) rr = *(const LAS u32x2*)(L + C::L_RES + row * 160 + n * 32 + g * 8);
            else rr = *(const LAS u32x2*)(L + C::L_RES + (2 * n + (g >> 1)) * 1024 + row * 16 + (g & 1) * 8);
            const unsigned a = pk2(v[0], v[1]), b = pk2(v[2], v[3]);
            const u32x2 o = {pk2(bflo(rr[0]) + bflo(a), bfhi(rr[0]) + bfhi(a)), pk2(bflo(rr[1]) + bflo(b), bfhi(rr[1]) + bfhi(b))};
            if (MODE == 0) {
#pragma unroll
                for (int e = 0; e < 2; ++e) { const float lo = bflo(o[e]), hi = bfhi(o[e]); ps += lo + hi; pq += lo * lo + hi * hi; }
                *(LAS u32x2*)(L + C::L_STG + (2 * n + (g >> 1)) * 1024 + row * 16 + (g & 1) * 8) = o;
            } else *(LAS u32x2*)(L + C::L_STG + row * 160 + n * 32 + g * 8) = o; }
        if (MODE == 0) { ps = sum_x32(sum_x16(ps)); pq = sum_x32(sum_x16(pq));
            if (g == 0) { char* dst = p.ws + WS_PS + ((size_t)(ti * 8 + ct) * 128 + 64 * rh + row) * 8; const u64 v = ((u64)__float_as_uint(pq) << 32) | __float_as_uint(ps);
                if (FUSED) st_dev(dst, v); else *(u64*)dst = v; } }
        BAR_L();
    }
    for (int n = wid; n < 10; n += 8) { const u32x4 v = *(const LAS u32x4*)(L + C::L_STG + n * 1024 + lane * 16);
        if (MODE == 0) st16<FUSED>(p.ws + WS_H + (size_t)(10 * ct + n) * 32768 + (size_t)r0 * 16 + lane * 16, v);
        else { const int u = 64 * n + lane; *(u32x4*)(p.out + (size_t)(r0 + u / 10) * D_ * 2 + ct * 160 + (u % 10) * 16) = v; } }
    STAMP(MODE ? 20 : 12);
    if (MODE == 0) publish<FUSED>(p, FL_C + (ti * 16 + tj) * 8, wid, lane, tag, 43);
}

namespace pd { constexpr int RB = 3, RA = 4, SB = 28672, LA = RB * SB, NST = 10, L_STAT = LA + RA * 16384, L_CS = L_STAT + 1024, L_STG = LA; }
template <bool FUSED>
DI void d_bissue(const char* wb, unsigned l, int lane) { gl_lin7<FUSED>(lane * 16, wb, l); gl_lin7<FUSED>(lane * 16, wb + 7168, l + 7168); }
template <bool FUSED>
DI void d_prefetch(const Params& p, lptr L, int lane, int wid, int tj) {
    const char* wb = p.ws + WS_PWG + (size_t)tj * 286720 + (wid - 6) * 14336 + 3072;
    const unsigned l = (unsigned)(uintptr_t)L + (wid - 6) * 14336 + 3072;
#pragma unroll 1
    for (int s = 0; s < pd::RB; ++s) d_bissue<FUSED>(wb + (size_t)s * pd::SB, l + s * pd::SB, lane);
}
template <bool FUSED>
DI void phase_d(const Params& p, lptr L, int lane, int wid, int ti, int tj, u64 tag) {
    using namespace pd;
    const unsigned lds0 = (unsigned)(uintptr_t)L;
    if (wid >= 6) {
        const int lb = wid - 6;
        u32x2 ps[8]; float cp[2][16];
        const int row = 64 * lb + lane;
        const char* wb = p.ws + WS_PWG + (size_t)tj * 286720 + lb * 14336 + 3072 + (size_t)RB * SB;
        const unsigned l0 = lds0 + lb * 14336 + 3072; unsigned l = l0;
        auto issue = [&]() { d_bissue<FUSED>(wb, l, lane); wb += SB; l = l == l0 + (RB - 1) * SB ? l0 : l + SB; };
        static_assert(RB == 3 && NST == 10, "the waits below");
        BAR_VL(28);
#pragma unroll
        for (int c = 0; c < 8; ++c) ld64_async(ps[c], p.ws + WS_PS + ((size_t)(ti * 8 + c) * 128 + row) * 8);
        BAR_VL(22); issue();
#pragma unroll
        for (int i = 0; i < 16; ++i) ld32_async(cp[0][i], (const float*)(p.ws + WS_CSP) + (i * 16 + tj) * 224 + row);
        BAR_VL(38); issue();
#pragma unroll
        for (int i = 0; i < 16; ++i) ld32_async(cp[1][i], (const float*)(p.ws + WS_CSP) + (i * 16 + tj) * 224 + (row + 128 < 224 ? row + 128 : 223));
        BAR_VL(46); issue();
        BAR_VL(30); issue();
        BAR_VL(14); issue();
        { float a = 0.f, q = 0.f;
#pragma unroll
          for (int c = 0; c < 8; ++c) { asm volatile("" : "+v"(ps[c])); a += __uint_as_float(ps[c][0]); q += __uint_as_float(ps[c][1]); }
          *(LAS f32x2*)(L + L_STAT + row * 8) = ln_scalars(a, q);
#pragma unroll
          for (int z = 0; z < 2; ++z) { float sm = 0.f;
#pragma unroll
              for (int i = 0; i < 16; ++i) { asm volatile("" : "+v"(cp[z][i])); sm += cp[z][i]; }
              if (row + 128 * z < 224) *(LAS float*)(L + L_CS + (row + 128 * z) * 4) = sm; } }
        BAR_VL(14); issue();
        BAR_VL(14); issue();
        BAR_VL(14);
        if (FUSED) {
            poll_flags(p.ws + WS_FLAG + FL_B + ((lane & 15) * 16 + (tj & ~1) + ((lane >> 4) & 1)) * 8, lane < 32, tag);
#pragma unroll 1
            for (int i = 0; i < 3; ++i) { int ln = (lb * 3 + i) * 64 + lane; ln = ln < 270 ? ln : 269;
                warm64(p.ws + WS_PWD + (size_t)(tj >> 1) * 276480 + (size_t)(2 * (ti & 3) + (tj & 1)) * 34560, (unsigned)ln * 128u, lds0); }
            BAR_VL(3);
        } else BAR_VL(0);
        BAR_L(); BAR_L();
    } else if (wid >= 4) {
        const int la = wid - 4;
        if (FUSED) poll_flags(p.ws + WS_FLAG + FL_C + (ti * 16 + (lane & 15)) * 8, lane < 16, tag);
        STAMPW(13, 4);
        const char* ab = p.ws + WS_H + (size_t)(128 * ti + 64 * la) * 16 + 4096;
        unsigned vp[4];
#pragma unroll
        for (int i = 0; i < 4; ++i) vp[i] = lane * 16 + (unsigned)i * (32768 - 2048);
        const unsigned l0 = lds0 + LA + la * 1024 + 4096; unsigned l = l0;
        ring_loader<RA, NST, 8, 1, true>([&]() { gl_str4w<FUSED>(vp, ab, l); gl_str4w<FUSED>(vp, ab + 4 * 32768, l + 8192); ab += 8 * 32768; l = l == l0 + (RA - 1) * 16384 ? l0 : l + 16384; }, [&](int) {}, [&](int) {});
        BAR_L(); BAR_L();
    } else {
        const int r32 = lane & 31, h = lane >> 5;
        const unsigned aA = (unsigned)(LA + h * 2048 + (32 * wid + r32) * 16), aB = (unsigned)(lane * 16);
        f32x16 acc[7];
#pragma unroll
        for (int t = 0; t < 7; ++t)
#pragma unroll
            for (int i = 0; i < 16; ++i) acc[t][i] = 0.f;
        bf16x8 A0, B0[7], A1, B1[7];
        auto rd = [&](bf16x8& Af, bf16x8* Bf, int sa, int sb, int u) {
            lcptr lb = (lcptr)L + aB + sb * SB + u * 7168;
            Af = ld128((lcptr)L + aA + sa * 16384 + u * 4096);
#pragma unroll
            for (int t = 0; t < 7; ++t) Bf[t] = ld128(lb + t * 1024);
        };
        auto mm = [&](const bf16x8& Af, const bf16x8* Bf) {
#pragma unroll
            for (int t = 0; t < 7; ++t) acc[t] = MFMA32(Bf[t], Af, acc[t]);
        };
        auto il = [&](const bf16x8& Af, const bf16x8* Bf) { SGB(0x8, 1); SGB(0x100, 2); SGB(0x8, 1); SGB(0x100, 2); SGB(0x8, 1); SGB(0x100, 2); SGB(0x8, 1); SGB(0x100, 2); SGB(0x8, 3); __builtin_amdgcn_sched_barrier(0);
            asm volatile("" :: "v"(Af), "v"(Bf[0]), "v"(Bf[1]), "v"(Bf[2]), "v"(Bf[3]), "v"(Bf[4]), "v"(Bf[5]), "v"(Bf[6])); __builtin_amdgcn_sched_barrier(0); };
        CBAR();
        STAMP(14); STAMPC(24);
        rd(A0, B0, 0, 0, 0);
        int sa = 0, sb = 0;
#pragma unroll 1
        for (int s = 0; s < NST - 1; ++s) {
            rd(A1, B1, sa, sb, 1); mm(A0, B0); il(A0, B0);
            rd(A0, B0, sa, sb, 2); mm(A1, B1); il(A1, B1);
            rd(A1, B1, sa, sb, 3); mm(A0, B0); il(A0, B0);
            sa = sa == RA - 1 ? 0 : sa + 1; sb = sb == RB - 1 ? 0 : sb + 1;
            CBAR();
            rd(A0, B0, sa, sb, 0); mm(A1, B1); il(A1, B1);
        }
        rd(A1, B1, sa, sb, 1); mm(A0, B0); il(A0, B0);
        rd(A0, B0, sa, sb, 2); mm(A1, B1); il(A1, B1);
        rd(A1, B1, sa, sb, 3); mm(A0, B0); il(A0, B0);
        mm(A1, B1);
        STAMP(15); STAMPC(25);
        BAR_L();
        STAMP(40);
        const int tok = 32 * wid + r32;
        const f32x2 st = *(const LAS f32x2*)(L + L_STAT + tok * 8);
        const float r2 = st.y * st.y;
        const f32x2 nm2 = {-st.x, -st.x}, r22 = {r2, r2};
        auto glu4 = [&](float g0, float g1, float g2, float g3, float u0, float u1, float u2, float u3, const f32x4& cg, const f32x4& cu) {
            const f32x2 za = (f32x2){cg[0], cg[1]} * nm2 + (f32x2){g0, g1}, zb = (f32x2){cg[2], cg[3]} * nm2 + (f32x2){g2, g3};
            const f32x2 ya = ((f32x2){cu[0], cu[1]} * nm2 + (f32x2){u0, u1}) * r22, yb = ((f32x2){cu[2], cu[3]} * nm2 + (f32x2){u2, u3}) * r22;
            const f32x2 oa = (f32x2){__builtin_fmaxf(za[0], 0.f), __builtin_fmaxf(za[1], 0.f)} * ya, ob = (f32x2){__builtin_fmaxf(zb[0], 0.f), __builtin_fmaxf(zb[1], 0.f)} * yb;
            return (u32x2){pk2(oa[0], oa[1]), pk2(ob[0], ob[1])}; };
#pragma unroll
        for (int a = 0; a < 3; ++a)
#pragma unroll
            for (int u = 0; u < 4; ++u) {
                const f32x4 cg = *(const LAS f32x4*)(L + L_CS + (64 * a + 8 * u + 4 * h) * 4), cu = *(const LAS f32x4*)(L + L_CS + (64 * a + 32 + 8 * u + 4 * h) * 4);
                *(LAS u32x2*)(L + L_STG + (4 * a + u) * 2048 + tok * 16 + h * 8) = glu4(acc[2 * a][4 * u], acc[2 * a][4 * u + 1], acc[2 * a][4 * u + 2], acc[2 * a][4 * u + 3],
                    acc[2 * a + 1][4 * u], acc[2 * a + 1][4 * u + 1], acc[2 * a + 1][4 * u + 2], acc[2 * a + 1][4 * u + 3], cg, cu); }
#pragma unroll
        for (int u = 0; u < 2; ++u) {
            const f32x4 cg = *(const LAS f32x4*)(L + L_CS + (192 + 8 * u + 4 * h) * 4), cu = *(const LAS f32x4*)(L + L_CS + (208 + 8 * u + 4 * h) * 4);
            *(LAS u32x2*)(L + L_STG + (12 + u) * 2048 + tok * 16 + h * 8) = glu4(acc[6][4 * u], acc[6][4 * u + 1], acc[6][4 * u + 2], acc[6][4 * u + 3],
                acc[6][8 + 4 * u], acc[6][8 + 4 * u + 1], acc[6][8 + 4 * u + 2], acc[6][8 + 4 * u + 3], cg, cu); }
        STAMP(41);
        BAR_L();
    }
    for (int n = wid; n < 28; n += 8) { const int c = 14 * tj + (n >> 1); if (c < 216)
        st16<FUSED>(p.ws + WS_HH + (size_t)c * 32768 + (size_t)(128 * ti + 64 * (n & 1)) * 16 + lane * 16, *(const LAS u32x4*)(L + L_STG + n * 1024 + lane * 16)); }
    STAMP(16);
    publish<FUSED>(p, FL_D + (ti * 16 + tj) * 8, wid, lane, tag, 44);
}

#define ROTC 0
#define ROTD 0
#define ROTE 1
#define PROLOG() \
    extern __shared__ __attribute__((aligned(16))) char smem[]; \
    lptr L = (lptr)smem; \
    const int tid = threadIdx.x, lane = tid & 63, wid = __builtin_amdgcn_readfirstlane(tid >> 6); \
    const int xcd = blockIdx.x & 7, idx = blockIdx.x >> 3; \
    const int ti = 4 * (xcd & 3) + (idx & 3), tj = 8 * (xcd >> 2) + (idx >> 2); \
    const int rot10 = ((idx >> 2) + 3 * (idx & 3)) % 10, rot27 = ((idx >> 2) * 3 + (idx & 3) * 7) % 27; \
    const int rotC = ROTC ? rot10 : 0, rotD = ROTD ? rot10 : 0, rotE = ROTE ? rot27 : 0, rotA = ROTA == 1 ? rot10 : ROTA == 2 ? (((idx >> 2) + (idx & 3)) & 3) : ROTA == 3 ? ((idx >> 2) & 1) * 5 : 0; \
    (void)rot10; (void)rot27; (void)tid; (void)rotA; (void)rotC; (void)rotD; (void)rotE

__global__ __launch_bounds__(512, 2) void k_fused(const Params p) {
    PROLOG();
    STAMP(0);
    u64 tag = 0; u32x2 ev;
    ld64_async(ev, p.ws + WS_FLAG + FL_E);
    if (wid < 4) {
        const char* pc = (const char*)(__builtin_amdgcn_s_getpc() & ~127ull);
        int ln = ((idx * 4 + wid) % 6) * 64 + lane; ln = ln < CODE_LINES ? ln : CODE_LINES - 1;
        warm64(pc, (unsigned)ln * 128u, (unsigned)(uintptr_t)L);
    }
    if (tj >= 15) phase_a_stats<true>(p, L, lane, wid, ti, ev, tag); else phase_a<true>(p, L, lane, wid, ti, tj, rotA, ev, tag);
    if (tid == 0) st_dev(p.ws + WS_FLAG + FL_START + blockIdx.x * 8, tag);
    phase_b<true>(p, L, lane, wid, ti, tj, tag);
    if (wid >= 6) {
        poll_flags(p.ws + WS_FLAG + FL_P + ((lane & 15) * 16 + (lane < 16 ? tj : (tj & ~1) + ((lane >> 4) & 1))) * 8, lane < 48, tag);
        ce_prefetch<true, 0>(p, L, lane, wid, tj);
    }
    STAMP(8);
    phase_ce<true, 0>(p, L, lane, wid, ti, tj, tag);
    if (wid >= 6) d_prefetch<true>(p, L, lane, wid, tj);
    if (blockIdx.x == 0 && wid == 5) {
        for (unsigned spins = 0; spins < (1u << 17); ++spins) {
            bool ok = true;
#pragma unroll
            for (int i = 0; i < 4; ++i) ok &= ld_dev(p.ws + WS_FLAG + FL_START + (lane * 4 + i) * 8) == tag;
            if (__all(ok)) break;
            __builtin_amdgcn_s_sleep(1);
        }
        if (lane == 0) st_dev(p.ws + WS_FLAG + FL_E, tag);
    }
    phase_d<true>(p, L, lane, wid, ti, tj, tag);
    if (wid >= 6) ce_prefetch<true, 1>(p, L, lane, wid, tj);
    phase_ce<true, 1>(p, L, lane, wid, ti, tj, tag);
    STAMP(21);
}

extern "C" void kernel_launch(void* const* d_in, const int* in_sizes, int n_in, void* d_out, int out_size, void* d_ws, size_t ws_size, hipStream_t stream) {
    Params p;
    p.x = (const u16*)d_in[0]; p.wqkv = (const u16*)d_in[1]; p.wo = (const u16*)d_in[2];
    p.wg = (const u16*)d_in[4]; p.wu = (const u16*)d_in[5]; p.wd = (const u16*)d_in[6];
    p.out = (char*)d_out; p.ws = (char*)d_ws;
    (void)hipFuncSetAttribute((const void*)k_fused, hipFuncAttributeMaxDynamicSharedMemorySize, LDS_BYTES);
    k_fused<<<256, 512, LDS_BYTES, stream>>>(p);
}
```

```cpp
#include <hip/hip_runtime.h>
#include <hip/hip_bf16.h>
#include <stdint.h>

#define LAS __attribute__((address_space(3)))
#define DI __device__ __forceinline__
typedef unsigned short u16;
typedef unsigned long long u64;
typedef short bf16x8 __attribute__((ext_vector_type(8)));
typedef short s16x4 __attribute__((ext_vector_type(4)));
typedef float f32x2 __attribute__((ext_vector_type(2)));
typedef float f32x4 __attribute__((ext_vector_type(4)));
typedef float f32x16 __attribute__((ext_vector_type(16)));
typedef unsigned u32x2 __attribute__((ext_vector_type(2)));
typedef unsigned u32x4 __attribute__((ext_vector_type(4)));
typedef __bf16 bf16x2_t __attribute__((ext_vector_type(2)));
typedef LAS char* lptr;
typedef LAS const char* lcptr;

static constexpr int S_ = 2048, D_ = 640, H_ = 32, DH_ = 20, F_ = 1728;
static constexpr size_t WS_FLAG = 0;
#define FSTR 128
static constexpr size_t FS = FSTR;
static constexpr size_t FL_E = 0, FL_START = 1024, FL_A = 4096, FL_B = FL_A + 256 * FS, FL_C = FL_B + 256 * FS, FL_D = FL_C + 256 * FS, FL_P = FL_D + 256 * FS, FL_END = FL_P + 256 * FS;
static constexpr size_t WS_QKV = 262144;
static constexpr size_t WS_O = WS_QKV + (size_t)480 * 16384;
static constexpr size_t WS_H = WS_O + (size_t)80 * 32768;
static constexpr size_t WS_HH = WS_H + (size_t)80 * 32768;
static constexpr size_t WS_CSP = WS_HH + (size_t)216 * 32768;
static constexpr size_t WS_PS = WS_CSP + (size_t)16 * 16 * 224 * 4;
static constexpr size_t WS_S1 = WS_PS + (size_t)16 * 8 * 128 * 8;
static constexpr size_t WS_PWO = WS_S1 + (size_t)2048 * 8;
static constexpr size_t WS_PWD = WS_PWO + (size_t)8 * 20 * 5 * 1024;
static constexpr size_t WS_PWG = WS_PWD + (size_t)8 * 54 * 5 * 1024;
static constexpr size_t WS_END = WS_PWG + (size_t)16 * 40 * 7 * 1024;
static constexpr int LDS_BYTES = 163840;
static constexpr int CODE_LINES = 344;
static constexpr float C2_ = 0.22360679774997896f * 1.4426950408889634f;

struct Params { const u16* x; const u16* wqkv; const u16* wo; const u16* wg; const u16* wu; const u16* wd; char* out; char* ws; };

#define STAMP(i) do {} while (0)
#define STAMPC(i) do {} while (0)
#define STAMPW(i, w) do {} while (0)

DI unsigned pk2(float a, float b) { f32x2 v = {a, b}; return __builtin_bit_cast(unsigned, __builtin_convertvector(v, bf16x2_t)); }
DI float bflo(unsigned u) { return __uint_as_float(u << 16); }
DI float bfhi(unsigned u) { return __uint_as_float(u & 0xffff0000u); }
DI float rbf(float a) { return bflo(pk2(a, 0.f) & 0xffffu); }
template <bool SC1> DI void glds16(unsigned voff, const void* sbase, unsigned ldsaddr) {
    unsigned keep;
    if (SC1) asm volatile("s_mov_b32 %0, m0\n\ts_mov_b32 m0, %3\n\ts_nop 0\n\tglobal_load_lds_dwordx4 %1, %2 sc1\n\ts_mov_b32 m0, %0" : "=&s"(keep) : "v"(voff), "s"(sbase), "s"(ldsaddr) : "memory");
    else asm volatile("s_mov_b32 %0, m0\n\ts_mov_b32 m0, %3\n\ts_nop 0\n\tglobal_load_lds_dwordx4 %1, %2\n\ts_mov_b32 m0, %0" : "=&s"(keep) : "v"(voff), "s"(sbase), "s"(ldsaddr) : "memory");
}
#define GL_(v, off, sc) "global_load_lds_dwordx4 %" #v ", %[g] offset:" #off sc "\n\t"
#define GLHEAD "s_mov_b32 m0, %[l]\n\ts_nop 0\n\t"
template <bool SC1> DI void gl_lin5(unsigned v, const char* g, unsigned l) {
    if (SC1) asm volatile(GLHEAD GL_(0, -2048, " sc1") GL_(0, -1024, " sc1") GL_(0, 0, " sc1") GL_(0, 1024, " sc1") GL_(0, 2048, " sc1") :: "v"(v), [g] "s"(g), [l] "s"(l) : "memory");
    else asm volatile(GLHEAD GL_(0, -2048, "") GL_(0, -1024, "") GL_(0, 0, "") GL_(0, 1024, "") GL_(0, 2048, "") :: "v"(v), [g] "s"(g), [l] "s"(l) : "memory");
}
template <bool SC1> DI void gl_lin7(unsigned v, const char* g, unsigned l) {
    if (SC1) asm volatile(GLHEAD GL_(0, -3072, " sc1") GL_(0, -2048, " sc1") GL_(0, -1024, " sc1") GL_(0, 0, " sc1") GL_(0, 1024, " sc1") GL_(0, 2048, " sc1") GL_(0, 3072, " sc1") :: "v"(v), [g] "s"(g), [l] "s"(l) : "memory");
    else asm volatile(GLHEAD GL_(0, -3072, "") GL_(0, -2048, "") GL_(0, -1024, "") GL_(0, 0, "") GL_(0, 1024, "") GL_(0, 2048, "") GL_(0, 3072, "") :: "v"(v), [g] "s"(g), [l] "s"(l) : "memory");
}
template <bool SC1> DI void gl_str4(const unsigned* v, const char* g, unsigned l) {
    if (SC1) asm volatile(GLHEAD GL_(0, -2048, " sc1") GL_(1, -1024, " sc1") GL_(2, 0, " sc1") GL_(3, 1024, " sc1") :: "v"(v[0]), "v"(v[1]), "v"(v[2]), "v"(v[3]), [g] "s"(g), [l] "s"(l) : "memory");
    else asm volatile(GLHEAD GL_(0, -2048, "") GL_(1, -1024, "") GL_(2, 0, "") GL_(3, 1024, "") :: "v"(v[0]), "v"(v[1]), "v"(v[2]), "v"(v[3]), [g] "s"(g), [l] "s"(l) : "memory");
}
template <bool SC1> DI void gl_str4w(const unsigned* v, const char* g, unsigned l) {
    if (SC1) asm volatile(GLHEAD GL_(0, -4096, " sc1") GL_(1, -2048, " sc1") GL_(2, 0, " sc1") GL_(3, 2048, " sc1") :: "v"(v[0]), "v"(v[1]), "v"(v[2]), "v"(v[3]), [g] "s"(g), [l] "s"(l) : "memory");
    else asm volatile(GLHEAD GL_(0, -4096, "") GL_(1, -2048, "") GL_(2, 0, "") GL_(3, 2048, "") :: "v"(v[0]), "v"(v[1]), "v"(v[2]), "v"(v[3]), [g] "s"(g), [l] "s"(l) : "memory");
}
DI void gl_str8(const unsigned* v, const char* g, unsigned l) {
    asm volatile(GLHEAD GL_(0, -4096, "") GL_(1, -3072, "") GL_(2, -2048, "") GL_(3, -1024, "") GL_(4, 0, "") GL_(5, 1024, "") GL_(6, 2048, "") GL_(7, 3072, "")
                 :: "v"(v[0]), "v"(v[1]), "v"(v[2]), "v"(v[3]), "v"(v[4]), "v"(v[5]), "v"(v[6]), "v"(v[7]), [g] "s"(g), [l] "s"(l) : "memory");
}
template <bool SC1> DI void st16(void* ptr, u32x4 v) {
    if (SC1) asm volatile("global_store_dwordx4 %0, %1, off sc1\n\ts_nop 1" :: "v"(ptr), "v"(v) : "memory");
    else *(u32x4*)ptr = v;
}
DI u64 ld_dev(const void* p_) { return __hip_atomic_load((const u64*)p_, __ATOMIC_RELAXED, __HIP_MEMORY_SCOPE_AGENT); }
DI void st_dev(void* p_, u64 v) { __hip_atomic_store((u64*)p_, v, __ATOMIC_RELAXED, __HIP_MEMORY_SCOPE_AGENT); }
DI void ld64_async(u32x2& v, const void* ptr) { asm volatile("global_load_dwordx2 %0, %1, off sc1" : "=v"(v) : "v"(ptr) : "memory"); }
DI void ld32_async(float& v, const void* ptr) { asm volatile("global_load_dword %0, %1, off sc1" : "=v"(v) : "v"(ptr) : "memory"); }
DI void poll_flags(const void* flag, bool need, u64 tag) {
    for (unsigned spins = 0; spins < (1u << 18); ++spins) {
        const u64 v = need ? ld_dev(flag) : tag;
        if (__all(v == tag)) break;
    }
    asm volatile("" ::: "memory");
}
DI void warm64(const void* base, unsigned voff, unsigned lds0) {
    unsigned keep;
    asm volatile("s_mov_b32 %0, m0\n\ts_mov_b32 m0, %3\n\ts_nop 0\n\tglobal_load_lds_dword %1, %2 sc1\n\ts_mov_b32 m0, %0" : "=&s"(keep) : "v"(voff), "s"(base), "s"(lds0 + (unsigned)(LDS_BYTES - 256)) : "memory");
}
#define VMWAIT(N) asm volatile("s_waitcnt vmcnt(%0)" :: "i"(N) : "memory")
#define BAR_L() asm volatile("s_waitcnt lgkmcnt(0)\n\ts_barrier" ::: "memory")
#define BAR_VL(N) asm volatile("s_waitcnt vmcnt(%0) lgkmcnt(0)\n\ts_barrier" :: "i"(N) : "memory")
#define WAIT_LGKM0() do { __builtin_amdgcn_sched_barrier(0); __builtin_amdgcn_s_waitcnt(0xC07F); __builtin_amdgcn_sched_barrier(0); } while (0)
#define CBAR() do { WAIT_LGKM0(); asm volatile("" ::: "memory"); __builtin_amdgcn_s_barrier(); asm volatile("" ::: "memory"); __builtin_amdgcn_sched_barrier(0); } while (0)
#define SGB(mask, n) __builtin_amdgcn_sched_group_barrier(mask, n, 0)
template <int STEP, int MAXN> DI void bar_vl_n(int n) {
    if (n >= MAXN) { BAR_VL((MAXN * STEP > 63 ? 63 : MAXN * STEP)); }
    else if constexpr (MAXN > 0) bar_vl_n<STEP, MAXN - 1>(n);
}
template <int N> struct IC { static constexpr int v = N; };
template <int K, int END, class F> DI void static_for(F&& f) { if constexpr (K < END) { f(IC<K>{}); static_for<K + 1, END>(f); } }
template <int R, int NST, int NP, int LAG, bool PROLOGUE, int NEND = NST, class FI, class FP, class FQ>
DI void ring_loader(FI&& issue, FP&& pre, FQ&& post) {
    constexpr int L0 = (R < NST ? R : NST) - 1;
    if (PROLOGUE) {
#pragma unroll 1
        for (int k = 0; k <= L0; ++k) issue();
    }
    static_for<0, LAG + 1>([&](auto kc) { constexpr int k = decltype(kc)::v; BAR_VL((L0 - k) * NP); pre(k); if constexpr (k >= LAG && k - LAG + R < NST) issue(); post(k); });
    constexpr int KS = NST - R + LAG;
    if constexpr (KS > LAG + 1) {
#pragma unroll 1
        for (int k = LAG + 1; k < KS; ++k) { BAR_VL((R - 1 - LAG) * NP); pre(k); issue(); post(k); }
    }
    static_for<(KS > LAG + 1 ? KS : LAG + 1), NEND>([&](auto kc) { constexpr int k = decltype(kc)::v; BAR_VL((NST - 1 - k) * NP); pre(k); post(k); });
}
DI bf16x8 ld128(lcptr p) { return *(const LAS bf16x8*)p; }
DI s16x4 ld64(lcptr p) { return *(const LAS s16x4*)p; }
DI s16x4 ld64v(lcptr p) { return *(const volatile LAS s16x4*)p; }
DI s16x4 ldtr(lcptr p) { typedef short v4 __attribute__((ext_vector_type(4))); return __builtin_bit_cast(s16x4, __builtin_amdgcn_ds_read_tr16_b64_v4i16((LAS v4*)p)); }
DI bf16x8 cat8(s16x4 a, s16x4 b) { return __builtin_shufflevector(a, b, 0, 1, 2, 3, 4, 5, 6, 7); }
#define MFMA16(a, b, c) __builtin_amdgcn_mfma_f32_16x16x32_bf16(a, b, c, 0, 0, 0)
#define MFMA32(a, b, c) __builtin_amdgcn_mfma_f32_32x32x16_bf16(a, b, c, 0, 0, 0)
#define MFMA32K8(a, b, c) __builtin_amdgcn_mfma_f32_32x32x8bf16_1k(a, b, c, 0, 0, 0)
template <int CTRL> DI float dpp_add(float v) { return v + __int_as_float(__builtin_amdgcn_update_dpp(0, __float_as_int(v), CTRL, 0xf, 0xf, true)); }
DI float sum8(float v) { v = dpp_add<0xB1>(v); v = dpp_add<0x4E>(v); return dpp_add<0x141>(v); }
DI float sum_x16(float v) { auto r = __builtin_amdgcn_permlane16_swap(__float_as_uint(v), __float_as_uint(v), false, false); return __uint_as_float(r[0]) + __uint_as_float(r[1]); }
DI float sum_x32(float v) { auto r = __builtin_amdgcn_permlane32_swap(__float_as_uint(v), __float_as_uint(v), false, false); return __uint_as_float(r[0]) + __uint_as_float(r[1]); }
DI bf16x8 ones8() { bf16x8 o = {0x3f80, 0x3f80, 0x3f80, 0x3f80, 0x3f80, 0x3f80, 0x3f80, 0x3f80}; asm volatile("" : "+v"(o)); return o; }

DI f32x2 ln_scalars(float sum, float sq) {
    const float mean = sum * (1.0f / D_);
    float var = sq * (1.0f / D_) - mean * mean; var = var > 0.f ? var : 0.f;
    const float sd = rbf(__builtin_sqrtf(rbf(rbf(var) + 1e-5f)));
    return (f32x2){rbf(mean), 1.0f / sd};
}
template <bool FUSED> DI void publish(const Params& p, size_t flag_off, int wid, int lane, u64 tag, int sidx = 47) {
    if (FUSED) { BAR_VL(0); STAMP(sidx); if (wid == 0 && lane == 0) st_dev(p.ws + WS_FLAG + flag_off, tag); }
}

namespace rp { constexpr int L_G = 132608, L_U = L_G + 10752, L_O = L_G + 21504, L_D = 102400; }
DI void rp1_addr(const Params& p, int lane, int lw, int ti, int tj, const char* (&ga)[7]) {
    const int ns = ti < 8 ? 3 : 2, s0 = ti < 8 ? 3 * ti : 24 + 2 * (ti - 8);
    const int ct = tj >> 1, z = 2 * ti + (tj & 1);
#pragma unroll
    for (int i = 0; i < 7; ++i) { const int n = lw + 4 * i;
        if (n < 21) { int u = 64 * n + lane; const int mat = u >= 672; u -= 672 * mat; int row = u / 14; int col = 112 * tj + 8 * (u - 14 * row); col = col > F_ - 8 ? F_ - 8 : col;
            row = row < 16 * ns ? row : 16 * ns - 1;
            ga[i] = (const char*)((mat ? p.wu : p.wg) + (size_t)(16 * s0 + row) * F_ + col);
        } else { const int m = n < 26 ? n - 21 : 4, u = 64 * m + lane, row = u / 10, c = u - 10 * row;
            ga[i] = (const char*)(p.wo + (size_t)(32 * (z < 20 ? z : 0) + row) * D_ + 80 * ct + 8 * c); } }
}
DI void glds16v(const void* vaddr, unsigned ldsaddr) {
    asm volatile("s_nop 0\n\ts_mov_b32 m0, %1\n\ts_nop 0\n\tglobal_load_lds_dwordx4 %0, off" :: "v"(vaddr), "s"(ldsaddr) : "memory");
}
DI void rp1_issue(const char* (&ga)[7], unsigned lds0, int lw) {
#pragma unroll
    for (int i = 0; i < 7; ++i) { const int n = lw + 4 * i; glds16v(ga[i], (unsigned)__builtin_amdgcn_readfirstlane(lds0 + rp::L_G + (n < 26 ? n : 25) * 1024)); }
}
template <bool FUSED>
DI void rp1_store(const Params& p, lptr L, int lane, int lw, int ti, int tj, int part = 3) {
    const int ns = ti < 8 ? 3 : 2, s0 = ti < 8 ? 3 * ti : 24 + 2 * (ti - 8);
    const int ct = tj >> 1, z = 2 * ti + (tj & 1);
    const int h = lane >> 5, blk = (lane >> 4) & 1, g = lane >> 4, q = (lane & 15) >> 2, pp = lane & 3;
    const bf16x8 ones = ones8();
#pragma unroll 1
    for (int t = lw + (part == 1 ? 4 : 0); t < (part == 0 ? 4 : part == 2 ? 0 : 7); t += 4) {
        lcptr src = (lcptr)L + (t < 6 ? ((t & 1) ? rp::L_U : rp::L_G) + (32 * (t >> 1) + 16 * blk + 4 * pp) * 2 : (blk ? rp::L_U : rp::L_G) + (96 + 4 * pp) * 2) + (8 * h + q) * 224;
        s16x4 f[3][2];
#pragma unroll
        for (int s = 0; s < 3; ++s) { f[s][0] = ldtr(src + s * 16 * 224); f[s][1] = ldtr(src + s * 16 * 224 + 4 * 224); }
        f32x16 cs = {};
        char* dst = p.ws + WS_PWG + ((size_t)(tj * 40 + s0) * 7 + t) * 1024 + lane * 16;
#pragma unroll
        for (int s = 0; s < 3; ++s) if (s < ns) { const bf16x8 fr = cat8(f[s][0], f[s][1]); st16<FUSED>(dst + s * 7168, __builtin_bit_cast(u32x4, fr)); cs = MFMA32(fr, ones, cs); }
        if ((lane & 31) == 0) { float* cd = (float*)(p.ws + WS_CSP) + (ti * 16 + tj) * 224 + 32 * t + 4 * h;
#pragma unroll
            for (int u = 0; u < 4; ++u) st16<FUSED>(cd + 8 * u, __builtin_bit_cast(u32x4, ((f32x4){cs[4 * u], cs[4 * u + 1], cs[4 * u + 2], cs[4 * u + 3]}))); }
    }
    if (z < 20 && part >= 2) {
#pragma unroll 1
        for (int nt = 3 - lw; nt < 5; nt += 4) {
            lcptr src = (lcptr)L + rp::L_O + (8 * g + q) * 160 + (16 * nt + 4 * pp) * 2;
            st16<FUSED>(p.ws + WS_PWO + ((size_t)(ct * 20 + z) * 5 + nt) * 1024 + lane * 16, __builtin_bit_cast(u32x4, cat8(ldtr(src), ldtr(src + 4 * 160)))); }
    }
}
DI void rp2_issue(const Params& p, unsigned lds0, int lane, int wid, int ti, int tj) {
    const int ct = tj >> 1, z = 2 * ti + (tj & 1), np = z + 32 < 54 ? 10 : 5;
#pragma unroll 1
    for (int n = wid; n < np; n += 8) { const int im = n >= 5, u = 64 * (n - 5 * im) + lane, row = u / 10, c = u - 10 * row;
        glds16<false>((unsigned)((32 * (z + 32 * im) + row) * D_ + 80 * ct + 8 * c) * 2u, p.wd, lds0 + rp::L_D + n * 1024); }
}
template <bool FUSED>
DI void rp2_store(const Params& p, lptr L, int lane, int wid, int ti, int tj) {
    const int ct = tj >> 1, z = 2 * ti + (tj & 1), np = z + 32 < 54 ? 10 : 5;
    const int g = lane >> 4, q = (lane & 15) >> 2, pp = lane & 3;
#pragma unroll 1
    for (int n = wid; n < np; n += 8) { const int im = n >= 5, nt = n - 5 * im;
        lcptr src = (lcptr)L + rp::L_D + im * 5120 + (8 * g + q) * 160 + (16 * nt + 4 * pp) * 2;
        st16<FUSED>(p.ws + WS_PWD + ((size_t)(ct * 54 + z + 32 * im) * 5 + nt) * 1024 + lane * 16, __builtin_bit_cast(u32x4, cat8(ldtr(src), ldtr(src + 4 * 160)))); }
}
DI u64 make_tag(u32x2& ev) {
    asm volatile("" : "+v"(ev));
    const u64 e = (((u64)(unsigned)__builtin_amdgcn_readfirstlane(ev[1]) << 32) | (unsigned)__builtin_amdgcn_readfirstlane(ev[0])) + 1;
    return e;
}

namespace pa { constexpr int R = 4, NST = 10, LA = 0, LB = R * 16384, L_STAT = 2 * R * 16384, L_CS = L_STAT + 1024; }
template <bool FUSED>
DI void phase_a_stats(const Params& p, lptr L, int lane, int wid, int ti, u32x2& ev, u64& tag) {
    const int r = lane >> 3, c = lane & 7;
    if (wid >= 4) { const char* ga[7]; rp1_addr(p, lane, wid - 4, ti, 15, ga); rp1_issue(ga, (unsigned)(uintptr_t)L, wid - 4); }
#pragma unroll
    for (int hlf = 0; hlf < 2; ++hlf) {
        const int row = 128 * ti + 16 * wid + 8 * hlf + r;
        const u32x4* src = (const u32x4*)(p.x + (size_t)row * D_) + c;
        u32x4 v[10];
#pragma unroll
        for (int i = 0; i < 10; ++i) v[i] = src[8 * i];
        float a = 0.f, q = 0.f;
#pragma unroll
        for (int i = 0; i < 10; ++i)
#pragma unroll
            for (int e = 0; e < 4; ++e) { const float lo = bflo(v[i][e]), hi = bfhi(v[i][e]); a += lo + hi; q += lo * lo + hi * hi; }
        const f32x2 st = ln_scalars(sum8(a), sum8(q));
        if (c == 0) { const u64 w = ((u64)__float_as_uint(st.y) << 32) | __float_as_uint(st.x); char* dst = p.ws + WS_S1 + (size_t)row * 8; if (FUSED) st_dev(dst, w); else *(u64*)dst = w; }
    }
    BAR_VL(0);
    if (FUSED) { tag = make_tag(ev); if (wid == 0 && lane == 0) st_dev(p.ws + WS_FLAG + FL_A + (ti * 16 + 15) * FS, tag); }
    if (wid >= 4) rp1_store<FUSED>(p, L, lane, wid - 4, ti, 15);
    if (FUSED) { BAR_VL(0); if (wid == 0 && lane == 0) st_dev(p.ws + WS_FLAG + FL_P + (ti * 16 + 15) * FS, tag); }
}
template <bool FUSED>
DI void phase_a(const Params& p, lptr L, int lane, int wid, int ti, int tj, int rot, u32x2& ev, u64& tag) {
    using namespace pa;
    const unsigned lds0 = (unsigned)(uintptr_t)L;
    if (wid >= 4) {
        const int lw = wid - 4, r = lane >> 3, c = lane & 7;
        const bool isA = lw < 2;
        unsigned vo[2];
#pragma unroll
        for (int e = 0; e < 2; ++e) vo[e] = isA ? (unsigned)(r * D_ + ((c ^ ((4 * e + (r >> 1)) & 7)) * 8)) * 2u : (unsigned)(r * 3 * D_ + ((c ^ (4 * ((r >> 1) & 1) + 2 * e)) * 8)) * 2u;
        unsigned vp[8];
#pragma unroll
        for (int i = 0; i < 8; ++i) vp[i] = vo[i & 1] + (isA ? (unsigned)i * (8 * D_ * 2 - 1024) : (unsigned)(i & 3) * (8 * 3 * D_ * 2 - 1024));
        const char* gb = isA ? (const char*)p.x + (size_t)(128 * ti + 64 * lw) * D_ * 2 + 4096 + rot * 128 : (const char*)p.wqkv + ((size_t)(32 * (lw - 2) + 64 * rot) * 3 * D_ + 128 * tj) * 2 + 2048;
        const int gstep = isA ? 128 : 64 * 3 * D_ * 2;
        int ks = rot; unsigned sl = lds0 + (isA ? LA + lw * 8192 + 4096 : LB + (lw - 2) * 4096 + 2048), sslot = 0;
        auto issue = [&]() {
            if (isA) gl_str8(vp, gb, sl + sslot); else { gl_str4<false>(vp, gb, sl + sslot); gl_str4<false>(vp, gb + 128, sl + sslot + 8192); }
            gb += gstep; if (++ks == NST) { ks = 0; gb -= (size_t)NST * gstep; }
            sslot = (sslot + 16384) & (R * 16384 - 1);
        };
        const int g = lane >> 4, q = (lane & 15) >> 2, pp = lane & 3;
        unsigned aC[2];
#pragma unroll
        for (int t = 0; t < 2; ++t) aC[t] = (unsigned)(LB + ((lw >> 1) * 8 + g) * 1024 + q * 128 + (((2 * (2 * (lw & 1) + t) + (pp >> 1)) ^ (4 * ((q >> 1) & 1) + 2 * (g & 1))) * 16) + (pp & 1) * 8);
        f32x4 cs[2] = {{0.f, 0.f, 0.f, 0.f}, {0.f, 0.f, 0.f, 0.f}};
        const bf16x8 ones = ones8();
        s16x4 cf[2][2][2];
        __builtin_amdgcn_s_setprio(3);
        auto pre = [&](int s) { lcptr q0 = (lcptr)L + (s & 3) * 16384;
#pragma unroll
            for (int ks2 = 0; ks2 < 2; ++ks2)
#pragma unroll
                for (int t = 0; t < 2; ++t) { cf[ks2][t][0] = ldtr(q0 + aC[t] + ks2 * 4096); cf[ks2][t][1] = ldtr(q0 + aC[t] + ks2 * 4096 + 512); } };
        auto post = [&](int) {
#pragma unroll
            for (int ks2 = 0; ks2 < 2; ++ks2)
#pragma unroll
                for (int t = 0; t < 2; ++t) cs[t] = MFMA16(cat8(cf[ks2][t][0], cf[ks2][t][1]), ones, cs[t]); };
        issue(); issue();
        __builtin_amdgcn_sched_barrier(0);
        const char* ga[7]; rp1_addr(p, lane, lw, ti, tj, ga);
        BAR_VL(8); pre(0); issue(); rp1_issue(ga, lds0, lw);
        if (FUSED) tag = make_tag(ev);
        BAR_VL(15); post(0); pre(1); issue();
        BAR_VL(15); post(1); pre(2); issue();
        BAR_VL(8); post(2); pre(3); issue(); rp1_store<FUSED>(p, L, lane, lw, ti, tj, 0);
        BAR_VL(14); post(3); pre(4); issue(); rp1_store<FUSED>(p, L, lane, lw, ti, tj, 1);
        if (lw < 3) BAR_VL(20); else BAR_VL(14);
        post(4); pre(5); issue(); rp1_store<FUSED>(p, L, lane, lw, ti, tj, 2);
#pragma unroll 1
        for (int k = 6; k < NST - 2; ++k) { BAR_VL(8); post(k - 1); pre(k); issue(); }
        BAR_VL(8); post(NST - 3); pre(NST - 2);
        BAR_VL(0); post(NST - 2); pre(NST - 1); post(NST - 1);
        if (isA) {
            if (FUSED) poll_flags(p.ws + WS_FLAG + FL_A + (ti * 16 + 15) * FS, lane == 0, tag);
            const char* src = p.ws + WS_S1 + (size_t)(128 * ti + 64 * lw + lane) * 8;
            const u64 w = FUSED ? ld_dev(src) : *(const u64*)src;
            *(LAS u64*)(L + L_STAT + (64 * lw + lane) * 8) = w;
        }
        __builtin_amdgcn_s_setprio(0);
        if ((lane & 15) == 0) {
#pragma unroll
            for (int t = 0; t < 2; ++t) *(LAS f32x4*)(L + L_CS + (32 * lw + 16 * t + 4 * g) * 4) = cs[t];
        }
        BAR_L();
        BAR_L();
    } else {
        const int wm = wid >> 1, wn = wid & 1, r32 = lane & 31, h = lane >> 5, blk = (lane >> 4) & 1, q = (lane & 15) >> 2, pp = lane & 3;
        unsigned aX[4], aW[2];
        { const int row = 64 * wm + r32;
#pragma unroll
          for (int u = 0; u < 4; ++u) aX[u] = (unsigned)(LA + (row >> 3) * 1024 + (row & 7) * 128 + (((2 * u + h) ^ ((row >> 1) & 7)) * 16));
#pragma unroll
          for (int nt = 0; nt < 2; ++nt) aW[nt] = (unsigned)(LB + (wn * 8 + h) * 1024 + q * 128 + (((4 * nt + 2 * blk + (pp >> 1)) ^ (4 * ((q >> 1) & 1) + 2 * h)) * 16) + (pp & 1) * 8); }
        f32x16 acc[2][2];
#pragma unroll
        for (int i = 0; i < 16; ++i) { acc[0][0][i] = 0.f; acc[0][1][i] = 0.f; acc[1][0][i] = 0.f; acc[1][1][i] = 0.f; }
        bf16x8 X[4][2], W[4][2];
        auto rd = [&](int slot, int u) {
            lcptr sl = (lcptr)L + slot * 16384;
#pragma unroll
            for (int nt = 0; nt < 2; ++nt) W[u][nt] = cat8(ldtr(sl + aW[nt] + u * 2048), ldtr(sl + aW[nt] + u * 2048 + 512));
#pragma unroll
            for (int mt = 0; mt < 2; ++mt) X[u][mt] = ld128(sl + aX[u] + mt * 4096);
        };
        auto mm = [&](int u) {
#pragma unroll
            for (int mt = 0; mt < 2; ++mt)
#pragma unroll
                for (int nt = 0; nt < 2; ++nt) acc[mt][nt] = MFMA32(W[u][nt], X[u][mt], acc[mt][nt]);
        };
        auto il = [&]() { SGB(0x8, 1); SGB(0x100, 2); SGB(0x8, 1); SGB(0x100, 2); SGB(0x8, 1); SGB(0x100, 2); SGB(0x8, 1); __builtin_amdgcn_sched_barrier(0); };
        auto bar = [&]() { __builtin_amdgcn_sched_barrier(0); asm volatile("" ::: "memory"); __builtin_amdgcn_s_barrier(); asm volatile("" ::: "memory"); __builtin_amdgcn_sched_barrier(0); };
        bar();
        if (FUSED) VMWAIT(0);
        STAMP(1); STAMPC(26);
        rd(0, 0); rd(0, 1);
#pragma unroll 1
        for (int s = 0; s < NST - 1; ++s) {
            const int c = s & 3, n = (s + 1) & 3;
            rd(c, 2); mm(0); il();
            rd(c, 3); mm(1); il();
            bar();
            rd(n, 0); mm(2); il();
            rd(n, 1); mm(3); il();
        }
        rd((NST - 1) & 3, 2); mm(0); il();
        rd((NST - 1) & 3, 3); mm(1); il();
        mm(2); mm(3);
        STAMP(2); STAMPC(27);
        if (FUSED) tag = make_tag(ev);
        BAR_L();
        STAMP(38);
        {
            f32x2 st[2]; float cv[2];
#pragma unroll
            for (int i = 0; i < 2; ++i) { st[i] = *(const LAS f32x2*)(L + L_STAT + (64 * wm + 32 * i + r32) * 8); cv[i] = *(const LAS float*)(L + L_CS + (64 * wn + 32 * i + r32) * 4); }
            bf16x8 cf[2], mf[2];
#pragma unroll
            for (int i = 0; i < 2; ++i) { const float hi = rbf(cv[i]); const unsigned c = h ? 0u : pk2(hi, cv[i] - hi), m = h ? 0u : pk2(-st[i].x, -st[i].x);
                cf[i] = __builtin_bit_cast(bf16x8, ((u32x4){c, 0u, 0u, 0u})); mf[i] = __builtin_bit_cast(bf16x8, ((u32x4){m, 0u, 0u, 0u})); }
#pragma unroll
            for (int mt = 0; mt < 2; ++mt)
#pragma unroll
                for (int nt = 0; nt < 2; ++nt) acc[mt][nt] = MFMA32(cf[nt], mf[mt], acc[mt][nt]);
#pragma unroll
            for (int mt = 0; mt < 2; ++mt) { const int tok = 64 * wm + 32 * mt + r32; const float sq = st[mt].y * C2_;
#pragma unroll
                for (int nt = 0; nt < 2; ++nt)
#pragma unroll
                    for (int u = 0; u < 4; ++u) { const int col = 64 * wn + 32 * nt + 8 * u + 4 * h;
                        const float sc = (128 * tj + col < D_) ? sq : st[mt].y;
                        *(LAS u32x2*)(L + (col >> 2) * 1024 + tok * 8) = (u32x2){pk2(acc[mt][nt][4 * u] * sc, acc[mt][nt][4 * u + 1] * sc), pk2(acc[mt][nt][4 * u + 2] * sc, acc[mt][nt][4 * u + 3] * sc)}; } }
        }
        BAR_L();
        STAMP(39);
    }
    char* dst = p.ws + WS_QKV + (size_t)(32 * tj) * 16384 + (size_t)ti * 1024;
#pragma unroll
    for (int i = 0; i < 4; ++i) { const int gq = 4 * wid + i; st16<FUSED>(dst + (size_t)gq * 16384 + lane * 16, *(const LAS u32x4*)(L + gq * 1024 + lane * 16)); }
    STAMP(3);
    publish<FUSED>(p, FL_A + (ti * 16 + tj) * FS, wid, lane, tag, 42);
    if (FUSED && wid == 1 && lane == 0) st_dev(p.ws + WS_FLAG + FL_P + (ti * 16 + tj) * FS, tag);
}

namespace pb {
constexpr int GS = 3104;
constexpr int L_K = 0, L_V = 10 * GS, L_ONE = 20 * GS, L_Q = 21 * GS, L_O = 102400;
static_assert(L_Q + 10240 <= L_O, "LDS map");
}
#define ATT_STAMP() do {} while (0)
template <bool EDGE>
DI f32x16 attn_wave(lcptr L, int hh, int qb, int lane, int tstart, u64* stq) {
    using namespace pb;
    const int r = lane & 31, h = lane >> 5;
    bf16x8 q0; s16x4 q1;
    { lcptr qp = L + L_Q + hh * 5 * 1024 + (32 * qb + r) * 8;
      q0 = cat8(ld64(qp + (2 * h) * 1024), ld64(qp + (2 * h + 1) * 1024));
      const s16x4 t = ld64(qp + 4 * 1024); q1 = h ? (s16x4){0, 0, 0, 0} : t; }
    lcptr kp = L + L_K + hh * 5 * GS + (32 * qb + r) * 8;
    const f32x16 z16 = {};
    bf16x8 k0[9]; s16x4 k1[9];
#pragma unroll
    for (int t = 0; t < 9; ++t) { k0[t] = cat8(ld64v(kp + (2 * h) * GS + t * 256), ld64v(kp + (2 * h + 1) * GS + t * 256)); k1[t] = ld64v(kp + 4 * GS + t * 256); }
    float m;
    { f32x16 a = MFMA32K8(k1[8], q1, MFMA32(k0[8], q0, z16)), b = MFMA32K8(k1[0], q1, MFMA32(k0[0], q0, z16));
      const float bias = (!EDGE || tstart == 0) ? 0.f : -INFINITY;
#pragma unroll
      for (int i = 0; i < 16; ++i) { const int c = (i & 3) + 8 * (i >> 2) + 4 * h; a[i] = (c <= r) ? a[i] : (EDGE ? b[i] + bias : b[i]); }
      float x = __builtin_fmaxf(__builtin_fmaxf(a[0], a[1]), a[2]), y = __builtin_fmaxf(__builtin_fmaxf(a[3], a[4]), a[5]);
#pragma unroll
      for (int i = 6; i < 14; i += 4) { x = __builtin_fmaxf(__builtin_fmaxf(x, a[i]), a[i + 1]); y = __builtin_fmaxf(__builtin_fmaxf(y, a[i + 2]), a[i + 3]); }
      m = __builtin_fmaxf(__builtin_fmaxf(__builtin_fmaxf(x, a[14]), a[15]), y); }
#pragma unroll
    for (int t = 1; t < 8; ++t) { const f32x16 a = MFMA32K8(k1[t], q1, MFMA32(k0[t], q0, z16));
        float x = __builtin_fmaxf(__builtin_fmaxf(a[0], a[1]), a[2]), y = __builtin_fmaxf(__builtin_fmaxf(a[3], a[4]), a[5]);
#pragma unroll
        for (int i = 6; i < 14; i += 4) { x = __builtin_fmaxf(__builtin_fmaxf(x, a[i]), a[i + 1]); y = __builtin_fmaxf(__builtin_fmaxf(y, a[i + 2]), a[i + 3]); }
        x = __builtin_fmaxf(__builtin_fmaxf(x, a[14]), a[15]);
        if (EDGE) x = t >= tstart ? x : -INFINITY;
        m = __builtin_fmaxf(__builtin_fmaxf(m, x), (EDGE && t < tstart) ? -INFINITY : y); }
    { auto rr = __builtin_amdgcn_permlane32_swap(__float_as_uint(m), __float_as_uint(m), false, false); m = __builtin_fmaxf(__uint_as_float(rr[0]), __uint_as_float(rr[1])); }
    f32x16 nm16;
#pragma unroll
    for (int i = 0; i < 16; ++i) nm16[i] = -m;
    BAR_VL(0);
    ATT_STAMP();
    const int blk = (lane >> 4) & 1, vq = (lane & 15) >> 2, vp = lane & 3, dg = 4 * blk + vp;
    lcptr vpz = L + (dg < 5 ? L_V + (hh * 5 + dg) * GS : L_ONE) + (32 * qb + 4 * h + vq) * 8;
    f32x16 O = z16;
#pragma unroll
    for (int t = 1; t < 8; ++t) {
        const bf16x8 v0 = cat8(ldtr(vpz + t * 256), ldtr(vpz + t * 256 + 64)), v1 = cat8(ldtr(vpz + t * 256 + 128), ldtr(vpz + t * 256 + 192));
        f32x16 a = MFMA32K8(k1[t], q1, MFMA32(k0[t], q0, nm16));
        if (EDGE) { const float bias = t >= tstart ? 0.f : -INFINITY;
#pragma unroll
            for (int i = 0; i < 16; ++i) a[i] += bias; }
        unsigned P[8];
#pragma unroll
        for (int i = 0; i < 8; ++i) P[i] = pk2(__builtin_amdgcn_exp2f(a[2 * i]), __builtin_amdgcn_exp2f(a[2 * i + 1]));
        O = MFMA32(v0, __builtin_bit_cast(bf16x8, ((u32x4){P[0], P[1], P[2], P[3]})), O);
        O = MFMA32(v1, __builtin_bit_cast(bf16x8, ((u32x4){P[4], P[5], P[6], P[7]})), O);
    }
    { const bf16x8 v0 = cat8(ldtr(vpz + 8 * 256), ldtr(vpz + 8 * 256 + 64)), v1 = cat8(ldtr(vpz + 8 * 256 + 128), ldtr(vpz + 8 * 256 + 192));
      const bf16x8 w0 = cat8(ldtr(vpz), ldtr(vpz + 64)), w1 = cat8(ldtr(vpz + 128), ldtr(vpz + 192));
      f32x16 a = MFMA32K8(k1[8], q1, MFMA32(k0[8], q0, nm16)); const f32x16 b = MFMA32K8(k1[0], q1, MFMA32(k0[0], q0, nm16));
      const float bias = (!EDGE || tstart == 0) ? 0.f : -INFINITY;
#pragma unroll
      for (int i = 0; i < 16; ++i) { const int c = (i & 3) + 8 * (i >> 2) + 4 * h; a[i] = (c <= r) ? a[i] : (EDGE ? b[i] + bias : b[i]); }
      unsigned P[8], P0[8];
#pragma unroll
      for (int i = 0; i < 8; ++i) { P[i] = pk2(__builtin_amdgcn_exp2f(a[2 * i]), __builtin_amdgcn_exp2f(a[2 * i + 1]));
          const int c0 = ((2 * i) & 3) + 8 * ((2 * i) >> 2) + 4 * h; const unsigned mk = (c0 <= r ? 0xffffu : 0u) | (c0 + 1 <= r ? 0xffff0000u : 0u);
          P0[i] = P[i] & ~mk; P[i] &= mk; }
      O = MFMA32(v0, __builtin_bit_cast(bf16x8, ((u32x4){P[0], P[1], P[2], P[3]})), O);
      O = MFMA32(v1, __builtin_bit_cast(bf16x8, ((u32x4){P[4], P[5], P[6], P[7]})), O);
      O = MFMA32(w0, __builtin_bit_cast(bf16x8, ((u32x4){P0[0], P0[1], P0[2], P0[3]})), O);
      O = MFMA32(w1, __builtin_bit_cast(bf16x8, ((u32x4){P0[4], P0[5], P0[6], P0[7]})), O); }
    return O;
}
template <bool FUSED>
DI void phase_b(const Params& p, lptr L, int lane, int wid, int ti, int tj, u64 tag) {
    using namespace pb;
    const int tid = wid * 64 + lane;
    const unsigned lds0 = (unsigned)(uintptr_t)L;
    rp2_issue(p, lds0, lane, wid, ti, tj);
    if (ti < 2) {
        for (int n = tid; n < (2 - ti) * 20 * 64; n += 512) { const int pc = n >> 6, mb = pc % (2 - ti), gq = pc / (2 - ti);
            *(LAS u32x4*)(L + gq * GS + mb * 1024 + (n & 63) * 16) = (u32x4){0u, 0u, 0u, 0u}; }
    }
    if (tid < 384) *(LAS u32x2*)(L + L_ONE + tid * 8) = (u32x2){0x00003f80u, 0u};
    const int sub = wid & 3, mtile = sub < 3 ? ti - 2 + sub : ti;
    const char* g0 = p.ws + WS_QKV + (size_t)((sub < 3 ? 160 : 0) + 5 * (2 * tj + (wid >> 2))) * 16384 + (size_t)(mtile < 0 ? 0 : mtile) * 1024;
    const unsigned l0 = lds0 + (sub < 3 ? L_K + (wid >> 2) * 5 * GS + sub * 1024 : L_Q + (wid >> 2) * 5120);
    BAR_VL(0);
    rp2_store<FUSED>(p, L, lane, wid, ti, tj);
    if (FUSED) {
        if (wid == 7) { const int mb = lane / 6, sec = (lane % 6) >> 1, e = lane & 1, mt2 = ti - 2 + mb;
            const bool need = lane < 18 && mt2 >= 0 && (sec > 0 || mb == 2);
            poll_flags(p.ws + WS_FLAG + FL_A + ((mt2 < 0 ? 0 : mt2) * 16 + ((640 * sec + 40 * tj + 39 * e) >> 7)) * FS, need, tag); STAMPW(35, 7); }
    }
    BAR_L();
    STAMP(4);
    if (sub == 3) {
#pragma unroll
        for (int d = 0; d < 5; ++d) glds16<FUSED>(lane * 16, g0 + d * 16384, l0 + d * 1024);
        BAR_VL(0);
    } else if (mtile >= 0) {
#pragma unroll
        for (int d = 0; d < 5; ++d) glds16<FUSED>(lane * 16, g0 + d * 16384, l0 + d * GS);
#pragma unroll
        for (int d = 0; d < 5; ++d) glds16<FUSED>(lane * 16, g0 + (160 + d) * 16384, l0 + L_V + d * GS);
        BAR_VL(5);
    } else BAR_L();
    STAMP(5);
    const int hh = wid >> 2, qb = wid & 3, r = lane & 31, h = lane >> 5;
    const int q0g = 128 * ti + 32 * qb;
    f32x16 O;
    u64* stq = nullptr;
    if (ti >= 2) O = attn_wave<false>((lcptr)L, hh, qb, lane, 0, stq); else O = attn_wave<true>((lcptr)L, hh, qb, lane, q0g >= 256 ? 0 : 8 - (q0g >> 5), stq);
    { auto rr = __builtin_amdgcn_permlane32_swap(__float_as_uint(O[8]), __float_as_uint(O[8]), false, false);
      const float il = 1.0f / __uint_as_float(rr[1]);
      const int row = 32 * qb + r;
#pragma unroll
      for (int u = 0; u < 3; ++u) { const int dgp = 2 * u + h; if (dgp < 5) { const int cgp = 5 * hh + dgp;
          *(LAS u32x2*)(L + L_O + (cgp >> 1) * 2048 + row * 16 + (cgp & 1) * 8) = (u32x2){pk2(O[4 * u] * il, O[4 * u + 1] * il), pk2(O[4 * u + 2] * il, O[4 * u + 3] * il)}; } } }
    STAMP(6); STAMPW(37, 7);
    BAR_L();
    for (int n = wid; n < 10; n += 8)
        st16<FUSED>(p.ws + WS_O + (size_t)(5 * tj + (n >> 1)) * 32768 + (size_t)(128 * ti + 64 * (n & 1)) * 16 + lane * 16, *(const LAS u32x4*)(L + L_O + n * 1024 + lane * 16));
    STAMP(7);
    publish<FUSED>(p, FL_B + (ti * 16 + tj) * FS, wid, lane, tag);
}

template <int MODE> struct PC;
template <> struct PC<0> { static constexpr int NST = 10, RB = 10, RA = 6, LA = 102400, L_RES = 151552, L_X = 102400, L_STG = 122880; };
template <> struct PC<1> { static constexpr int NST = 27, RB = 8, RA = 8, LA = 81920, L_RES = 147456, L_X = 81920, L_STG = 102400; };
template <bool FUSED, int MODE>
DI void ce_prefetch(const Params& p, lptr L, int lane, int wid, int tj) {
    const char* wb = p.ws + (MODE ? WS_PWD + (size_t)(tj >> 1) * 54 * 5120 : WS_PWO + (size_t)(tj >> 1) * 20 * 5120) + (wid - 6) * 5120 + 2048;
    const unsigned l = (unsigned)(uintptr_t)L + (wid - 6) * 5120 + 2048;
#pragma unroll 1
    for (int s = 0; s < PC<MODE>::RB; ++s) gl_lin5<FUSED>(lane * 16, wb + (size_t)s * 10240, l + s * 10240);
}
template <bool FUSED, int MODE>
DI void phase_ce(const Params& p, lptr L, int lane, int wid, int ti, int tj, u64 tag) {
    typedef PC<MODE> C;
    constexpr int NST = C::NST, RA = C::RA, RB = C::RB;
    const unsigned lds0 = (unsigned)(uintptr_t)L;
    const int rh = tj & 1, ct = tj >> 1, r0 = 128 * ti + 64 * rh;
    const int wm = (wid >> 1) & 1, wk = wid & 1, r16 = lane & 15, g = lane >> 4;
    if (wid >= 6) {
        if (MODE == 0) {
            bar_vl_n<5, RB - 1>(RB - 1);
            BAR_VL(0);
#pragma unroll 1
            for (int s = 2; s < NST; ++s) BAR_L();
        } else {
            const char* wb = p.ws + WS_PWD + (size_t)ct * 54 * 5120 + (wid - 6) * 5120 + 2048 + (size_t)RB * 10240;
            const unsigned l0 = lds0 + (wid - 6) * 5120 + 2048; unsigned l = l0;
            ring_loader<RB, NST, 5, 2, false>([&]() { gl_lin5<FUSED>(lane * 16, wb, l); wb += 10240; l = l == l0 + (RB - 1) * 10240 ? l0 : l + 10240; }, [&](int) {}, [&](int) {});
        }
        BAR_L(); BAR_L();
    } else if (wid >= 4) {
        const int la = wid - 4;
        const unsigned voA = lane * 16;
#pragma unroll
        for (int i = 0; i < 5; ++i) { const int pc = 5 * la + i;
            if (MODE == 0) { const int n = 64 * pc + lane; glds16<false>((unsigned)((n / 10) * D_ * 2 + (n % 10) * 16), (const char*)p.x + (size_t)r0 * D_ * 2 + ct * 160, lds0 + C::L_RES + pc * 1024); }
            else glds16<FUSED>(voA, p.ws + WS_H + (size_t)(10 * ct + pc) * 32768 + (size_t)r0 * 16, lds0 + C::L_RES + pc * 1024); }
        if (FUSED) poll_flags(p.ws + WS_FLAG + (MODE ? FL_D : FL_B) + (ti * 16 + (lane & 15)) * FS, lane < 16, tag);
        STAMPW(MODE ? 17 : 9, 4);
        const char* ab = p.ws + (MODE ? WS_HH : WS_O) + (size_t)r0 * 16 + (size_t)(4 * la) * 32768 + 2048;
        unsigned vp[4];
#pragma unroll
        for (int i = 0; i < 4; ++i) vp[i] = voA + (unsigned)i * (32768 - 1024);
        const unsigned l0 = lds0 + C::LA + la * 4096 + 2048; unsigned l = l0;
        ring_loader<RA, NST, 4, 2, true>([&]() { gl_str4<FUSED>(vp, ab, l); ab += 8 * 32768; l = l == l0 + (RA - 1) * 8192 ? l0 : l + 8192; }, [&](int) {}, [&](int) {});
        BAR_L(); BAR_L();
    } else {
        const unsigned aA = (unsigned)(C::LA + (4 * wk + g) * 1024 + (32 * wm + r16) * 16), aB = (unsigned)(wk * 5120 + lane * 16);
        f32x4 acc[2][5];
#pragma unroll
        for (int i = 0; i < 2; ++i)
#pragma unroll
            for (int n = 0; n < 5; ++n) acc[i][n] = (f32x4){0.f, 0.f, 0.f, 0.f};
        bf16x8 Af[3][2], Bf[3][5];
        const int kofs = wk * 256;
        int sa = 0, sb = 0;
        auto rd = [&](int b) {
            lcptr la = (lcptr)L + aA + sa * 8192; lcptr lb = (lcptr)L + aB + sb * 10240;
            Af[b][0] = ld128(la + kofs); Af[b][1] = ld128(la + (256 - kofs));
#pragma unroll
            for (int n = 0; n < 5; ++n) Bf[b][n] = ld128(lb + n * 1024);
            sa = sa == RA - 1 ? 0 : sa + 1; sb = sb == RB - 1 ? 0 : sb + 1;
        };
        auto mm = [&](int b) {
#pragma unroll
            for (int n = 0; n < 5; ++n)
#pragma unroll
                for (int i = 0; i < 2; ++i) acc[i][n] = MFMA16(Bf[b][n], Af[b][i], acc[i][n]);
        };
        auto bar = [&]() { __builtin_amdgcn_sched_barrier(0); __builtin_amdgcn_s_waitcnt(0xC77F); asm volatile("" ::: "memory"); __builtin_amdgcn_s_barrier(); asm volatile("" ::: "memory"); __builtin_amdgcn_sched_barrier(0); };
        auto il = [&]() { SGB(0x8, 1); SGB(0x100, 2); SGB(0x8, 1); SGB(0x100, 2); SGB(0x8, 1); SGB(0x100, 2); SGB(0x8, 1); SGB(0x100, 1); SGB(0x8, 6); __builtin_amdgcn_sched_barrier(0); };
        bar(); STAMP(MODE ? 18 : 10); if (MODE) STAMPC(28); rd(0);
        bar(); rd(1);
        int s = 0;
#pragma unroll 1
        for (; s + 4 < NST; s += 3) {
            bar(); rd(2); mm(0); il();
            bar(); rd(0); mm(1); il();
            bar(); rd(1); mm(2); il();
        }
        if (NST % 3 == 0) { bar(); rd(2); mm(0); il(); mm(1); mm(2); }
        else { bar(); rd(2); mm(0); il(); bar(); rd(0); mm(1); il(); mm(2); mm(0); }
        STAMP(MODE ? 19 : 11); if (MODE) STAMPC(29);
        BAR_L();
        STAMP(MODE ? 46 : 45);
#pragma unroll
        for (int n = 0; n < 5; ++n) *(LAS f32x4*)(L + C::L_X + (wid * 5 + n) * 1024 + lane * 16) = acc[1][n];
        BAR_L();
        STAMP(MODE ? 31 : 30);
        const int row = 32 * wm + 16 * wk + r16;
        f32x4 pv[5]; u32x2 rr[5];
#pragma unroll
        for (int n = 0; n < 5; ++n) { pv[n] = *(const LAS f32x4*)(L + C::L_X + ((wid ^ 1) * 5 + n) * 1024 + lane * 16);
            rr[n] = MODE == 0 ? *(const LAS u32x2*)(L + C::L_RES + row * 160 + n * 32 + g * 8) : *(const LAS u32x2*)(L + C::L_RES + (2 * n + (g >> 1)) * 1024 + row * 16 + (g & 1) * 8); }
        u32x2 o[5]; f32x2 ps2 = {0.f, 0.f}, pq2 = {0.f, 0.f};
#pragma unroll
        for (int n = 0; n < 5; ++n) { const f32x4 v = acc[0][n] + pv[n];
            const f32x2 a = (f32x2){v[0], v[1]} + (f32x2){bflo(rr[n][0]), bfhi(rr[n][0])}, b = (f32x2){v[2], v[3]} + (f32x2){bflo(rr[n][1]), bfhi(rr[n][1])};
            o[n] = (u32x2){pk2(a[0], a[1]), pk2(b[0], b[1])};
            if (MODE == 0) { ps2 += a + b; pq2 += a * a + b * b; } }
        float ps = ps2[0] + ps2[1], pq = pq2[0] + pq2[1];
#pragma unroll
        for (int n = 0; n < 5; n += 2) { const int n1 = n + 1 < 5 ? n + 1 : n;
            auto s0 = __builtin_amdgcn_permlane16_swap(o[n][0], o[n1][0], false, false); auto s1 = __builtin_amdgcn_permlane16_swap(o[n][1], o[n1][1], false, false);
            const u32x4 v = {s0[0], s1[0], s0[1], s1[1]};
            const int ch = 2 * (n + (g & 1)) + (g >> 1);
            if (n + 1 < 5 || !(g & 1)) {
                if (MODE == 0) st16<FUSED>(p.ws + WS_H + (size_t)(10 * ct + ch) * 32768 + (size_t)(r0 + row) * 16, v);
                else *(u32x4*)(p.out + (size_t)(r0 + row) * D_ * 2 + ct * 160 + ch * 16) = v; } }
        if (MODE == 0) { ps = sum_x32(sum_x16(ps)); pq = sum_x32(sum_x16(pq));
            if (g == 0) { char* dst = p.ws + WS_PS + ((size_t)(ti * 8 + ct) * 128 + 64 * rh + row) * 8; const u64 v = ((u64)__float_as_uint(pq) << 32) | __float_as_uint(ps);
                if (FUSED) st_dev(dst, v); else *(u64*)dst = v; } }
    }
    STAMP(MODE ? 20 : 12);
    if (MODE == 0) publish<FUSED>(p, FL_C + (ti * 16 + tj) * FS, wid, lane, tag, 43);
}

namespace pd { constexpr int RB = 3, RA = 4, SB = 28672, LA = RB * SB, NST = 10, L_STAT = LA + RA * 16384, L_CF = L_STAT + 1024  , L_STG = LA; }
template <bool FUSED>
DI void d_bissue(const char* wb, unsigned l, int lane) { gl_lin7<FUSED>(lane * 16, wb, l); gl_lin7<FUSED>(lane * 16, wb + 7168, l + 7168); }
template <bool FUSED>
DI void d_prefetch(const Params& p, lptr L, int lane, int wid, int tj) {
    const char* wb = p.ws + WS_PWG + (size_t)tj * 286720 + (wid - 6) * 14336 + 3072;
    const unsigned l = (unsigned)(uintptr_t)L + (wid - 6) * 14336 + 3072;
#pragma unroll 1
    for (int s = 0; s < pd::RB; ++s) d_bissue<FUSED>(wb + (size_t)s * pd::SB, l + s * pd::SB, lane);
}
template <bool FUSED>
DI void phase_d(const Params& p, lptr L, int lane, int wid, int ti, int tj, u64 tag) {
    using namespace pd;
    const unsigned lds0 = (unsigned)(uintptr_t)L;
    if (wid >= 6) {
        const int lb = wid - 6;
        u32x2 ps[8]; float cp[2][16];
        const int row = 64 * lb + lane;
        const char* wb = p.ws + WS_PWG + (size_t)tj * 286720 + lb * 14336 + 3072 + (size_t)RB * SB;
        const unsigned l0 = lds0 + lb * 14336 + 3072; unsigned l = l0;
        auto issue = [&]() { d_bissue<FUSED>(wb, l, lane); wb += SB; l = l == l0 + (RB - 1) * SB ? l0 : l + SB; };
        static_assert(RB == 3 && NST == 10, "the waits below");
        BAR_VL(28);
#pragma unroll
        for (int c = 0; c < 8; ++c) ld64_async(ps[c], p.ws + WS_PS + ((size_t)(ti * 8 + c) * 128 + row) * 8);
        BAR_VL(22); issue();
#pragma unroll
        for (int i = 0; i < 16; ++i) ld32_async(cp[0][i], (const float*)(p.ws + WS_CSP) + (i * 16 + tj) * 224 + row);
        BAR_VL(38); issue();
#pragma unroll
        for (int i = 0; i < 16; ++i) ld32_async(cp[1][i], (const float*)(p.ws + WS_CSP) + (i * 16 + tj) * 224 + (row + 128 < 224 ? row + 128 : 223));
        BAR_VL(46); issue();
        BAR_VL(30); issue();
        BAR_VL(14); issue();
        { float a = 0.f, q = 0.f;
#pragma unroll
          for (int c = 0; c < 8; ++c) { asm volatile("" : "+v"(ps[c])); a += __uint_as_float(ps[c][0]); q += __uint_as_float(ps[c][1]); }
          *(LAS f32x2*)(L + L_STAT + row * 8) = ln_scalars(a, q);
#pragma unroll
          for (int z = 0; z < 2; ++z) { float sm = 0.f;
#pragma unroll
              for (int i = 0; i < 16; ++i) { asm volatile("" : "+v"(cp[z][i])); sm += cp[z][i]; }
              const int c = row + 128 * z;
              if (c < 224) { const float hi = rbf(sm); *(LAS u32x4*)(L + L_CF + (c >> 5) * 1024 + (c & 31) * 16) = (u32x4){pk2(hi, sm - hi), 0u, 0u, 0u};
                  *(LAS u32x4*)(L + L_CF + (c >> 5) * 1024 + 512 + (c & 31) * 16) = (u32x4){0u, 0u, 0u, 0u}; } } }
        BAR_VL(14); issue();
        BAR_VL(14); issue();
        BAR_VL(14);
        if (FUSED) {
            poll_flags(p.ws + WS_FLAG + FL_B + ((lane & 15) * 16 + (tj & ~1) + ((lane >> 4) & 1)) * FS, lane < 32, tag);
#pragma unroll 1
            for (int i = 0; i < 3; ++i) { int ln = (lb * 3 + i) * 64 + lane; ln = ln < 270 ? ln : 269;
                warm64(p.ws + WS_PWD + (size_t)(tj >> 1) * 276480 + (size_t)(2 * (ti & 3) + (tj & 1)) * 34560, (unsigned)ln * 128u, lds0); }
            BAR_VL(3);
        } else BAR_VL(0);
        BAR_L();
    } else if (wid >= 4) {
        const int la = wid - 4;
        if (FUSED) poll_flags(p.ws + WS_FLAG + FL_C + (ti * 16 + (lane & 15)) * FS, lane < 16, tag);
        STAMPW(13, 4);
        const char* ab = p.ws + WS_H + (size_t)(128 * ti + 64 * la) * 16 + 4096;
        unsigned vp[4];
#pragma unroll
        for (int i = 0; i < 4; ++i) vp[i] = lane * 16 + (unsigned)i * (32768 - 2048);
        const unsigned l0 = lds0 + LA + la * 1024 + 4096; unsigned l = l0;
        ring_loader<RA, NST, 8, 1, true>([&]() { gl_str4w<FUSED>(vp, ab, l); gl_str4w<FUSED>(vp, ab + 4 * 32768, l + 8192); ab += 8 * 32768; l = l == l0 + (RA - 1) * 16384 ? l0 : l + 16384; }, [&](int) {}, [&](int) {});
        BAR_L();
    } else {
        const int r32 = lane & 31, h = lane >> 5;
        const unsigned aA = (unsigned)(LA + h * 2048 + (32 * wid + r32) * 16), aB = (unsigned)(lane * 16);
        f32x16 acc[7];
#pragma unroll
        for (int t = 0; t < 7; ++t)
#pragma unroll
            for (int i = 0; i < 16; ++i) acc[t][i] = 0.f;
        bf16x8 A0, B0[7], A1, B1[7];
        auto rd = [&](bf16x8& Af, bf16x8* Bf, int sa, int sb, int u) {
            lcptr lb = (lcptr)L + aB + sb * SB + u * 7168;
            Af = ld128((lcptr)L + aA + sa * 16384 + u * 4096);
#pragma unroll
            for (int t = 0; t < 7; ++t) Bf[t] = ld128(lb + t * 1024);
        };
        auto mm = [&](const bf16x8& Af, const bf16x8* Bf) {
#pragma unroll
            for (int t = 0; t < 7; ++t) acc[t] = MFMA32(Bf[t], Af, acc[t]);
        };
        auto il = [&](const bf16x8& Af, const bf16x8* Bf) { SGB(0x8, 1); SGB(0x100, 2); SGB(0x8, 1); SGB(0x100, 2); SGB(0x8, 1); SGB(0x100, 2); SGB(0x8, 1); SGB(0x100, 2); SGB(0x8, 3); __builtin_amdgcn_sched_barrier(0);
            asm volatile("" :: "v"(Af), "v"(Bf[0]), "v"(Bf[1]), "v"(Bf[2]), "v"(Bf[3]), "v"(Bf[4]), "v"(Bf[5]), "v"(Bf[6])); __builtin_amdgcn_sched_barrier(0); };
        CBAR();
        STAMP(14); STAMPC(24);
        rd(A0, B0, 0, 0, 0);
        int sa = 0, sb = 0;
#pragma unroll 1
        for (int s = 0; s < NST - 1; ++s) {
            rd(A1, B1, sa, sb, 1); mm(A0, B0); il(A0, B0);
            rd(A0, B0, sa, sb, 2); mm(A1, B1); il(A1, B1);
            rd(A1, B1, sa, sb, 3); mm(A0, B0); il(A0, B0);
            sa = sa == RA - 1 ? 0 : sa + 1; sb = sb == RB - 1 ? 0 : sb + 1;
            CBAR();
            rd(A0, B0, sa, sb, 0); mm(A1, B1); il(A1, B1);
        }
        rd(A1, B1, sa, sb, 1); mm(A0, B0); il(A0, B0);
        rd(A0, B0, sa, sb, 2); mm(A1, B1); il(A1, B1);
        rd(A1, B1, sa, sb, 3); mm(A0, B0); il(A0, B0);
        mm(A1, B1);
        STAMP(15); STAMPC(25);
        BAR_L();
        STAMP(40);
        const int tok = 32 * wid + r32;
        const f32x2 st = *(const LAS f32x2*)(L + L_STAT + tok * 8);
        const float r2 = st.y * st.y;
        {
            bf16x8 cf[7];
#pragma unroll
            for (int t = 0; t < 7; ++t) cf[t] = ld128((lcptr)L + L_CF + t * 1024 + lane * 16);
            const bf16x8 mf = __builtin_bit_cast(bf16x8, ((u32x4){h ? 0u : pk2(-st.x, -st.x), 0u, 0u, 0u}));
#pragma unroll
            for (int t = 0; t < 7; ++t) acc[t] = MFMA32(cf[t], mf, acc[t]);
        }
        const f32x2 r22 = {r2, r2};
        auto glu4 = [&](float g0, float g1, float g2, float g3, float u0, float u1, float u2, float u3) {
            const f32x2 ya = (f32x2){u0, u1} * r22, yb = (f32x2){u2, u3} * r22;
            const f32x2 oa = (f32x2){__builtin_fmaxf(g0, 0.f), __builtin_fmaxf(g1, 0.f)} * ya, ob = (f32x2){__builtin_fmaxf(g2, 0.f), __builtin_fmaxf(g3, 0.f)} * yb;
            return (u32x2){pk2(oa[0], oa[1]), pk2(ob[0], ob[1])}; };
        char* dst = p.ws + WS_HH + (size_t)(14 * tj + h) * 32768 + (size_t)(128 * ti + tok) * 16;
        const int cmax = 216 - 14 * tj - h;
        auto put = [&](int c, u32x2 x, u32x2 y) {
            auto s0 = __builtin_amdgcn_permlane32_swap(x[0], y[0], false, false); auto s1 = __builtin_amdgcn_permlane32_swap(x[1], y[1], false, false);
            if (c < cmax) st16<FUSED>(dst + (size_t)c * 32768, (u32x4){s0[0], s1[0], s0[1], s1[1]}); };
#pragma unroll
        for (int a = 0; a < 3; ++a)
#pragma unroll
            for (int u = 0; u < 4; u += 2)
                put(4 * a + u, glu4(acc[2 * a][4 * u], acc[2 * a][4 * u + 1], acc[2 * a][4 * u + 2], acc[2 * a][4 * u + 3], acc[2 * a + 1][4 * u], acc[2 * a + 1][4 * u + 1], acc[2 * a + 1][4 * u + 2], acc[2 * a + 1][4 * u + 3]),
                    glu4(acc[2 * a][4 * u + 4], acc[2 * a][4 * u + 5], acc[2 * a][4 * u + 6], acc[2 * a][4 * u + 7], acc[2 * a + 1][4 * u + 4], acc[2 * a + 1][4 * u + 5], acc[2 * a + 1][4 * u + 6], acc[2 * a + 1][4 * u + 7]));
        put(12, glu4(acc[6][0], acc[6][1], acc[6][2], acc[6][3], acc[6][8], acc[6][9], acc[6][10], acc[6][11]), glu4(acc[6][4], acc[6][5], acc[6][6], acc[6][7], acc[6][12], acc[6][13], acc[6][14], acc[6][15]));
        STAMP(41);
    }
    STAMP(16);
    publish<FUSED>(p, FL_D + (ti * 16 + tj) * FS, wid, lane, tag, 44);
}

#define ROTA 2
#define ROTC 0
#define ROTD 0
#define ROTE 1
#define PROLOG() \
    extern __shared__ __attribute__((aligned(16))) char smem[]; \
    lptr L = (lptr)smem; \
    const int tid = threadIdx.x, lane = tid & 63, wid = __builtin_amdgcn_readfirstlane(tid >> 6); \
    const int xcd = blockIdx.x & 7, idx = blockIdx.x >> 3; \
    const int ti = 4 * (xcd & 3) + (idx & 3), tj = 8 * (xcd >> 2) + (idx >> 2); \
    const int rot10 = ((idx >> 2) + 3 * (idx & 3)) % 10, rot27 = ((idx >> 2) * 3 + (idx & 3) * 7) % 27; \
    const int rotC = ROTC ? rot10 : 0, rotD = ROTD ? rot10 : 0, rotE = ROTE ? rot27 : 0, rotA = ROTA == 1 ? rot10 : ROTA == 2 ? (((idx >> 2) + (idx & 3)) & 3) : ROTA == 3 ? ((idx >> 2) & 1) * 5 : 0; \
    (void)rot10; (void)rot27; (void)tid; (void)rotA; (void)rotC; (void)rotD; (void)rotE

__global__ __launch_bounds__(512, 2) void k_fused(const Params p) {
    PROLOG();
    STAMP(0);
    u64 tag = 0; u32x2 ev;
    ld64_async(ev, p.ws + WS_FLAG + FL_E);
    if (wid < 4) {
        const char* pc = (const char*)(__builtin_amdgcn_s_getpc() & ~127ull);
        int ln = ((idx * 4 + wid) % 6) * 64 + lane; ln = ln < CODE_LINES ? ln : CODE_LINES - 1;
        warm64(pc, (unsigned)ln * 128u, (unsigned)(uintptr_t)L);
    }
    if (tj >= 15) phase_a_stats<true>(p, L, lane, wid, ti, ev, tag); else phase_a<true>(p, L, lane, wid, ti, tj, rotA, ev, tag);
    if (tid == 0) st_dev(p.ws + WS_FLAG + FL_START + blockIdx.x * 8, tag);
    phase_b<true>(p, L, lane, wid, ti, tj, tag);
    if (wid >= 6) {
        poll_flags(p.ws + WS_FLAG + FL_P + ((lane & 15) * 16 + (lane < 16 ? tj : (tj & ~1) + ((lane >> 4) & 1))) * FS, lane < 48, tag);
        ce_prefetch<true, 0>(p, L, lane, wid, tj);
    }
    STAMP(8);
    phase_ce<true, 0>(p, L, lane, wid, ti, tj, tag);
    if (wid >= 6) d_prefetch<true>(p, L, lane, wid, tj);
    if (blockIdx.x == 0 && wid == 5) {
        for (unsigned spins = 0; spins < (1u << 17); ++spins) {
            bool ok = true;
#pragma unroll
            for (int i = 0; i < 4; ++i) ok &= ld_dev(p.ws + WS_FLAG + FL_START + (lane * 4 + i) * 8) == tag;
            if (__all(ok)) break;
            __builtin_amdgcn_s_sleep(1);
        }
        if (lane == 0) st_dev(p.ws + WS_FLAG + FL_E, tag);
    }
    phase_d<true>(p, L, lane, wid, ti, tj, tag);
    if (wid >= 6) ce_prefetch<true, 1>(p, L, lane, wid, tj);
    phase_ce<true, 1>(p, L, lane, wid, ti, tj, tag);
    STAMP(21);
}

extern "C" void kernel_launch(void* const* d_in, const int* in_sizes, int n_in, void* d_out, int out_size, void* d_ws, size_t ws_size, hipStream_t stream) {
    Params p;
    p.x = (const u16*)d_in[0]; p.wqkv = (const u16*)d_in[1]; p.wo = (const u16*)d_in[2];
    p.wg = (const u16*)d_in[4]; p.wu = (const u16*)d_in[5]; p.wd = (const u16*)d_in[6];
    p.out = (char*)d_out; p.ws = (char*)d_ws;
    (void)hipFuncSetAttribute((const void*)k_fused, hipFuncAttributeMaxDynamicSharedMemorySize, LDS_BYTES);
    k_fused<<<256, 512, LDS_BYTES, stream>>>(p);
}
```

```cpp
#define TLB_WARM 11
#include <hip/hip_runtime.h>
#include <hip/hip_bf16.h>
#include <stdint.h>

#define LAS __attribute__((address_space(3)))
#define DI __device__ __forceinline__
typedef unsigned short u16;
typedef unsigned long long u64;
typedef short bf16x8 __attribute__((ext_vector_type(8)));
typedef short s16x4 __attribute__((ext_vector_type(4)));
typedef float f32x2 __attribute__((ext_vector_type(2)));
typedef float f32x4 __attribute__((ext_vector_type(4)));
typedef float f32x16 __attribute__((ext_vector_type(16)));
typedef unsigned u32x2 __attribute__((ext_vector_type(2)));
typedef unsigned u32x4 __attribute__((ext_vector_type(4)));
typedef __bf16 bf16x2_t __attribute__((ext_vector_type(2)));
typedef LAS char* lptr;
typedef LAS const char* lcptr;

static constexpr int S_ = 2048, D_ = 640, H_ = 32, DH_ = 20, F_ = 1728;
static constexpr size_t WS_FLAG = 0;
#define FSTR 128
static constexpr size_t FS = FSTR;
static constexpr size_t FL_E = 0, FL_START = 1024, FL_A = 4096, FL_B = FL_A + 256 * FS, FL_C = FL_B + 256 * FS, FL_D = FL_C + 256 * FS, FL_P = FL_D + 256 * FS, FL_END = FL_P + 256 * FS;
static constexpr size_t WS_QKV = 262144;
static constexpr size_t WS_O = WS_QKV + (size_t)480 * 16384;
static constexpr size_t WS_H = WS_O + (size_t)80 * 32768;
static constexpr size_t WS_HH = WS_H + (size_t)80 * 32768;
static constexpr size_t WS_CSP = WS_HH + (size_t)216 * 32768;
static constexpr size_t WS_PS = WS_CSP + (size_t)16 * 16 * 224 * 4;
static constexpr size_t WS_S1 = WS_PS + (size_t)16 * 8 * 128 * 8;
static constexpr size_t WS_PWO = WS_S1 + (size_t)2048 * 8;
static constexpr size_t WS_PWD = WS_PWO + (size_t)8 * 20 * 5 * 1024;
static constexpr size_t WS_PWG = WS_PWD + (size_t)8 * 54 * 5 * 1024;
static constexpr size_t WS_END = WS_PWG + (size_t)16 * 40 * 7 * 1024;
static constexpr int LDS_BYTES = 163840;
static constexpr int CODE_LINES = 344;
static constexpr float C2_ = 0.22360679774997896f * 1.4426950408889634f;

struct Params { const u16* x; const u16* wqkv; const u16* wo; const u16* wg; const u16* wu; const u16* wd; char* out; char* ws; };

#define STAMP(i) do {} while (0)
#define STAMPC(i) do {} while (0)
#define STAMPW(i, w) do {} while (0)

DI unsigned pk2(float a, float b) { f32x2 v = {a, b}; return __builtin_bit_cast(unsigned, __builtin_convertvector(v, bf16x2_t)); }
DI float bflo(unsigned u) { return __uint_as_float(u << 16); }
DI float bfhi(unsigned u) { return __uint_as_float(u & 0xffff0000u); }
DI float rbf(float a) { return bflo(pk2(a, 0.f) & 0xffffu); }
template <bool SC1> DI void glds16(unsigned voff, const void* sbase, unsigned ldsaddr) {
    unsigned keep;
    if (SC1) asm volatile("s_mov_b32 %0, m0\n\ts_mov_b32 m0, %3\n\ts_nop 0\n\tglobal_load_lds_dwordx4 %1, %2 sc1\n\ts_mov_b32 m0, %0" : "=&s"(keep) : "v"(voff), "s"(sbase), "s"(ldsaddr) : "memory");
    else asm volatile("s_mov_b32 %0, m0\n\ts_mov_b32 m0, %3\n\ts_nop 0\n\tglobal_load_lds_dwordx4 %1, %2\n\ts_mov_b32 m0, %0" : "=&s"(keep) : "v"(voff), "s"(sbase), "s"(ldsaddr) : "memory");
}
#define GL_(v, off, sc) "global_load_lds_dwordx4 %" #v ", %[g] offset:" #off sc "\n\t"
#define GLHEAD "s_mov_b32 m0, %[l]\n\ts_nop 0\n\t"
template <bool SC1> DI void gl_lin5(unsigned v, const char* g, unsigned l) {
    if (SC1) asm volatile(GLHEAD GL_(0, -2048, " sc1") GL_(0, -1024, " sc1") GL_(0, 0, " sc1") GL_(0, 1024, " sc1") GL_(0, 2048, " sc1") :: "v"(v), [g] "s"(g), [l] "s"(l) : "memory");
    else asm volatile(GLHEAD GL_(0, -2048, "") GL_(0, -1024, "") GL_(0, 0, "") GL_(0, 1024, "") GL_(0, 2048, "") :: "v"(v), [g] "s"(g), [l] "s"(l) : "memory");
}
template <bool SC1> DI void gl_lin7(unsigned v, const char* g, unsigned l) {
    if (SC1) asm volatile(GLHEAD GL_(0, -3072, " sc1") GL_(0, -2048, " sc1") GL_(0, -1024, " sc1") GL_(0, 0, " sc1") GL_(0, 1024, " sc1") GL_(0, 2048, " sc1") GL_(0, 3072, " sc1") :: "v"(v), [g] "s"(g), [l] "s"(l) : "memory");
    else asm volatile(GLHEAD GL_(0, -3072, "") GL_(0, -2048, "") GL_(0, -1024, "") GL_(0, 0, "") GL_(0, 1024, "") GL_(0, 2048, "") GL_(0, 3072, "") :: "v"(v), [g] "s"(g), [l] "s"(l) : "memory");
}
template <bool SC1> DI void gl_str4(const unsigned* v, const char* g, unsigned l) {
    if (SC1) asm volatile(GLHEAD GL_(0, -2048, " sc1") GL_(1, -1024, " sc1") GL_(2, 0, " sc1") GL_(3, 1024, " sc1") :: "v"(v[0]), "v"(v[1]), "v"(v[2]), "v"(v[3]), [g] "s"(g), [l] "s"(l) : "memory");
    else asm volatile(GLHEAD GL_(0, -2048, "") GL_(1, -1024, "") GL_(2, 0, "") GL_(3, 1024, "") :: "v"(v[0]), "v"(v[1]), "v"(v[2]), "v"(v[3]), [g] "s"(g), [l] "s"(l) : "memory");
}
template <bool SC1> DI void gl_str4w(const unsigned* v, const char* g, unsigned l) {
    if (SC1) asm volatile(GLHEAD GL_(0, -4096, " sc1") GL_(1, -2048, " sc1") GL_(2, 0, " sc1") GL_(3, 2048, " sc1") :: "v"(v[0]), "v"(v[1]), "v"(v[2]), "v"(v[3]), [g] "s"(g), [l] "s"(l) : "memory");
    else asm volatile(GLHEAD GL_(0, -4096, "") GL_(1, -2048, "") GL_(2, 0, "") GL_(3, 2048, "") :: "v"(v[0]), "v"(v[1]), "v"(v[2]), "v"(v[3]), [g] "s"(g), [l] "s"(l) : "memory");
}
DI void gl_str8(const unsigned* v, const char* g, unsigned l) {
    asm volatile(GLHEAD GL_(0, -4096, "") GL_(1, -3072, "") GL_(2, -2048, "") GL_(3, -1024, "") GL_(4, 0, "") GL_(5, 1024, "") GL_(6, 2048, "") GL_(7, 3072, "")
                 :: "v"(v[0]), "v"(v[1]), "v"(v[2]), "v"(v[3]), "v"(v[4]), "v"(v[5]), "v"(v[6]), "v"(v[7]), [g] "s"(g), [l] "s"(l) : "memory");
}
template <bool SC1> DI void st16(void* ptr, u32x4 v) {
    if (SC1) asm volatile("global_store_dwordx4 %0, %1, off sc1\n\ts_nop 1" :: "v"(ptr), "v"(v) : "memory");
    else *(u32x4*)ptr = v;
}
DI u64 ld_dev(const void* p_) { return __hip_atomic_load((const u64*)p_, __ATOMIC_RELAXED, __HIP_MEMORY_SCOPE_AGENT); }
DI void st_dev(void* p_, u64 v) { __hip_atomic_store((u64*)p_, v, __ATOMIC_RELAXED, __HIP_MEMORY_SCOPE_AGENT); }
DI void ld64_async(u32x2& v, const void* ptr) { asm volatile("global_load_dwordx2 %0, %1, off sc1" : "=v"(v) : "v"(ptr) : "memory"); }
DI void ld32_async(float& v, const void* ptr) { asm volatile("global_load_dword %0, %1, off sc1" : "=v"(v) : "v"(ptr) : "memory"); }
DI void poll_flags(const void* flag, bool need, u64 tag) {
    for (unsigned spins = 0; spins < (1u << 18); ++spins) {
        const u64 v = need ? ld_dev(flag) : tag;
        if (__all(v == tag)) break;
    }
    asm volatile("" ::: "memory");
}
DI void warm64(const void* base, unsigned voff, unsigned lds0) {
    unsigned keep;
    asm volatile("s_mov_b32 %0, m0\n\ts_mov_b32 m0, %3\n\ts_nop 0\n\tglobal_load_lds_dword %1, %2 sc1\n\ts_mov_b32 m0, %0" : "=&s"(keep) : "v"(voff), "s"(base), "s"(lds0 + (unsigned)(LDS_BYTES - 256)) : "memory");
}
#define VMWAIT(N) asm volatile("s_waitcnt vmcnt(%0)" :: "i"(N) : "memory")
#define BAR_L() asm volatile("s_waitcnt lgkmcnt(0)\n\ts_barrier" ::: "memory")
#define BAR_VL(N) asm volatile("s_waitcnt vmcnt(%0) lgkmcnt(0)\n\ts_barrier" :: "i"(N) : "memory")
#define WAIT_LGKM0() do { __builtin_amdgcn_sched_barrier(0); __builtin_amdgcn_s_waitcnt(0xC07F); __builtin_amdgcn_sched_barrier(0); } while (0)
#define CBAR() do { WAIT_LGKM0(); asm volatile("" ::: "memory"); __builtin_amdgcn_s_barrier(); asm volatile("" ::: "memory"); __builtin_amdgcn_sched_barrier(0); } while (0)
#define SGB(mask, n) __builtin_amdgcn_sched_group_barrier(mask, n, 0)
template <int STEP, int MAXN> DI void bar_vl_n(int n) {
    if (n >= MAXN) { BAR_VL((MAXN * STEP > 63 ? 63 : MAXN * STEP)); }
    else if constexpr (MAXN > 0) bar_vl_n<STEP, MAXN - 1>(n);
}
template <int N> struct IC { static constexpr int v = N; };
template <int K, int END, class F> DI void static_for(F&& f) { if constexpr (K < END) { f(IC<K>{}); static_for<K + 1, END>(f); } }
template <int R, int NST, int NP, int LAG, bool PROLOGUE, int NEND = NST, class FI, class FP, class FQ>
DI void ring_loader(FI&& issue, FP&& pre, FQ&& post) {
    constexpr int L0 = (R < NST ? R : NST) - 1;
    if (PROLOGUE) {
#pragma unroll 1
        for (int k = 0; k <= L0; ++k) issue();
    }
    static_for<0, LAG + 1>([&](auto kc) { constexpr int k = decltype(kc)::v; BAR_VL((L0 - k) * NP); pre(k); if constexpr (k >= LAG && k - LAG + R < NST) issue(); post(k); });
    constexpr int KS = NST - R + LAG;
    if constexpr (KS > LAG + 1) {
#pragma unroll 1
        for (int k = LAG + 1; k < KS; ++k) { BAR_VL((R - 1 - LAG) * NP); pre(k); issue(); post(k); }
    }
    static_for<(KS > LAG + 1 ? KS : LAG + 1), NEND>([&](auto kc) { constexpr int k = decltype(kc)::v; BAR_VL((NST - 1 - k) * NP); pre(k); post(k); });
}
DI bf16x8 ld128(lcptr p) { return *(const LAS bf16x8*)p; }
DI s16x4 ld64(lcptr p) { return *(const LAS s16x4*)p; }
DI s16x4 ld64v(lcptr p) { return *(const volatile LAS s16x4*)p; }
DI s16x4 ldtr(lcptr p) { typedef short v4 __attribute__((ext_vector_type(4))); return __builtin_bit_cast(s16x4, __builtin_amdgcn_ds_read_tr16_b64_v4i16((LAS v4*)p)); }
DI bf16x8 cat8(s16x4 a, s16x4 b) { return __builtin_shufflevector(a, b, 0, 1, 2, 3, 4, 5, 6, 7); }
#define MFMA16(a, b, c) __builtin_amdgcn_mfma_f32_16x16x32_bf16(a, b, c, 0, 0, 0)
#define MFMA32(a, b, c) __builtin_amdgcn_mfma_f32_32x32x16_bf16(a, b, c, 0, 0, 0)
#define MFMA32K8(a, b, c) __builtin_amdgcn_mfma_f32_32x32x8bf16_1k(a, b, c, 0, 0, 0)
template <int CTRL> DI float dpp_add(float v) { return v + __int_as_float(__builtin_amdgcn_update_dpp(0, __float_as_int(v), CTRL, 0xf, 0xf, true)); }
DI float sum8(float v) { v = dpp_add<0xB1>(v); v = dpp_add<0x4E>(v); return dpp_add<0x141>(v); }
DI float sum_x16(float v) { auto r = __builtin_amdgcn_permlane16_swap(__float_as_uint(v), __float_as_uint(v), false, false); return __uint_as_float(r[0]) + __uint_as_float(r[1]); }
DI float sum_x32(float v) { auto r = __builtin_amdgcn_permlane32_swap(__float_as_uint(v), __float_as_uint(v), false, false); return __uint_as_float(r[0]) + __uint_as_float(r[1]); }
DI bf16x8 ones8() { bf16x8 o = {0x3f80, 0x3f80, 0x3f80, 0x3f80, 0x3f80, 0x3f80, 0x3f80, 0x3f80}; asm volatile("" : "+v"(o)); return o; }

DI f32x2 ln_scalars(float sum, float sq) {
    const float mean = sum * (1.0f / D_);
    float var = sq * (1.0f / D_) - mean * mean; var = var > 0.f ? var : 0.f;
    const float sd = rbf(__builtin_sqrtf(rbf(rbf(var) + 1e-5f)));
    return (f32x2){rbf(mean), 1.0f / sd};
}
template <bool FUSED> DI void publish(const Params& p, size_t flag_off, int wid, int lane, u64 tag, int sidx = 47) {
    if (FUSED) { BAR_VL(0); STAMP(sidx); if (wid == 0 && lane == 0) st_dev(p.ws + WS_FLAG + flag_off, tag); }
}

namespace rp { constexpr int L_G = 132608, L_U = L_G + 10752, L_O = L_G + 21504, L_D = 102400; }
DI void rp1_addr(const Params& p, int lane, int lw, int ti, int tj, const char* (&ga)[7]) {
    const int ns = ti < 8 ? 3 : 2, s0 = ti < 8 ? 3 * ti : 24 + 2 * (ti - 8);
    const int ct = tj >> 1, z = 2 * ti + (tj & 1);
#pragma unroll
    for (int i = 0; i < 7; ++i) { const int n = lw + 4 * i;
        if (n < 21) { int u = 64 * n + lane; const int mat = u >= 672; u -= 672 * mat; int row = u / 14; int col = 112 * tj + 8 * (u - 14 * row); col = col > F_ - 8 ? F_ - 8 : col;
            row = row < 16 * ns ? row : 16 * ns - 1;
            ga[i] = (const char*)((mat ? p.wu : p.wg) + (size_t)(16 * s0 + row) * F_ + col);
        } else { const int m = n < 26 ? n - 21 : 4, u = 64 * m + lane, row = u / 10, c = u - 10 * row;
            ga[i] = (const char*)(p.wo + (size_t)(32 * (z < 20 ? z : 0) + row) * D_ + 80 * ct + 8 * c); } }
}
DI void glds16v(const void* vaddr, unsigned ldsaddr) {
    asm volatile("s_nop 0\n\ts_mov_b32 m0, %1\n\ts_nop 0\n\tglobal_load_lds_dwordx4 %0, off" :: "v"(vaddr), "s"(ldsaddr) : "memory");
}
DI void rp1_issue(const char* (&ga)[7], unsigned lds0, int lw) {
#pragma unroll
    for (int i = 0; i < 7; ++i) { const int n = lw + 4 * i; glds16v(ga[i], (unsigned)__builtin_amdgcn_readfirstlane(lds0 + rp::L_G + (n < 26 ? n : 25) * 1024)); }
}
template <bool FUSED>
DI void rp1_store(const Params& p, lptr L, int lane, int lw, int ti, int tj, int part = 3) {
    const int ns = ti < 8 ? 3 : 2, s0 = ti < 8 ? 3 * ti : 24 + 2 * (ti - 8);
    const int ct = tj >> 1, z = 2 * ti + (tj & 1);
    const int h = lane >> 5, blk = (lane >> 4) & 1, g = lane >> 4, q = (lane & 15) >> 2, pp = lane & 3;
    const bf16x8 ones = ones8();
#pragma unroll 1
    for (int t = lw + (part == 1 ? 4 : 0); t < (part == 0 ? 4 : part == 2 ? 0 : 7); t += 4) {
        lcptr src = (lcptr)L + (t < 6 ? ((t & 1) ? rp::L_U : rp::L_G) + (32 * (t >> 1) + 16 * blk + 4 * pp) * 2 : (blk ? rp::L_U : rp::L_G) + (96 + 4 * pp) * 2) + (8 * h + q) * 224;
        s16x4 f[3][2];
#pragma unroll
        for (int s = 0; s < 3; ++s) { f[s][0] = ldtr(src + s * 16 * 224); f[s][1] = ldtr(src + s * 16 * 224 + 4 * 224); }
        f32x16 cs = {};
        char* dst = p.ws + WS_PWG + ((size_t)(tj * 40 + s0) * 7 + t) * 1024 + lane * 16;
#pragma unroll
        for (int s = 0; s < 3; ++s) if (s < ns) { const bf16x8 fr = cat8(f[s][0], f[s][1]); st16<FUSED>(dst + s * 7168, __builtin_bit_cast(u32x4, fr)); cs = MFMA32(fr, ones, cs); }
        if ((lane & 31) == 0) { float* cd = (float*)(p.ws + WS_CSP) + (ti * 16 + tj) * 224 + 32 * t + 4 * h;
#pragma unroll
            for (int u = 0; u < 4; ++u) st16<FUSED>(cd + 8 * u, __builtin_bit_cast(u32x4, ((f32x4){cs[4 * u], cs[4 * u + 1], cs[4 * u + 2], cs[4 * u + 3]}))); }
    }
    if (z < 20 && part >= 2) {
#pragma unroll 1
        for (int nt = 3 - lw; nt < 5; nt += 4) {
            lcptr src = (lcptr)L + rp::L_O + (8 * g + q) * 160 + (16 * nt + 4 * pp) * 2;
            st16<FUSED>(p.ws + WS_PWO + ((size_t)(ct * 20 + z) * 5 + nt) * 1024 + lane * 16, __builtin_bit_cast(u32x4, cat8(ldtr(src), ldtr(src + 4 * 160)))); }
    }
}
DI void rp2_issue(const Params& p, unsigned lds0, int lane, int wid, int ti, int tj) {
    const int ct = tj >> 1, z = 2 * ti + (tj & 1), np = z + 32 < 54 ? 10 : 5;
#pragma unroll 1
    for (int n = wid; n < np; n += 8) { const int im = n >= 5, u = 64 * (n - 5 * im) + lane, row = u / 10, c = u - 10 * row;
        glds16<false>((unsigned)((32 * (z + 32 * im) + row) * D_ + 80 * ct + 8 * c) * 2u, p.wd, lds0 + rp::L_D + n * 1024); }
}
template <bool FUSED>
DI void rp2_store(const Params& p, lptr L, int lane, int wid, int ti, int tj) {
    const int ct = tj >> 1, z = 2 * ti + (tj & 1), np = z + 32 < 54 ? 10 : 5;
    const int g = lane >> 4, q = (lane & 15) >> 2, pp = lane & 3;
#pragma unroll 1
    for (int n = wid; n < np; n += 8) { const int im = n >= 5, nt = n - 5 * im;
        lcptr src = (lcptr)L + rp::L_D + im * 5120 + (8 * g + q) * 160 + (16 * nt + 4 * pp) * 2;
        st16<FUSED>(p.ws + WS_PWD + ((size_t)(ct * 54 + z + 32 * im) * 5 + nt) * 1024 + lane * 16, __builtin_bit_cast(u32x4, cat8(ldtr(src), ldtr(src + 4 * 160)))); }
}
DI u64 make_tag(u32x2& ev) {
    asm volatile("" : "+v"(ev));
    const u64 e = (((u64)(unsigned)__builtin_amdgcn_readfirstlane(ev[1]) << 32) | (unsigned)__builtin_amdgcn_readfirstlane(ev[0])) + 1;
    return e;
}

namespace pa { constexpr int R = 4, NST = 10, LA = 0, LB = R * 16384, L_STAT = 2 * R * 16384, L_CS = L_STAT + 1024; }
template <bool FUSED>
DI void phase_a_stats(const Params& p, lptr L, int lane, int wid, int ti, u32x2& ev, u64& tag) {
    const int r = lane >> 3, c = lane & 7;
    if (wid >= 4) { const char* ga[7]; rp1_addr(p, lane, wid - 4, ti, 15, ga); rp1_issue(ga, (unsigned)(uintptr_t)L, wid - 4); }
#pragma unroll
    for (int hlf = 0; hlf < 2; ++hlf) {
        const int row = 128 * ti + 16 * wid + 8 * hlf + r;
        const u32x4* src = (const u32x4*)(p.x + (size_t)row * D_) + c;
        u32x4 v[10];
#pragma unroll
        for (int i = 0; i < 10; ++i) v[i] = src[8 * i];
        float a = 0.f, q = 0.f;
#pragma unroll
        for (int i = 0; i < 10; ++i)
#pragma unroll
            for (int e = 0; e < 4; ++e) { const float lo = bflo(v[i][e]), hi = bfhi(v[i][e]); a += lo + hi; q += lo * lo + hi * hi; }
        const f32x2 st = ln_scalars(sum8(a), sum8(q));
        if (c == 0) { const u64 w = ((u64)__float_as_uint(st.y) << 32) | __float_as_uint(st.x); char* dst = p.ws + WS_S1 + (size_t)row * 8; if (FUSED) st_dev(dst, w); else *(u64*)dst = w; }
    }
    BAR_VL(0);
    if (FUSED) { tag = make_tag(ev); if (wid == 0 && lane == 0) st_dev(p.ws + WS_FLAG + FL_A + (ti * 16 + 15) * FS, tag); }
    if (wid >= 4) rp1_store<FUSED>(p, L, lane, wid - 4, ti, 15);
    if (FUSED) { BAR_VL(0); if (wid == 0 && lane == 0) st_dev(p.ws + WS_FLAG + FL_P + (ti * 16 + 15) * FS, tag); }
}
template <bool FUSED>
DI void phase_a(const Params& p, lptr L, int lane, int wid, int ti, int tj, int rot, u32x2& ev, u64& tag) {
    using namespace pa;
    const unsigned lds0 = (unsigned)(uintptr_t)L;
    if (wid >= 4) {
        const int lw = wid - 4, r = lane >> 3, c = lane & 7;
        const bool isA = lw < 2;
        unsigned vo[2];
#pragma unroll
        for (int e = 0; e < 2; ++e) vo[e] = isA ? (unsigned)(r * D_ + ((c ^ ((4 * e + (r >> 1)) & 7)) * 8)) * 2u : (unsigned)(r * 3 * D_ + ((c ^ (4 * ((r >> 1) & 1) + 2 * e)) * 8)) * 2u;
        unsigned vp[8];
#pragma unroll
        for (int i = 0; i < 8; ++i) vp[i] = vo[i & 1] + (isA ? (unsigned)i * (8 * D_ * 2 - 1024) : (unsigned)(i & 3) * (8 * 3 * D_ * 2 - 1024));
        const char* gb = isA ? (const char*)p.x + (size_t)(128 * ti + 64 * lw) * D_ * 2 + 4096 + rot * 128 : (const char*)p.wqkv + ((size_t)(32 * (lw - 2) + 64 * rot) * 3 * D_ + 128 * tj) * 2 + 2048;
        const int gstep = isA ? 128 : 64 * 3 * D_ * 2;
        int ks = rot; unsigned sl = lds0 + (isA ? LA + lw * 8192 + 4096 : LB + (lw - 2) * 4096 + 2048), sslot = 0;
        auto issue = [&]() {
            if (isA) gl_str8(vp, gb, sl + sslot); else { gl_str4<false>(vp, gb, sl + sslot); gl_str4<false>(vp, gb + 128, sl + sslot + 8192); }
            gb += gstep; if (++ks == NST) { ks = 0; gb -= (size_t)NST * gstep; }
            sslot = (sslot + 16384) & (R * 16384 - 1);
        };
        const int g = lane >> 4, q = (lane & 15) >> 2, pp = lane & 3;
        unsigned aC[2];
#pragma unroll
        for (int t = 0; t < 2; ++t) aC[t] = (unsigned)(LB + ((lw >> 1) * 8 + g) * 1024 + q * 128 + (((2 * (2 * (lw & 1) + t) + (pp >> 1)) ^ (4 * ((q >> 1) & 1) + 2 * (g & 1))) * 16) + (pp & 1) * 8);
        f32x4 cs[2] = {{0.f, 0.f, 0.f, 0.f}, {0.f, 0.f, 0.f, 0.f}};
        const bf16x8 ones = ones8();
        s16x4 cf[2][2][2];
        __builtin_amdgcn_s_setprio(3);
        auto pre = [&](int s) { lcptr q0 = (lcptr)L + (s & 3) * 16384;
#pragma unroll
            for (int ks2 = 0; ks2 < 2; ++ks2)
#pragma unroll
                for (int t = 0; t < 2; ++t) { cf[ks2][t][0] = ldtr(q0 + aC[t] + ks2 * 4096); cf[ks2][t][1] = ldtr(q0 + aC[t] + ks2 * 4096 + 512); } };
        auto post = [&](int) {
#pragma unroll
            for (int ks2 = 0; ks2 < 2; ++ks2)
#pragma unroll
                for (int t = 0; t < 2; ++t) cs[t] = MFMA16(cat8(cf[ks2][t][0], cf[ks2][t][1]), ones, cs[t]); };
        issue(); issue();
        __builtin_amdgcn_sched_barrier(0);
        const char* ga[7]; rp1_addr(p, lane, lw, ti, tj, ga);
        BAR_VL(8); pre(0); issue(); rp1_issue(ga, lds0, lw);
        if (FUSED) tag = make_tag(ev);
        BAR_VL(15); post(0); pre(1); issue();
        BAR_VL(15); post(1); pre(2); issue();
        BAR_VL(8); post(2); pre(3); issue(); rp1_store<FUSED>(p, L, lane, lw, ti, tj, 0);
        BAR_VL(14); post(3); pre(4); issue(); rp1_store<FUSED>(p, L, lane, lw, ti, tj, 1);
        if (lw < 3) BAR_VL(20); else BAR_VL(14);
        post(4); pre(5); issue(); rp1_store<FUSED>(p, L, lane, lw, ti, tj, 2);
#pragma unroll 1
        for (int k = 6; k < NST - 2; ++k) { BAR_VL(8); post(k - 1); pre(k); issue(); }
        BAR_VL(8); post(NST - 3); pre(NST - 2);
        BAR_VL(0); post(NST - 2); pre(NST - 1); post(NST - 1);
        if (isA) {
            if (FUSED) poll_flags(p.ws + WS_FLAG + FL_A + (ti * 16 + 15) * FS, lane == 0, tag);
            const char* src = p.ws + WS_S1 + (size_t)(128 * ti + 64 * lw + lane) * 8;
            const u64 w = FUSED ? ld_dev(src) : *(const u64*)src;
            *(LAS u64*)(L + L_STAT + (64 * lw + lane) * 8) = w;
        }
        __builtin_amdgcn_s_setprio(0);
        if ((lane & 15) == 0) {
#pragma unroll
            for (int t = 0; t < 2; ++t) *(LAS f32x4*)(L + L_CS + (32 * lw + 16 * t + 4 * g) * 4) = cs[t];
        }
        BAR_L();
        BAR_L();
    } else {
        const int wm = wid >> 1, wn = wid & 1, r32 = lane & 31, h = lane >> 5, blk = (lane >> 4) & 1, q = (lane & 15) >> 2, pp = lane & 3;
        unsigned aX[4], aW[2];
        { const int row = 64 * wm + r32;
#pragma unroll
          for (int u = 0; u < 4; ++u) aX[u] = (unsigned)(LA + (row >> 3) * 1024 + (row & 7) * 128 + (((2 * u + h) ^ ((row >> 1) & 7)) * 16));
#pragma unroll
          for (int nt = 0; nt < 2; ++nt) aW[nt] = (unsigned)(LB + (wn * 8 + h) * 1024 + q * 128 + (((4 * nt + 2 * blk + (pp >> 1)) ^ (4 * ((q >> 1) & 1) + 2 * h)) * 16) + (pp & 1) * 8); }
        f32x16 acc[2][2];
#pragma unroll
        for (int i = 0; i < 16; ++i) { acc[0][0][i] = 0.f; acc[0][1][i] = 0.f; acc[1][0][i] = 0.f; acc[1][1][i] = 0.f; }
        bf16x8 X[4][2], W[4][2];
        auto rd = [&](int slot, int u) {
            lcptr sl = (lcptr)L + slot * 16384;
#pragma unroll
            for (int nt = 0; nt < 2; ++nt) W[u][nt] = cat8(ldtr(sl + aW[nt] + u * 2048), ldtr(sl + aW[nt] + u * 2048 + 512));
#pragma unroll
            for (int mt = 0; mt < 2; ++mt) X[u][mt] = ld128(sl + aX[u] + mt * 4096);
        };
        auto mm = [&](int u) {
#pragma unroll
            for (int mt = 0; mt < 2; ++mt)
#pragma unroll
                for (int nt = 0; nt < 2; ++nt) acc[mt][nt] = MFMA32(W[u][nt], X[u][mt], acc[mt][nt]);
        };
        auto il = [&]() { SGB(0x8, 1); SGB(0x100, 2); SGB(0x8, 1); SGB(0x100, 2); SGB(0x8, 1); SGB(0x100, 2); SGB(0x8, 1); __builtin_amdgcn_sched_barrier(0); };
        auto bar = [&]() { __builtin_amdgcn_sched_barrier(0); asm volatile("" ::: "memory"); __builtin_amdgcn_s_barrier(); asm volatile("" ::: "memory"); __builtin_amdgcn_sched_barrier(0); };
        bar();
        if (FUSED) VMWAIT(0);
        STAMP(1); STAMPC(26);
        rd(0, 0); rd(0, 1);
#pragma unroll 1
        for (int s = 0; s < NST - 1; ++s) {
            const int c = s & 3, n = (s + 1) & 3;
            rd(c, 2); mm(0); il();
            rd(c, 3); mm(1); il();
            bar();
            rd(n, 0); mm(2); il();
            rd(n, 1); mm(3); il();
        }
        rd((NST - 1) & 3, 2); mm(0); il();
        rd((NST - 1) & 3, 3); mm(1); il();
        mm(2); mm(3);
        STAMP(2); STAMPC(27);
        if (FUSED) tag = make_tag(ev);
        BAR_L();
        STAMP(38);
        {
            f32x2 st[2]; float cv[2];
#pragma unroll
            for (int i = 0; i < 2; ++i) { st[i] = *(const LAS f32x2*)(L + L_STAT + (64 * wm + 32 * i + r32) * 8); cv[i] = *(const LAS float*)(L + L_CS + (64 * wn + 32 * i + r32) * 4); }
            bf16x8 cf[2], mf[2];
#pragma unroll
            for (int i = 0; i < 2; ++i) { const float hi = rbf(cv[i]); const unsigned c = h ? 0u : pk2(hi, cv[i] - hi), m = h ? 0u : pk2(-st[i].x, -st[i].x);
                cf[i] = __builtin_bit_cast(bf16x8, ((u32x4){c, 0u, 0u, 0u})); mf[i] = __builtin_bit_cast(bf16x8, ((u32x4){m, 0u, 0u, 0u})); }
#pragma unroll
            for (int mt = 0; mt < 2; ++mt)
#pragma unroll
                for (int nt = 0; nt < 2; ++nt) acc[mt][nt] = MFMA32(cf[nt], mf[mt], acc[mt][nt]);
#pragma unroll
            for (int mt = 0; mt < 2; ++mt) { const int tok = 64 * wm + 32 * mt + r32; const float sq = st[mt].y * C2_;
#pragma unroll
                for (int nt = 0; nt < 2; ++nt)
#pragma unroll
                    for (int u = 0; u < 4; ++u) { const int col = 64 * wn + 32 * nt + 8 * u + 4 * h;
                        const float sc = (128 * tj + col < D_) ? sq : st[mt].y;
                        *(LAS u32x2*)(L + (col >> 2) * 1024 + tok * 8) = (u32x2){pk2(acc[mt][nt][4 * u] * sc, acc[mt][nt][4 * u + 1] * sc), pk2(acc[mt][nt][4 * u + 2] * sc, acc[mt][nt][4 * u + 3] * sc)}; } }
        }
        BAR_L();
        STAMP(39);
    }
    char* dst = p.ws + WS_QKV + (size_t)(32 * tj) * 16384 + (size_t)ti * 1024;
#pragma unroll
    for (int i = 0; i < 4; ++i) { const int gq = 4 * wid + i; st16<FUSED>(dst + (size_t)gq * 16384 + lane * 16, *(const LAS u32x4*)(L + gq * 1024 + lane * 16)); }
    STAMP(3);
    publish<FUSED>(p, FL_A + (ti * 16 + tj) * FS, wid, lane, tag, 42);
    if (FUSED && wid == 1 && lane == 0) st_dev(p.ws + WS_FLAG + FL_P + (ti * 16 + tj) * FS, tag);
}

namespace pb {
constexpr int GS = 3104;
constexpr int L_K = 0, L_V = 10 * GS, L_ONE = 20 * GS, L_Q = 21 * GS, L_O = 102400;
static_assert(L_Q + 10240 <= L_O, "LDS map");
}
#define ATT_STAMP() do {} while (0)
template <bool EDGE>
DI f32x16 attn_wave(lcptr L, int hh, int qb, int lane, int tstart, u64* stq) {
    using namespace pb;
    const int r = lane & 31, h = lane >> 5;
    bf16x8 q0; s16x4 q1;
    { lcptr qp = L + L_Q + hh * 5 * 1024 + (32 * qb + r) * 8;
      q0 = cat8(ld64(qp + (2 * h) * 1024), ld64(qp + (2 * h + 1) * 1024));
      const s16x4 t = ld64(qp + 4 * 1024); q1 = h ? (s16x4){0, 0, 0, 0} : t; }
    lcptr kp = L + L_K + hh * 5 * GS + (32 * qb + r) * 8;
    const f32x16 z16 = {};
    bf16x8 k0[9]; s16x4 k1[9];
#pragma unroll
    for (int t = 0; t < 9; ++t) { k0[t] = cat8(ld64v(kp + (2 * h) * GS + t * 256), ld64v(kp + (2 * h + 1) * GS + t * 256)); k1[t] = ld64v(kp + 4 * GS + t * 256); }
    float m;
    { f32x16 a = MFMA32K8(k1[8], q1, MFMA32(k0[8], q0, z16)), b = MFMA32K8(k1[0], q1, MFMA32(k0[0], q0, z16));
      const float bias = (!EDGE || tstart == 0) ? 0.f : -INFINITY;
#pragma unroll
      for (int i = 0; i < 16; ++i) { const int c = (i & 3) + 8 * (i >> 2) + 4 * h; a[i] = (c <= r) ? a[i] : (EDGE ? b[i] + bias : b[i]); }
      float x = __builtin_fmaxf(__builtin_fmaxf(a[0], a[1]), a[2]), y = __builtin_fmaxf(__builtin_fmaxf(a[3], a[4]), a[5]);
#pragma unroll
      for (int i = 6; i < 14; i += 4) { x = __builtin_fmaxf(__builtin_fmaxf(x, a[i]), a[i + 1]); y = __builtin_fmaxf(__builtin_fmaxf(y, a[i + 2]), a[i + 3]); }
      m = __builtin_fmaxf(__builtin_fmaxf(__builtin_fmaxf(x, a[14]), a[15]), y); }
#pragma unroll
    for (int t = 1; t < 8; ++t) { const f32x16 a = MFMA32K8(k1[t], q1, MFMA32(k0[t], q0, z16));
        float x = __builtin_fmaxf(__builtin_fmaxf(a[0], a[1]), a[2]), y = __builtin_fmaxf(__builtin_fmaxf(a[3], a[4]), a[5]);
#pragma unroll
        for (int i = 6; i < 14; i += 4) { x = __builtin_fmaxf(__builtin_fmaxf(x, a[i]), a[i + 1]); y = __builtin_fmaxf(__builtin_fmaxf(y, a[i + 2]), a[i + 3]); }
        x = __builtin_fmaxf(__builtin_fmaxf(x, a[14]), a[15]);
        if (EDGE) x = t >= tstart ? x : -INFINITY;
        m = __builtin_fmaxf(__builtin_fmaxf(m, x), (EDGE && t < tstart) ? -INFINITY : y); }
    { auto rr = __builtin_amdgcn_permlane32_swap(__float_as_uint(m), __float_as_uint(m), false, false); m = __builtin_fmaxf(__uint_as_float(rr[0]), __uint_as_float(rr[1])); }
    f32x16 nm16;
#pragma unroll
    for (int i = 0; i < 16; ++i) nm16[i] = -m;
    BAR_VL(0);
    ATT_STAMP();
    const int blk = (lane >> 4) & 1, vq = (lane & 15) >> 2, vp = lane & 3, dg = 4 * blk + vp;
    lcptr vpz = L + (dg < 5 ? L_V + (hh * 5 + dg) * GS : L_ONE) + (32 * qb + 4 * h + vq) * 8;
    f32x16 O = z16;
#pragma unroll
    for (int t = 1; t < 8; ++t) {
        const bf16x8 v0 = cat8(ldtr(vpz + t * 256), ldtr(vpz + t * 256 + 64)), v1 = cat8(ldtr(vpz + t * 256 + 128), ldtr(vpz + t * 256 + 192));
        f32x16 a = MFMA32K8(k1[t], q1, MFMA32(k0[t], q0, nm16));
        if (EDGE) { const float bias = t >= tstart ? 0.f : -INFINITY;
#pragma unroll
            for (int i = 0; i < 16; ++i) a[i] += bias; }
        unsigned P[8];
#pragma unroll
        for (int i = 0; i < 8; ++i) P[i] = pk2(__builtin_amdgcn_exp2f(a[2 * i]), __builtin_amdgcn_exp2f(a[2 * i + 1]));
        O = MFMA32(v0, __builtin_bit_cast(bf16x8, ((u32x4){P[0], P[1], P[2], P[3]})), O);
        O = MFMA32(v1, __builtin_bit_cast(bf16x8, ((u32x4){P[4], P[5], P[6], P[7]})), O);
    }
    { const bf16x8 v0 = cat8(ldtr(vpz + 8 * 256), ldtr(vpz + 8 * 256 + 64)), v1 = cat8(ldtr(vpz + 8 * 256 + 128), ldtr(vpz + 8 * 256 + 192));
      const bf16x8 w0 = cat8(ldtr(vpz), ldtr(vpz + 64)), w1 = cat8(ldtr(vpz + 128), ldtr(vpz + 192));
      f32x16 a = MFMA32K8(k1[8], q1, MFMA32(k0[8], q0, nm16)); const f32x16 b = MFMA32K8(k1[0], q1, MFMA32(k0[0], q0, nm16));
      const float bias = (!EDGE || tstart == 0) ? 0.f : -INFINITY;
#pragma unroll
      for (int i = 0; i < 16; ++i) { const int c = (i & 3) + 8 * (i >> 2) + 4 * h; a[i] = (c <= r) ? a[i] : (EDGE ? b[i] + bias : b[i]); }
      unsigned P[8], P0[8];
#pragma unroll
      for (int i = 0; i < 8; ++i) { P[i] = pk2(__builtin_amdgcn_exp2f(a[2 * i]), __builtin_amdgcn_exp2f(a[2 * i + 1]));
          const int c0 = ((2 * i) & 3) + 8 * ((2 * i) >> 2) + 4 * h; const unsigned mk = (c0 <= r ? 0xffffu : 0u) | (c0 + 1 <= r ? 0xffff0000u : 0u);
          P0[i] = P[i] & ~mk; P[i] &= mk; }
      O = MFMA32(v0, __builtin_bit_cast(bf16x8, ((u32x4){P[0], P[1], P[2], P[3]})), O);
      O = MFMA32(v1, __builtin_bit_cast(bf16x8, ((u32x4){P[4], P[5], P[6], P[7]})), O);
      O = MFMA32(w0, __builtin_bit_cast(bf16x8, ((u32x4){P0[0], P0[1], P0[2], P0[3]})), O);
      O = MFMA32(w1, __builtin_bit_cast(bf16x8, ((u32x4){P0[4], P0[5], P0[6], P0[7]})), O); }
    return O;
}
template <bool FUSED>
DI void phase_b(const Params& p, lptr L, int lane, int wid, int ti, int tj, u64 tag) {
    using namespace pb;
    const int tid = wid * 64 + lane;
    const unsigned lds0 = (unsigned)(uintptr_t)L;
    rp2_issue(p, lds0, lane, wid, ti, tj);
    if (ti < 2) {
        for (int n = tid; n < (2 - ti) * 20 * 64; n += 512) { const int pc = n >> 6, mb = pc % (2 - ti), gq = pc / (2 - ti);
            *(LAS u32x4*)(L + gq * GS + mb * 1024 + (n & 63) * 16) = (u32x4){0u, 0u, 0u, 0u}; }
    }
    if (tid < 384) *(LAS u32x2*)(L + L_ONE + tid * 8) = (u32x2){0x00003f80u, 0u};
    const int sub = wid & 3, mtile = sub < 3 ? ti - 2 + sub : ti;
    const char* g0 = p.ws + WS_QKV + (size_t)((sub < 3 ? 160 : 0) + 5 * (2 * tj + (wid >> 2))) * 16384 + (size_t)(mtile < 0 ? 0 : mtile) * 1024;
    const unsigned l0 = lds0 + (sub < 3 ? L_K + (wid >> 2) * 5 * GS + sub * 1024 : L_Q + (wid >> 2) * 5120);
    BAR_VL(0);
    rp2_store<FUSED>(p, L, lane, wid, ti, tj);
    if (FUSED) {
        if (wid == 7) { const int mb = lane / 6, sec = (lane % 6) >> 1, e = lane & 1, mt2 = ti - 2 + mb;
            const bool need = lane < 18 && mt2 >= 0 && (sec > 0 || mb == 2);
            poll_flags(p.ws + WS_FLAG + FL_A + ((mt2 < 0 ? 0 : mt2) * 16 + ((640 * sec + 40 * tj + 39 * e) >> 7)) * FS, need, tag); STAMPW(35, 7); }
    }
    BAR_L();
    STAMP(4);
    if (sub == 3) {
#pragma unroll
        for (int d = 0; d < 5; ++d) glds16<FUSED>(lane * 16, g0 + d * 16384, l0 + d * 1024);
        BAR_VL(0);
    } else if (mtile >= 0) {
#pragma unroll
        for (int d = 0; d < 5; ++d) glds16<FUSED>(lane * 16, g0 + d * 16384, l0 + d * GS);
#pragma unroll
        for (int d = 0; d < 5; ++d) glds16<FUSED>(lane * 16, g0 + (160 + d) * 16384, l0 + L_V + d * GS);
        BAR_VL(5);
    } else BAR_L();
    STAMP(5);
    const int hh = wid >> 2, qb = wid & 3, r = lane & 31, h = lane >> 5;
    const int q0g = 128 * ti + 32 * qb;
    f32x16 O;
    u64* stq = nullptr;
    if (ti >= 2) O = attn_wave<false>((lcptr)L, hh, qb, lane, 0, stq); else O = attn_wave<true>((lcptr)L, hh, qb, lane, q0g >= 256 ? 0 : 8 - (q0g >> 5), stq);
    { auto rr = __builtin_amdgcn_permlane32_swap(__float_as_uint(O[8]), __float_as_uint(O[8]), false, false);
      const float il = 1.0f / __uint_as_float(rr[1]);
      const int row = 32 * qb + r;
#pragma unroll
      for (int u = 0; u < 3; ++u) { const int dgp = 2 * u + h; if (dgp < 5) { const int cgp = 5 * hh + dgp;
          *(LAS u32x2*)(L + L_O + (cgp >> 1) * 2048 + row * 16 + (cgp & 1) * 8) = (u32x2){pk2(O[4 * u] * il, O[4 * u + 1] * il), pk2(O[4 * u + 2] * il, O[4 * u + 3] * il)}; } } }
    STAMP(6); STAMPW(37, 7);
    BAR_L();
    for (int n = wid; n < 10; n += 8)
        st16<FUSED>(p.ws + WS_O + (size_t)(5 * tj + (n >> 1)) * 32768 + (size_t)(128 * ti + 64 * (n & 1)) * 16 + lane * 16, *(const LAS u32x4*)(L + L_O + n * 1024 + lane * 16));
    STAMP(7);
    publish<FUSED>(p, FL_B + (ti * 16 + tj) * FS, wid, lane, tag);
}

template <int MODE> struct PC;
template <> struct PC<0> { static constexpr int NST = 10, RB = 10, RA = 6, LA = 102400, L_RES = 151552, L_X = 102400, L_STG = 122880; };
template <> struct PC<1> { static constexpr int NST = 27, RB = 8, RA = 8, LA = 81920, L_RES = 147456, L_X = 81920, L_STG = 102400; };
template <bool FUSED, int MODE>
DI void ce_prefetch(const Params& p, lptr L, int lane, int wid, int tj) {
    const char* wb = p.ws + (MODE ? WS_PWD + (size_t)(tj >> 1) * 54 * 5120 : WS_PWO + (size_t)(tj >> 1) * 20 * 5120) + (wid - 6) * 5120 + 2048;
    const unsigned l = (unsigned)(uintptr_t)L + (wid - 6) * 5120 + 2048;
#pragma unroll 1
    for (int s = 0; s < PC<MODE>::RB; ++s) gl_lin5<FUSED>(lane * 16, wb + (size_t)s * 10240, l + s * 10240);
}
template <bool FUSED, int MODE>
DI void phase_ce(const Params& p, lptr L, int lane, int wid, int ti, int tj, u64 tag) {
    typedef PC<MODE> C;
    constexpr int NST = C::NST, RA = C::RA, RB = C::RB;
    const unsigned lds0 = (unsigned)(uintptr_t)L;
    const int rh = tj & 1, ct = tj >> 1, r0 = 128 * ti + 64 * rh;
    const int wm = (wid >> 1) & 1, wk = wid & 1, r16 = lane & 15, g = lane >> 4;
    if (wid >= 6) {
        if (MODE == 0) {
            bar_vl_n<5, RB - 1>(RB - 1);
            BAR_VL(0);
#pragma unroll 1
            for (int s = 2; s < NST; ++s) BAR_L();
        } else {
            const char* wb = p.ws + WS_PWD + (size_t)ct * 54 * 5120 + (wid - 6) * 5120 + 2048 + (size_t)RB * 10240;
            const unsigned l0 = lds0 + (wid - 6) * 5120 + 2048; unsigned l = l0;
            ring_loader<RB, NST, 5, 2, false>([&]() { gl_lin5<FUSED>(lane * 16, wb, l); wb += 10240; l = l == l0 + (RB - 1) * 10240 ? l0 : l + 10240; }, [&](int) {}, [&](int) {});
        }
        BAR_L(); BAR_L();
    } else if (wid >= 4) {
        const int la = wid - 4;
        const unsigned voA = lane * 16;
#pragma unroll
        for (int i = 0; i < 5; ++i) { const int pc = 5 * la + i;
            if (MODE == 0) { const int n = 64 * pc + lane; glds16<false>((unsigned)((n / 10) * D_ * 2 + (n % 10) * 16), (const char*)p.x + (size_t)r0 * D_ * 2 + ct * 160, lds0 + C::L_RES + pc * 1024); }
            else glds16<FUSED>(voA, p.ws + WS_H + (size_t)(10 * ct + pc) * 32768 + (size_t)r0 * 16, lds0 + C::L_RES + pc * 1024); }
        if (FUSED) poll_flags(p.ws + WS_FLAG + (MODE ? FL_D : FL_B) + (ti * 16 + (lane & 15)) * FS, lane < 16, tag);
        STAMPW(MODE ? 17 : 9, 4);
        const char* ab = p.ws + (MODE ? WS_HH : WS_O) + (size_t)r0 * 16 + (size_t)(4 * la) * 32768 + 2048;
        unsigned vp[4];
#pragma unroll
        for (int i = 0; i < 4; ++i) vp[i] = voA + (unsigned)i * (32768 - 1024);
        const unsigned l0 = lds0 + C::LA + la * 4096 + 2048; unsigned l = l0;
        ring_loader<RA, NST, 4, 2, true>([&]() { gl_str4<FUSED>(vp, ab, l); ab += 8 * 32768; l = l == l0 + (RA - 1) * 8192 ? l0 : l + 8192; }, [&](int) {}, [&](int) {});
        BAR_L(); BAR_L();
    } else {
        const unsigned aA = (unsigned)(C::LA + (4 * wk + g) * 1024 + (32 * wm + r16) * 16), aB = (unsigned)(wk * 5120 + lane * 16);
        f32x4 acc[2][5];
#pragma unroll
        for (int i = 0; i < 2; ++i)
#pragma unroll
            for (int n = 0; n < 5; ++n) acc[i][n] = (f32x4){0.f, 0.f, 0.f, 0.f};
        bf16x8 Af[3][2], Bf[3][5];
        const int kofs = wk * 256;
        int sa = 0, sb = 0;
        auto rd = [&](int b) {
            lcptr la = (lcptr)L + aA + sa * 8192; lcptr lb = (lcptr)L + aB + sb * 10240;
            Af[b][0] = ld128(la + kofs); Af[b][1] = ld128(la + (256 - kofs));
#pragma unroll
            for (int n = 0; n < 5; ++n) Bf[b][n] = ld128(lb + n * 1024);
            sa = sa == RA - 1 ? 0 : sa + 1; sb = sb == RB - 1 ? 0 : sb + 1;
        };
        auto mm = [&](int b) {
#pragma unroll
            for (int n = 0; n < 5; ++n)
#pragma unroll
                for (int i = 0; i < 2; ++i) acc[i][n] = MFMA16(Bf[b][n], Af[b][i], acc[i][n]);
        };
        auto bar = [&]() { __builtin_amdgcn_sched_barrier(0); __builtin_amdgcn_s_waitcnt(0xC77F); asm volatile("" ::: "memory"); __builtin_amdgcn_s_barrier(); asm volatile("" ::: "memory"); __builtin_amdgcn_sched_barrier(0); };
        auto il = [&]() { SGB(0x8, 1); SGB(0x100, 2); SGB(0x8, 1); SGB(0x100, 2); SGB(0x8, 1); SGB(0x100, 2); SGB(0x8, 1); SGB(0x100, 1); SGB(0x8, 6); __builtin_amdgcn_sched_barrier(0); };
        bar(); STAMP(MODE ? 18 : 10); if (MODE) STAMPC(28); rd(0);
        bar(); rd(1);
        int s = 0;
#pragma unroll 1
        for (; s + 4 < NST; s += 3) {
            bar(); rd(2); mm(0); il();
            bar(); rd(0); mm(1); il();
            bar(); rd(1); mm(2); il();
        }
        if (NST % 3 == 0) { bar(); rd(2); mm(0); il(); mm(1); mm(2); }
        else { bar(); rd(2); mm(0); il(); bar(); rd(0); mm(1); il(); mm(2); mm(0); }
        STAMP(MODE ? 19 : 11); if (MODE) STAMPC(29);
        BAR_L();
        STAMP(MODE ? 46 : 45);
#pragma unroll
        for (int n = 0; n < 5; ++n) *(LAS f32x4*)(L + C::L_X + (wid * 5 + n) * 1024 + lane * 16) = acc[1][n];
        BAR_L();
        STAMP(MODE ? 31 : 30);
        const int row = 32 * wm + 16 * wk + r16;
        f32x4 pv[5]; u32x2 rr[5];
#pragma unroll
        for (int n = 0; n < 5; ++n) { pv[n] = *(const LAS f32x4*)(L + C::L_X + ((wid ^ 1) * 5 + n) * 1024 + lane * 16);
            rr[n] = MODE == 0 ? *(const LAS u32x2*)(L + C::L_RES + row * 160 + n * 32 + g * 8) : *(const LAS u32x2*)(L + C::L_RES + (2 * n + (g >> 1)) * 1024 + row * 16 + (g & 1) * 8); }
        u32x2 o[5]; f32x2 ps2 = {0.f, 0.f}, pq2 = {0.f, 0.f};
#pragma unroll
        for (int n = 0; n < 5; ++n) { const f32x4 v = acc[0][n] + pv[n];
            const f32x2 a = (f32x2){v[0], v[1]} + (f32x2){bflo(rr[n][0]), bfhi(rr[n][0])}, b = (f32x2){v[2], v[3]} + (f32x2){bflo(rr[n][1]), bfhi(rr[n][1])};
            o[n] = (u32x2){pk2(a[0], a[1]), pk2(b[0], b[1])};
            if (MODE == 0) { ps2 += a + b; pq2 += a * a + b * b; } }
        float ps = ps2[0] + ps2[1], pq = pq2[0] + pq2[1];
#pragma unroll
        for (int n = 0; n < 5; n += 2) { const int n1 = n + 1 < 5 ? n + 1 : n;
            auto s0 = __builtin_amdgcn_permlane16_swap(o[n][0], o[n1][0], false, false); auto s1 = __builtin_amdgcn_permlane16_swap(o[n][1], o[n1][1], false, false);
            const u32x4 v = {s0[0], s1[0], s0[1], s1[1]};
            const int ch = 2 * (n + (g & 1)) + (g >> 1);
            if (n + 1 < 5 || !(g & 1)) {
                if (MODE == 0) st16<FUSED>(p.ws + WS_H + (size_t)(10 * ct + ch) * 32768 + (size_t)(r0 + row) * 16, v);
                else *(u32x4*)(p.out + (size_t)(r0 + row) * D_ * 2 + ct * 160 + ch * 16) = v; } }
        if (MODE == 0) { ps = sum_x32(sum_x16(ps)); pq = sum_x32(sum_x16(pq));
            if (g == 0) { char* dst = p.ws + WS_PS + ((size_t)(ti * 8 + ct) * 128 + 64 * rh + row) * 8; const u64 v = ((u64)__float_as_uint(pq) << 32) | __float_as_uint(ps);
                if (FUSED) st_dev(dst, v); else *(u64*)dst = v; } }
    }
    STAMP(MODE ? 20 : 12);
    if (MODE == 0) publish<FUSED>(p, FL_C + (ti * 16 + tj) * FS, wid, lane, tag, 43);
}

namespace pd { constexpr int RB = 3, RA = 4, SB = 28672, LA = RB * SB, NST = 10, L_STAT = LA + RA * 16384, L_CF = L_STAT + 1024  , L_STG = LA; }
template <bool FUSED>
DI void d_bissue(const char* wb, unsigned l, int lane) { gl_lin7<FUSED>(lane * 16, wb, l); gl_lin7<FUSED>(lane * 16, wb + 7168, l + 7168); }
template <bool FUSED>
DI void d_prefetch(const Params& p, lptr L, int lane, int wid, int tj) {
    const char* wb = p.ws + WS_PWG + (size_t)tj * 286720 + (wid - 6) * 14336 + 3072;
    const unsigned l = (unsigned)(uintptr_t)L + (wid - 6) * 14336 + 3072;
#pragma unroll 1
    for (int s = 0; s < pd::RB; ++s) d_bissue<FUSED>(wb + (size_t)s * pd::SB, l + s * pd::SB, lane);
}
template <bool FUSED>
DI void phase_d(const Params& p, lptr L, int lane, int wid, int ti, int tj, u64 tag) {
    using namespace pd;
    const unsigned lds0 = (unsigned)(uintptr_t)L;
    if (wid >= 6) {
        const int lb = wid - 6;
        u32x2 ps[8]; float cp[2][16];
        const int row = 64 * lb + lane;
        const char* wb = p.ws + WS_PWG + (size_t)tj * 286720 + lb * 14336 + 3072 + (size_t)RB * SB;
        const unsigned l0 = lds0 + lb * 14336 + 3072; unsigned l = l0;
        auto issue = [&]() { d_bissue<FUSED>(wb, l, lane); wb += SB; l = l == l0 + (RB - 1) * SB ? l0 : l + SB; };
        static_assert(RB == 3 && NST == 10, "the waits below");
        BAR_VL(28);
#pragma unroll
        for (int c = 0; c < 8; ++c) ld64_async(ps[c], p.ws + WS_PS + ((size_t)(ti * 8 + c) * 128 + row) * 8);
        BAR_VL(22); issue();
#pragma unroll
        for (int i = 0; i < 16; ++i) ld32_async(cp[0][i], (const float*)(p.ws + WS_CSP) + (i * 16 + tj) * 224 + row);
        BAR_VL(38); issue();
#pragma unroll
        for (int i = 0; i < 16; ++i) ld32_async(cp[1][i], (const float*)(p.ws + WS_CSP) + (i * 16 + tj) * 224 + (row + 128 < 224 ? row + 128 : 223));
        BAR_VL(46); issue();
        BAR_VL(30); issue();
        BAR_VL(14); issue();
        { float a = 0.f, q = 0.f;
#pragma unroll
          for (int c = 0; c < 8; ++c) { asm volatile("" : "+v"(ps[c])); a += __uint_as_float(ps[c][0]); q += __uint_as_float(ps[c][1]); }
          *(LAS f32x2*)(L + L_STAT + row * 8) = ln_scalars(a, q);
#pragma unroll
          for (int z = 0; z < 2; ++z) { float sm = 0.f;
#pragma unroll
              for (int i = 0; i < 16; ++i) { asm volatile("" : "+v"(cp[z][i])); sm += cp[z][i]; }
              const int c = row + 128 * z;
              if (c < 224) { const float hi = rbf(sm); *(LAS u32x4*)(L + L_CF + (c >> 5) * 1024 + (c & 31) * 16) = (u32x4){pk2(hi, sm - hi), 0u, 0u, 0u};
                  *(LAS u32x4*)(L + L_CF + (c >> 5) * 1024 + 512 + (c & 31) * 16) = (u32x4){0u, 0u, 0u, 0u}; } } }
        BAR_VL(14); issue();
        BAR_VL(14); issue();
        BAR_VL(14);
        if (FUSED) {
            poll_flags(p.ws + WS_FLAG + FL_B + ((lane & 15) * 16 + (tj & ~1) + ((lane >> 4) & 1)) * FS, lane < 32, tag);
#pragma unroll 1
            for (int i = 0; i < 3; ++i) { int ln = (lb * 3 + i) * 64 + lane; ln = ln < 270 ? ln : 269;
                warm64(p.ws + WS_PWD + (size_t)(tj >> 1) * 276480 + (size_t)(2 * (ti & 3) + (tj & 1)) * 34560, (unsigned)ln * 128u, lds0); }
            BAR_VL(3);
        } else BAR_VL(0);
        BAR_L();
    } else if (wid >= 4) {
        const int la = wid - 4;
        if (FUSED) poll_flags(p.ws + WS_FLAG + FL_C + (ti * 16 + (lane & 15)) * FS, lane < 16, tag);
        STAMPW(13, 4);
        const char* ab = p.ws + WS_H + (size_t)(128 * ti + 64 * la) * 16 + 4096;
        unsigned vp[4];
#pragma unroll
        for (int i = 0; i < 4; ++i) vp[i] = lane * 16 + (unsigned)i * (32768 - 2048);
        const unsigned l0 = lds0 + LA + la * 1024 + 4096; unsigned l = l0;
        ring_loader<RA, NST, 8, 1, true>([&]() { gl_str4w<FUSED>(vp, ab, l); gl_str4w<FUSED>(vp, ab + 4 * 32768, l + 8192); ab += 8 * 32768; l = l == l0 + (RA - 1) * 16384 ? l0 : l + 16384; }, [&](int) {}, [&](int) {});
        BAR_L();
    } else {
        const int r32 = lane & 31, h = lane >> 5;
        const unsigned aA = (unsigned)(LA + h * 2048 + (32 * wid + r32) * 16), aB = (unsigned)(lane * 16);
        f32x16 acc[7];
#pragma unroll
        for (int t = 0; t < 7; ++t)
#pragma unroll
            for (int i = 0; i < 16; ++i) acc[t][i] = 0.f;
        bf16x8 A0, B0[7], A1, B1[7];
        auto rd = [&](bf16x8& Af, bf16x8* Bf, int sa, int sb, int u) {
            lcptr lb = (lcptr)L + aB + sb * SB + u * 7168;
            Af = ld128((lcptr)L + aA + sa * 16384 + u * 4096);
#pragma unroll
            for (int t = 0; t < 7; ++t) Bf[t] = ld128(lb + t * 1024);
        };
        auto mm = [&](const bf16x8& Af, const bf16x8* Bf) {
#pragma unroll
            for (int t = 0; t < 7; ++t) acc[t] = MFMA32(Bf[t], Af, acc[t]);
        };
        auto il = [&](const bf16x8& Af, const bf16x8* Bf) { SGB(0x8, 1); SGB(0x100, 2); SGB(0x8, 1); SGB(0x100, 2); SGB(0x8, 1); SGB(0x100, 2); SGB(0x8, 1); SGB(0x100, 2); SGB(0x8, 3); __builtin_amdgcn_sched_barrier(0);
            asm volatile("" :: "v"(Af), "v"(Bf[0]), "v"(Bf[1]), "v"(Bf[2]), "v"(Bf[3]), "v"(Bf[4]), "v"(Bf[5]), "v"(Bf[6])); __builtin_amdgcn_sched_barrier(0); };
        CBAR();
        STAMP(14); STAMPC(24);
        rd(A0, B0, 0, 0, 0);
        int sa = 0, sb = 0;
#pragma unroll 1
        for (int s = 0; s < NST - 1; ++s) {
            rd(A1, B1, sa, sb, 1); mm(A0, B0); il(A0, B0);
            rd(A0, B0, sa, sb, 2); mm(A1, B1); il(A1, B1);
            rd(A1, B1, sa, sb, 3); mm(A0, B0); il(A0, B0);
            sa = sa == RA - 1 ? 0 : sa + 1; sb = sb == RB - 1 ? 0 : sb + 1;
            CBAR();
            rd(A0, B0, sa, sb, 0); mm(A1, B1); il(A1, B1);
        }
        rd(A1, B1, sa, sb, 1); mm(A0, B0); il(A0, B0);
        rd(A0, B0, sa, sb, 2); mm(A1, B1); il(A1, B1);
        rd(A1, B1, sa, sb, 3); mm(A0, B0); il(A0, B0);
        mm(A1, B1);
        STAMP(15); STAMPC(25);
        BAR_L();
        STAMP(40);
        const int tok = 32 * wid + r32;
        const f32x2 st = *(const LAS f32x2*)(L + L_STAT + tok * 8);
        const float r2 = st.y * st.y;
        {
            bf16x8 cf[7];
#pragma unroll
            for (int t = 0; t < 7; ++t) cf[t] = ld128((lcptr)L + L_CF + t * 1024 + lane * 16);
            const bf16x8 mf = __builtin_bit_cast(bf16x8, ((u32x4){h ? 0u : pk2(-st.x, -st.x), 0u, 0u, 0u}));
#pragma unroll
            for (int t = 0; t < 7; ++t) acc[t] = MFMA32(cf[t], mf, acc[t]);
        }
        const f32x2 r22 = {r2, r2};
        auto glu4 = [&](float g0, float g1, float g2, float g3, float u0, float u1, float u2, float u3) {
            const f32x2 ya = (f32x2){u0, u1} * r22, yb = (f32x2){u2, u3} * r22;
            const f32x2 oa = (f32x2){__builtin_fmaxf(g0, 0.f), __builtin_fmaxf(g1, 0.f)} * ya, ob = (f32x2){__builtin_fmaxf(g2, 0.f), __builtin_fmaxf(g3, 0.f)} * yb;
            return (u32x2){pk2(oa[0], oa[1]), pk2(ob[0], ob[1])}; };
        char* dst = p.ws + WS_HH + (size_t)(14 * tj + h) * 32768 + (size_t)(128 * ti + tok) * 16;
        const int cmax = 216 - 14 * tj - h;
        auto put = [&](int c, u32x2 x, u32x2 y) {
            auto s0 = __builtin_amdgcn_permlane32_swap(x[0], y[0], false, false); auto s1 = __builtin_amdgcn_permlane32_swap(x[1], y[1], false, false);
            if (c < cmax) st16<FUSED>(dst + (size_t)c * 32768, (u32x4){s0[0], s1[0], s0[1], s1[1]}); };
#pragma unroll
        for (int a = 0; a < 3; ++a)
#pragma unroll
            for (int u = 0; u < 4; u += 2)
                put(4 * a + u, glu4(acc[2 * a][4 * u], acc[2 * a][4 * u + 1], acc[2 * a][4 * u + 2], acc[2 * a][4 * u + 3], acc[2 * a + 1][4 * u], acc[2 * a + 1][4 * u + 1], acc[2 * a + 1][4 * u + 2], acc[2 * a + 1][4 * u + 3]),
                    glu4(acc[2 * a][4 * u + 4], acc[2 * a][4 * u + 5], acc[2 * a][4 * u + 6], acc[2 * a][4 * u + 7], acc[2 * a + 1][4 * u + 4], acc[2 * a + 1][4 * u + 5], acc[2 * a + 1][4 * u + 6], acc[2 * a + 1][4 * u + 7]));
        put(12, glu4(acc[6][0], acc[6][1], acc[6][2], acc[6][3], acc[6][8], acc[6][9], acc[6][10], acc[6][11]), glu4(acc[6][4], acc[6][5], acc[6][6], acc[6][7], acc[6][12], acc[6][13], acc[6][14], acc[6][15]));
        STAMP(41);
    }
    STAMP(16);
    publish<FUSED>(p, FL_D + (ti * 16 + tj) * FS, wid, lane, tag, 44);
}

#define ROTA 2
#define ROTC 0
#define ROTD 0
#define ROTE 1
#define PROLOG() \
    extern __shared__ __attribute__((aligned(16))) char smem[]; \
    lptr L = (lptr)smem; \
    const int tid = threadIdx.x, lane = tid & 63, wid = __builtin_amdgcn_readfirstlane(tid >> 6); \
    const int xcd = blockIdx.x & 7, idx = blockIdx.x >> 3; \
    const int ti = 4 * (xcd & 3) + (idx & 3), tj = 8 * (xcd >> 2) + (idx >> 2); \
    const int rot10 = ((idx >> 2) + 3 * (idx & 3)) % 10, rot27 = ((idx >> 2) * 3 + (idx & 3) * 7) % 27; \
    const int rotC = ROTC ? rot10 : 0, rotD = ROTD ? rot10 : 0, rotE = ROTE ? rot27 : 0, rotA = ROTA == 1 ? rot10 : ROTA == 2 ? (((idx >> 2) + (idx & 3)) & 3) : ROTA == 3 ? ((idx >> 2) & 1) * 5 : 0; \
    (void)rot10; (void)rot27; (void)tid; (void)rotA; (void)rotC; (void)rotD; (void)rotE

__global__ __launch_bounds__(512, 2) void k_fused(const Params p) {
    PROLOG();
    STAMP(0);
    u64 tag = 0; u32x2 ev;
    ld64_async(ev, p.ws + WS_FLAG + FL_E);
    {
        const int n = (idx * 8 + wid) % TLB_WARM;
        const int u = 64 * n + lane;
        constexpr int NWS = (int)((WS_END + 65535) / 65536);
        if (wid < 4 || tj >= 15) {
        if (64 * n < NWS) warm64(p.ws, (unsigned)(u < NWS ? u : NWS - 1) * 65536u, (unsigned)(uintptr_t)L);
        else { const int v = u - ((NWS + 63) / 64) * 64; const int k = v / 40, o = (v % 40) * 65536;
            const char* b = k == 0 ? p.out : k == 1 ? (const char*)p.wo : k == 2 ? (const char*)p.wg : k == 3 ? (const char*)p.wu : (const char*)p.wd;
            const int sz = k == 0 ? S_ * D_ * 2 : k == 1 ? D_ * D_ * 2 : D_ * F_ * 2;
            const char* a = b + (o < sz - 4 ? o : sz - 4);
            asm volatile("s_mov_b32 m0, %1\n\ts_nop 0\n\tglobal_load_lds_dword %0, off" :: "v"(a), "s"((unsigned)(uintptr_t)L + (unsigned)(LDS_BYTES - 256)) : "memory"); }
        }
    }
    if (wid < 4) {
        const char* pc = (const char*)(__builtin_amdgcn_s_getpc() & ~127ull);
        int ln = ((idx * 4 + wid) % 6) * 64 + lane; ln = ln < CODE_LINES ? ln : CODE_LINES - 1;
        warm64(pc, (unsigned)ln * 128u, (unsigned)(uintptr_t)L);
    }
    if (tj >= 15) phase_a_stats<true>(p, L, lane, wid, ti, ev, tag); else phase_a<true>(p, L, lane, wid, ti, tj, rotA, ev, tag);
    if (tid == 0) st_dev(p.ws + WS_FLAG + FL_START + blockIdx.x * 8, tag);
    phase_b<true>(p, L, lane, wid, ti, tj, tag);
    if (wid >= 6) {
        poll_flags(p.ws + WS_FLAG + FL_P + ((lane & 15) * 16 + (lane < 16 ? tj : (tj & ~1) + ((lane >> 4) & 1))) * FS, lane < 48, tag);
        ce_prefetch<true, 0>(p, L, lane, wid, tj);
    }
    STAMP(8);
    phase_ce<true, 0>(p, L, lane, wid, ti, tj, tag);
    if (wid >= 6) d_prefetch<true>(p, L, lane, wid, tj);
    if (blockIdx.x == 0 && wid == 5) {
        for (unsigned spins = 0; spins < (1u << 17); ++spins) {
            bool ok = true;
#pragma unroll
            for (int i = 0; i < 4; ++i) ok &= ld_dev(p.ws + WS_FLAG + FL_START + (lane * 4 + i) * 8) == tag;
            if (__all(ok)) break;
            __builtin_amdgcn_s_sleep(1);
        }
        if (lane == 0) st_dev(p.ws + WS_FLAG + FL_E, tag);
    }
    phase_d<true>(p, L, lane, wid, ti, tj, tag);
    if (wid >= 6) ce_prefetch<true, 1>(p, L, lane, wid, tj);
    phase_ce<true, 1>(p, L, lane, wid, ti, tj, tag);
    STAMP(21);
}

extern "C" void kernel_launch(void* const* d_in, const int* in_sizes, int n_in, void* d_out, int out_size, void* d_ws, size_t ws_size, hipStream_t stream) {
    Params p;
    p.x = (const u16*)d_in[0]; p.wqkv = (const u16*)d_in[1]; p.wo = (const u16*)d_in[2];
    p.wg = (const u16*)d_in[4]; p.wu = (const u16*)d_in[5]; p.wd = (const u16*)d_in[6];
    p.out = (char*)d_out; p.ws = (char*)d_ws;
    (void)hipFuncSetAttribute((const void*)k_fused, hipFuncAttributeMaxDynamicSharedMemorySize, LDS_BYTES);
    k_fused<<<256, 512, LDS_BYTES, stream>>>(p);
}
```

```cpp
#include <hip/hip_runtime.h>
#include <hip/hip_bf16.h>
#include <stdint.h>

#define OUT_SC1 false
#define LAS __attribute__((address_space(3)))
#define DI __device__ __forceinline__
typedef unsigned short u16;
typedef unsigned long long u64;
typedef short bf16x8 __attribute__((ext_vector_type(8)));
typedef short s16x4 __attribute__((ext_vector_type(4)));
typedef float f32x2 __attribute__((ext_vector_type(2)));
typedef float f32x4 __attribute__((ext_vector_type(4)));
typedef float f32x16 __attribute__((ext_vector_type(16)));
typedef unsigned u32x2 __attribute__((ext_vector_type(2)));
typedef unsigned u32x4 __attribute__((ext_vector_type(4)));
typedef __bf16 bf16x2_t __attribute__((ext_vector_type(2)));
typedef LAS char* lptr;
typedef LAS const char* lcptr;

static constexpr int S_ = 2048, D_ = 640, H_ = 32, DH_ = 20, F_ = 1728;
static constexpr size_t WS_FLAG = 0;
#define FSTR 128
static constexpr size_t FS = FSTR;
static constexpr size_t FL_E = 0, FL_START = 1024, FL_A = 4096, FL_B = FL_A + 256 * FS, FL_C = FL_B + 256 * FS, FL_D = FL_C + 256 * FS, FL_P = FL_D + 256 * FS, FL_END = FL_P + 256 * FS;
static constexpr size_t WS_QKV = 262144;
static constexpr size_t WS_O = WS_QKV + (size_t)480 * 16384;
static constexpr size_t WS_H = WS_O + (size_t)80 * 32768;
static constexpr size_t WS_HH = WS_H + (size_t)80 * 32768;
static constexpr size_t WS_CSP = WS_HH + (size_t)216 * 32768;
static constexpr size_t WS_PS = WS_CSP + (size_t)16 * 16 * 224 * 4;
static constexpr size_t WS_S1 = WS_PS + (size_t)16 * 8 * 128 * 8;
static constexpr size_t WS_PWO = WS_S1 + (size_t)2048 * 8;
static constexpr size_t WS_PWD = WS_PWO + (size_t)8 * 20 * 5 * 1024;
static constexpr size_t WS_PWG = WS_PWD + (size_t)8 * 54 * 5 * 1024;
static constexpr size_t WS_END = WS_PWG + (size_t)16 * 40 * 7 * 1024;
static constexpr int LDS_BYTES = 163840;
static constexpr int CODE_LINES = 344;
static constexpr float C2_ = 0.22360679774997896f * 1.4426950408889634f;

struct Params { const u16* x; const u16* wqkv; const u16* wo; const u16* wg; const u16* wu; const u16* wd; char* out; char* ws; };

#define STAMP(i) do {} while (0)
#define STAMPC(i) do {} while (0)
#define STAMPW(i, w) do {} while (0)

DI unsigned pk2(float a, float b) { f32x2 v = {a, b}; return __builtin_bit_cast(unsigned, __builtin_convertvector(v, bf16x2_t)); }
DI float bflo(unsigned u) { return __uint_as_float(u << 16); }
DI float bfhi(unsigned u) { return __uint_as_float(u & 0xffff0000u); }
DI float rbf(float a) { return bflo(pk2(a, 0.f) & 0xffffu); }
template <bool SC1> DI void glds16(unsigned voff, const void* sbase, unsigned ldsaddr) {
    unsigned keep;
    if (SC1) asm volatile("s_mov_b32 %0, m0\n\ts_mov_b32 m0, %3\n\ts_nop 0\n\tglobal_load_lds_dwordx4 %1, %2 sc1\n\ts_mov_b32 m0, %0" : "=&s"(keep) : "v"(voff), "s"(sbase), "s"(ldsaddr) : "memory");
    else asm volatile("s_mov_b32 %0, m0\n\ts_mov_b32 m0, %3\n\ts_nop 0\n\tglobal_load_lds_dwordx4 %1, %2\n\ts_mov_b32 m0, %0" : "=&s"(keep) : "v"(voff), "s"(sbase), "s"(ldsaddr) : "memory");
}
#define GL_(v, off, sc) "global_load_lds_dwordx4 %" #v ", %[g] offset:" #off sc "\n\t"
#define GLHEAD "s_mov_b32 m0, %[l]\n\ts_nop 0\n\t"
template <bool SC1> DI void gl_lin5(unsigned v, const char* g, unsigned l) {
    if (SC1) asm volatile(GLHEAD GL_(0, -2048, " sc1") GL_(0, -1024, " sc1") GL_(0, 0, " sc1") GL_(0, 1024, " sc1") GL_(0, 2048, " sc1") :: "v"(v), [g] "s"(g), [l] "s"(l) : "memory");
    else asm volatile(GLHEAD GL_(0, -2048, "") GL_(0, -1024, "") GL_(0, 0, "") GL_(0, 1024, "") GL_(0, 2048, "") :: "v"(v), [g] "s"(g), [l] "s"(l) : "memory");
}
template <bool SC1> DI void gl_lin7(unsigned v, const char* g, unsigned l) {
    if (SC1) asm volatile(GLHEAD GL_(0, -3072, " sc1") GL_(0, -2048, " sc1") GL_(0, -1024, " sc1") GL_(0, 0, " sc1") GL_(0, 1024, " sc1") GL_(0, 2048, " sc1") GL_(0, 3072, " sc1") :: "v"(v), [g] "s"(g), [l] "s"(l) : "memory");
    else asm volatile(GLHEAD GL_(0, -3072, "") GL_(0, -2048, "") GL_(0, -1024, "") GL_(0, 0, "") GL_(0, 1024, "") GL_(0, 2048, "") GL_(0, 3072, "") :: "v"(v), [g] "s"(g), [l] "s"(l) : "memory");
}
template <bool SC1> DI void gl_str4(const unsigned* v, const char* g, unsigned l) {
    if (SC1) asm volatile(GLHEAD GL_(0, -2048, " sc1") GL_(1, -1024, " sc1") GL_(2, 0, " sc1") GL_(3, 1024, " sc1") :: "v"(v[0]), "v"(v[1]), "v"(v[2]), "v"(v[3]), [g] "s"(g), [l] "s"(l) : "memory");
    else asm volatile(GLHEAD GL_(0, -2048, "") GL_(1, -1024, "") GL_(2, 0, "") GL_(3, 1024, "") :: "v"(v[0]), "v"(v[1]), "v"(v[2]), "v"(v[3]), [g] "s"(g), [l] "s"(l) : "memory");
}
template <bool SC1> DI void gl_str4w(const unsigned* v, const char* g, unsigned l) {
    if (SC1) asm volatile(GLHEAD GL_(0, -4096, " sc1") GL_(1, -2048, " sc1") GL_(2, 0, " sc1") GL_(3, 2048, " sc1") :: "v"(v[0]), "v"(v[1]), "v"(v[2]), "v"(v[3]), [g] "s"(g), [l] "s"(l) : "memory");
    else asm volatile(GLHEAD GL_(0, -4096, "") GL_(1, -2048, "") GL_(2, 0, "") GL_(3, 2048, "") :: "v"(v[0]), "v"(v[1]), "v"(v[2]), "v"(v[3]), [g] "s"(g), [l] "s"(l) : "memory");
}
DI void gl_str8(const unsigned* v, const char* g, unsigned l) {
    asm volatile(GLHEAD GL_(0, -4096, "") GL_(1, -3072, "") GL_(2, -2048, "") GL_(3, -1024, "") GL_(4, 0, "") GL_(5, 1024, "") GL_(6, 2048, "") GL_(7, 3072, "")
                 :: "v"(v[0]), "v"(v[1]), "v"(v[2]), "v"(v[3]), "v"(v[4]), "v"(v[5]), "v"(v[6]), "v"(v[7]), [g] "s"(g), [l] "s"(l) : "memory");
}
template <bool SC1> DI void st16(void* ptr, u32x4 v) {
    if (SC1) asm volatile("global_store_dwordx4 %0, %1, off sc1\n\ts_nop 1" :: "v"(ptr), "v"(v) : "memory");
    else *(u32x4*)ptr = v;
}
DI u64 ld_dev(const void* p_) { return __hip_atomic_load((const u64*)p_, __ATOMIC_RELAXED, __HIP_MEMORY_SCOPE_AGENT); }
DI void st_dev(void* p_, u64 v) { __hip_atomic_store((u64*)p_, v, __ATOMIC_RELAXED, __HIP_MEMORY_SCOPE_AGENT); }
DI void ld64_async(u32x2& v, const void* ptr) { asm volatile("global_load_dwordx2 %0, %1, off sc1" : "=v"(v) : "v"(ptr) : "memory"); }
DI void ld32_async(float& v, const void* ptr) { asm volatile("global_load_dword %0, %1, off sc1" : "=v"(v) : "v"(ptr) : "memory"); }
DI void poll_flags(const void* flag, bool need, u64 tag) {
    for (unsigned spins = 0; spins < (1u << 18); ++spins) {
        const u64 v = need ? ld_dev(flag) : tag;
        if (__all(v == tag)) break;
    }
    asm volatile("" ::: "memory");
}
DI void warm64(const void* base, unsigned voff, unsigned lds0) {
    unsigned keep;
    asm volatile("s_mov_b32 %0, m0\n\ts_mov_b32 m0, %3\n\ts_nop 0\n\tglobal_load_lds_dword %1, %2 sc1\n\ts_mov_b32 m0, %0" : "=&s"(keep) : "v"(voff), "s"(base), "s"(lds0 + (unsigned)(LDS_BYTES - 256)) : "memory");
}
#define VMWAIT(N) asm volatile("s_waitcnt vmcnt(%0)" :: "i"(N) : "memory")
#define BAR_L() asm volatile("s_waitcnt lgkmcnt(0)\n\ts_barrier" ::: "memory")
#define BAR_VL(N) asm volatile("s_waitcnt vmcnt(%0) lgkmcnt(0)\n\ts_barrier" :: "i"(N) : "memory")
#define WAIT_LGKM0() do { __builtin_amdgcn_sched_barrier(0); __builtin_amdgcn_s_waitcnt(0xC07F); __builtin_amdgcn_sched_barrier(0); } while (0)
#define CBAR() do { WAIT_LGKM0(); asm volatile("" ::: "memory"); __builtin_amdgcn_s_barrier(); asm volatile("" ::: "memory"); __builtin_amdgcn_sched_barrier(0); } while (0)
#define SGB(mask, n) __builtin_amdgcn_sched_group_barrier(mask, n, 0)
template <int STEP, int MAXN> DI void bar_vl_n(int n) {
    if (n >= MAXN) { BAR_VL((MAXN * STEP > 63 ? 63 : MAXN * STEP)); }
    else if constexpr (MAXN > 0) bar_vl_n<STEP, MAXN - 1>(n);
}
template <int N> struct IC { static constexpr int v = N; };
template <int K, int END, class F> DI void static_for(F&& f) { if constexpr (K < END) { f(IC<K>{}); static_for<K + 1, END>(f); } }
template <int R, int NST, int NP, int LAG, bool PROLOGUE, int NEND = NST, class FI, class FP, class FQ>
DI void ring_loader(FI&& issue, FP&& pre, FQ&& post) {
    constexpr int L0 = (R < NST ? R : NST) - 1;
    if (PROLOGUE) {
#pragma unroll 1
        for (int k = 0; k <= L0; ++k) issue();
    }
    static_for<0, LAG + 1>([&](auto kc) { constexpr int k = decltype(kc)::v; BAR_VL((L0 - k) * NP); pre(k); if constexpr (k >= LAG && k - LAG + R < NST) issue(); post(k); });
    constexpr int KS = NST - R + LAG;
    if constexpr (KS > LAG + 1) {
#pragma unroll 1
        for (int k = LAG + 1; k < KS; ++k) { BAR_VL((R - 1 - LAG) * NP); pre(k); issue(); post(k); }
    }
    static_for<(KS > LAG + 1 ? KS : LAG + 1), NEND>([&](auto kc) { constexpr int k = decltype(kc)::v; BAR_VL((NST - 1 - k) * NP); pre(k); post(k); });
}
DI bf16x8 ld128(lcptr p) { return *(const LAS bf16x8*)p; }
DI s16x4 ld64(lcptr p) { return *(const LAS s16x4*)p; }
DI s16x4 ld64v(lcptr p) { return *(const volatile LAS s16x4*)p; }
DI s16x4 ldtr(lcptr p) { typedef short v4 __attribute__((ext_vector_type(4))); return __builtin_bit_cast(s16x4, __builtin_amdgcn_ds_read_tr16_b64_v4i16((LAS v4*)p)); }
DI bf16x8 cat8(s16x4 a, s16x4 b) { return __builtin_shufflevector(a, b, 0, 1, 2, 3, 4, 5, 6, 7); }
#define MFMA16(a, b, c) __builtin_amdgcn_mfma_f32_16x16x32_bf16(a, b, c, 0, 0, 0)
#define MFMA32(a, b, c) __builtin_amdgcn_mfma_f32_32x32x16_bf16(a, b, c, 0, 0, 0)
#define MFMA32K8(a, b, c) __builtin_amdgcn_mfma_f32_32x32x8bf16_1k(a, b, c, 0, 0, 0)
template <int CTRL> DI float dpp_add(float v) { return v + __int_as_float(__builtin_amdgcn_update_dpp(0, __float_as_int(v), CTRL, 0xf, 0xf, true)); }
DI float sum8(float v) { v = dpp_add<0xB1>(v); v = dpp_add<0x4E>(v); return dpp_add<0x141>(v); }
DI float sum_x16(float v) { auto r = __builtin_amdgcn_permlane16_swap(__float_as_uint(v), __float_as_uint(v), false, false); return __uint_as_float(r[0]) + __uint_as_float(r[1]); }
DI float sum_x32(float v) { auto r = __builtin_amdgcn_permlane32_swap(__float_as_uint(v), __float_as_uint(v), false, false); return __uint_as_float(r[0]) + __uint_as_float(r[1]); }
DI bf16x8 ones8() { bf16x8 o = {0x3f80, 0x3f80, 0x3f80, 0x3f80, 0x3f80, 0x3f80, 0x3f80, 0x3f80}; asm volatile("" : "+v"(o)); return o; }

DI f32x2 ln_scalars(float sum, float sq) {
    const float mean = sum * (1.0f / D_);
    float var = sq * (1.0f / D_) - mean * mean; var = var > 0.f ? var : 0.f;
    const float sd = rbf(__builtin_sqrtf(rbf(rbf(var) + 1e-5f)));
    return (f32x2){rbf(mean), 1.0f / sd};
}
template <bool FUSED> DI void publish(const Params& p, size_t flag_off, int wid, int lane, u64 tag, int sidx = 47) {
    if (FUSED) { BAR_VL(0); STAMP(sidx); if (wid == 0 && lane == 0) st_dev(p.ws + WS_FLAG + flag_off, tag); }
}

namespace rp { constexpr int L_G = 132608, L_U = L_G + 10752, L_O = L_G + 21504, L_D = 102400; }
DI void rp1_addr(const Params& p, int lane, int lw, int ti, int tj, const char* (&ga)[7]) {
    const int ns = ti < 8 ? 3 : 2, s0 = ti < 8 ? 3 * ti : 24 + 2 * (ti - 8);
    const int ct = tj >> 1, z = 2 * ti + (tj & 1);
#pragma unroll
    for (int i = 0; i < 7; ++i) { const int n = lw + 4 * i;
        if (n < 21) { int u = 64 * n + lane; const int mat = u >= 672; u -= 672 * mat; int row = u / 14; int col = 112 * tj + 8 * (u - 14 * row); col = col > F_ - 8 ? F_ - 8 : col;
            row = row < 16 * ns ? row : 16 * ns - 1;
            ga[i] = (const char*)((mat ? p.wu : p.wg) + (size_t)(16 * s0 + row) * F_ + col);
        } else { const int m = n < 26 ? n - 21 : 4, u = 64 * m + lane, row = u / 10, c = u - 10 * row;
            ga[i] = (const char*)(p.wo + (size_t)(32 * (z < 20 ? z : 0) + row) * D_ + 80 * ct + 8 * c); } }
}
DI void glds16v(const void* vaddr, unsigned ldsaddr) {
    asm volatile("s_nop 0\n\ts_mov_b32 m0, %1\n\ts_nop 0\n\tglobal_load_lds_dwordx4 %0, off" :: "v"(vaddr), "s"(ldsaddr) : "memory");
}
DI void rp1_issue(const char* (&ga)[7], unsigned lds0, int lw) {
#pragma unroll
    for (int i = 0; i < 7; ++i) { const int n = lw + 4 * i; glds16v(ga[i], (unsigned)__builtin_amdgcn_readfirstlane(lds0 + rp::L_G + (n < 26 ? n : 25) * 1024)); }
}
template <bool FUSED>
DI void rp1_store(const Params& p, lptr L, int lane, int lw, int ti, int tj, int part = 3) {
    const int ns = ti < 8 ? 3 : 2, s0 = ti < 8 ? 3 * ti : 24 + 2 * (ti - 8);
    const int ct = tj >> 1, z = 2 * ti + (tj & 1);
    const int h = lane >> 5, blk = (lane >> 4) & 1, g = lane >> 4, q = (lane & 15) >> 2, pp = lane & 3;
    const bf16x8 ones = ones8();
#pragma unroll 1
    for (int t = lw + (part == 1 ? 4 : 0); t < (part == 0 ? 4 : part == 2 ? 0 : 7); t += 4) {
        lcptr src = (lcptr)L + (t < 6 ? ((t & 1) ? rp::L_U : rp::L_G) + (32 * (t >> 1) + 16 * blk + 4 * pp) * 2 : (blk ? rp::L_U : rp::L_G) + (96 + 4 * pp) * 2) + (8 * h + q) * 224;
        s16x4 f[3][2];
#pragma unroll
        for (int s = 0; s < 3; ++s) { f[s][0] = ldtr(src + s * 16 * 224); f[s][1] = ldtr(src + s * 16 * 224 + 4 * 224); }
        f32x16 cs = {};
        char* dst = p.ws + WS_PWG + ((size_t)(tj * 40 + s0) * 7 + t) * 1024 + lane * 16;
#pragma unroll
        for (int s = 0; s < 3; ++s) if (s < ns) { const bf16x8 fr = cat8(f[s][0], f[s][1]); st16<FUSED>(dst + s * 7168, __builtin_bit_cast(u32x4, fr)); cs = MFMA32(fr, ones, cs); }
        if ((lane & 31) == 0) { float* cd = (float*)(p.ws + WS_CSP) + (ti * 16 + tj) * 224 + 32 * t + 4 * h;
#pragma unroll
            for (int u = 0; u < 4; ++u) st16<FUSED>(cd + 8 * u, __builtin_bit_cast(u32x4, ((f32x4){cs[4 * u], cs[4 * u + 1], cs[4 * u + 2], cs[4 * u + 3]}))); }
    }
    if (z < 20 && part >= 2) {
#pragma unroll 1
        for (int nt = 3 - lw; nt < 5; nt += 4) {
            lcptr src = (lcptr)L + rp::L_O + (8 * g + q) * 160 + (16 * nt + 4 * pp) * 2;
            st16<FUSED>(p.ws + WS_PWO + ((size_t)(ct * 20 + z) * 5 + nt) * 1024 + lane * 16, __builtin_bit_cast(u32x4, cat8(ldtr(src), ldtr(src + 4 * 160)))); }
    }
}
DI void rp2_issue(const Params& p, unsigned lds0, int lane, int wid, int ti, int tj) {
    const int ct = tj >> 1, z = 2 * ti + (tj & 1), np = z + 32 < 54 ? 10 : 5;
#pragma unroll 1
    for (int n = wid; n < np; n += 8) { const int im = n >= 5, u = 64 * (n - 5 * im) + lane, row = u / 10, c = u - 10 * row;
        glds16<false>((unsigned)((32 * (z + 32 * im) + row) * D_ + 80 * ct + 8 * c) * 2u, p.wd, lds0 + rp::L_D + n * 1024); }
}
template <bool FUSED>
DI void rp2_store(const Params& p, lptr L, int lane, int wid, int ti, int tj) {
    const int ct = tj >> 1, z = 2 * ti + (tj & 1), np = z + 32 < 54 ? 10 : 5;
    const int g = lane >> 4, q = (lane & 15) >> 2, pp = lane & 3;
#pragma unroll 1
    for (int n = wid; n < np; n += 8) { const int im = n >= 5, nt = n - 5 * im;
        lcptr src = (lcptr)L + rp::L_D + im * 5120 + (8 * g + q) * 160 + (16 * nt + 4 * pp) * 2;
        st16<FUSED>(p.ws + WS_PWD + ((size_t)(ct * 54 + z + 32 * im) * 5 + nt) * 1024 + lane * 16, __builtin_bit_cast(u32x4, cat8(ldtr(src), ldtr(src + 4 * 160)))); }
}
DI u64 make_tag(u32x2& ev) {
    asm volatile("" : "+v"(ev));
    const u64 e = (((u64)(unsigned)__builtin_amdgcn_readfirstlane(ev[1]) << 32) | (unsigned)__builtin_amdgcn_readfirstlane(ev[0])) + 1;
    return e;
}

namespace pa { constexpr int R = 4, NST = 10, LA = 0, LB = R * 16384, L_STAT = 2 * R * 16384, L_CS = L_STAT + 1024; }
template <bool FUSED>
DI void phase_a_stats(const Params& p, lptr L, int lane, int wid, int ti, u32x2& ev, u64& tag) {
    const int r = lane >> 3, c = lane & 7;
    if (wid >= 4) { const char* ga[7]; rp1_addr(p, lane, wid - 4, ti, 15, ga); rp1_issue(ga, (unsigned)(uintptr_t)L, wid - 4); }
#pragma unroll
    for (int hlf = 0; hlf < 2; ++hlf) {
        const int row = 128 * ti + 16 * wid + 8 * hlf + r;
        const u32x4* src = (const u32x4*)(p.x + (size_t)row * D_) + c;
        u32x4 v[10];
#pragma unroll
        for (int i = 0; i < 10; ++i) v[i] = src[8 * i];
        float a = 0.f, q = 0.f;
#pragma unroll
        for (int i = 0; i < 10; ++i)
#pragma unroll
            for (int e = 0; e < 4; ++e) { const float lo = bflo(v[i][e]), hi = bfhi(v[i][e]); a += lo + hi; q += lo * lo + hi * hi; }
        const f32x2 st = ln_scalars(sum8(a), sum8(q));
        if (c == 0) { const u64 w = ((u64)__float_as_uint(st.y) << 32) | __float_as_uint(st.x); char* dst = p.ws + WS_S1 + (size_t)row * 8; if (FUSED) st_dev(dst, w); else *(u64*)dst = w; }
    }
    BAR_VL(0);
    if (FUSED) { tag = make_tag(ev); if (wid == 0 && lane == 0) st_dev(p.ws + WS_FLAG + FL_A + (ti * 16 + 15) * FS, tag); }
    if (wid >= 4) rp1_store<FUSED>(p, L, lane, wid - 4, ti, 15);
    if (FUSED) { BAR_VL(0); if (wid == 0 && lane == 0) st_dev(p.ws + WS_FLAG + FL_P + (ti * 16 + 15) * FS, tag); }
}
template <bool FUSED>
DI void phase_a(const Params& p, lptr L, int lane, int wid, int ti, int tj, int rot, u32x2& ev, u64& tag) {
    using namespace pa;
    const unsigned lds0 = (unsigned)(uintptr_t)L;
    if (wid >= 4) {
        const int lw = wid - 4, r = lane >> 3, c = lane & 7;
        const bool isA = lw < 2;
        unsigned vo[2];
#pragma unroll
        for (int e = 0; e < 2; ++e) vo[e] = isA ? (unsigned)(r * D_ + ((c ^ ((4 * e + (r >> 1)) & 7)) * 8)) * 2u : (unsigned)(r * 3 * D_ + ((c ^ (4 * ((r >> 1) & 1) + 2 * e)) * 8)) * 2u;
        unsigned vp[8];
#pragma unroll
        for (int i = 0; i < 8; ++i) vp[i] = vo[i & 1] + (isA ? (unsigned)i * (8 * D_ * 2 - 1024) : (unsigned)(i & 3) * (8 * 3 * D_ * 2 - 1024));
        const char* gb = isA ? (const char*)p.x + (size_t)(128 * ti + 64 * lw) * D_ * 2 + 4096 + rot * 128 : (const char*)p.wqkv + ((size_t)(32 * (lw - 2) + 64 * rot) * 3 * D_ + 128 * tj) * 2 + 2048;
        const int gstep = isA ? 128 : 64 * 3 * D_ * 2;
        int ks = rot; unsigned sl = lds0 + (isA ? LA + lw * 8192 + 4096 : LB + (lw - 2) * 4096 + 2048), sslot = 0;
        auto issue = [&]() {
            if (isA) gl_str8(vp, gb, sl + sslot); else { gl_str4<false>(vp, gb, sl + sslot); gl_str4<false>(vp, gb + 128, sl + sslot + 8192); }
            gb += gstep; if (++ks == NST) { ks = 0; gb -= (size_t)NST * gstep; }
            sslot = (sslot + 16384) & (R * 16384 - 1);
        };
        const int g = lane >> 4, q = (lane & 15) >> 2, pp = lane & 3;
        unsigned aC[2];
#pragma unroll
        for (int t = 0; t < 2; ++t) aC[t] = (unsigned)(LB + ((lw >> 1) * 8 + g) * 1024 + q * 128 + (((2 * (2 * (lw & 1) + t) + (pp >> 1)) ^ (4 * ((q >> 1) & 1) + 2 * (g & 1))) * 16) + (pp & 1) * 8);
        f32x4 cs[2] = {{0.f, 0.f, 0.f, 0.f}, {0.f, 0.f, 0.f, 0.f}};
        const bf16x8 ones = ones8();
        s16x4 cf[2][2][2];
        __builtin_amdgcn_s_setprio(3);
        auto pre = [&](int s) { lcptr q0 = (lcptr)L + (s & 3) * 16384;
#pragma unroll
            for (int ks2 = 0; ks2 < 2; ++ks2)
#pragma unroll
                for (int t = 0; t < 2; ++t) { cf[ks2][t][0] = ldtr(q0 + aC[t] + ks2 * 4096); cf[ks2][t][1] = ldtr(q0 + aC[t] + ks2 * 4096 + 512); } };
        auto post = [&](int) {
#pragma unroll
            for (int ks2 = 0; ks2 < 2; ++ks2)
#pragma unroll
                for (int t = 0; t < 2; ++t) cs[t] = MFMA16(cat8(cf[ks2][t][0], cf[ks2][t][1]), ones, cs[t]); };
        issue(); issue();
        __builtin_amdgcn_sched_barrier(0);
        const char* ga[7]; rp1_addr(p, lane, lw, ti, tj, ga);
        BAR_VL(8); pre(0); issue(); rp1_issue(ga, lds0, lw);
        if (FUSED) tag = make_tag(ev);
        BAR_VL(15); post(0); pre(1); issue();
        BAR_VL(15); post(1); pre(2); issue();
        BAR_VL(8); post(2); pre(3); issue(); rp1_store<FUSED>(p, L, lane, lw, ti, tj, 0);
        BAR_VL(14); post(3); pre(4); issue(); rp1_store<FUSED>(p, L, lane, lw, ti, tj, 1);
        if (lw < 3) BAR_VL(20); else BAR_VL(14);
        post(4); pre(5); issue(); rp1_store<FUSED>(p, L, lane, lw, ti, tj, 2);
#pragma unroll 1
        for (int k = 6; k < NST - 2; ++k) { BAR_VL(8); post(k - 1); pre(k); issue(); }
        BAR_VL(8); post(NST - 3); pre(NST - 2);
        BAR_VL(0); post(NST - 2); pre(NST - 1); post(NST - 1);
        if (isA) {
            if (FUSED) poll_flags(p.ws + WS_FLAG + FL_A + (ti * 16 + 15) * FS, lane == 0, tag);
            const char* src = p.ws + WS_S1 + (size_t)(128 * ti + 64 * lw + lane) * 8;
            const u64 w = FUSED ? ld_dev(src) : *(const u64*)src;
            *(LAS u64*)(L + L_STAT + (64 * lw + lane) * 8) = w;
        }
        __builtin_amdgcn_s_setprio(0);
        if ((lane & 15) == 0) {
#pragma unroll
            for (int t = 0; t < 2; ++t) *(LAS f32x4*)(L + L_CS + (32 * lw + 16 * t + 4 * g) * 4) = cs[t];
        }
        BAR_L();
        BAR_L();
    } else {
        const int wm = wid >> 1, wn = wid & 1, r32 = lane & 31, h = lane >> 5, blk = (lane >> 4) & 1, q = (lane & 15) >> 2, pp = lane & 3;
        unsigned aX[4], aW[2];
        { const int row = 64 * wm + r32;
#pragma unroll
          for (int u = 0; u < 4; ++u) aX[u] = (unsigned)(LA + (row >> 3) * 1024 + (row & 7) * 128 + (((2 * u + h) ^ ((row >> 1) & 7)) * 16));
#pragma unroll
          for (int nt = 0; nt < 2; ++nt) aW[nt] = (unsigned)(LB + (wn * 8 + h) * 1024 + q * 128 + (((4 * nt + 2 * blk + (pp >> 1)) ^ (4 * ((q >> 1) & 1) + 2 * h)) * 16) + (pp & 1) * 8); }
        f32x16 acc[2][2];
#pragma unroll
        for (int i = 0; i < 16; ++i) { acc[0][0][i] = 0.f; acc[0][1][i] = 0.f; acc[1][0][i] = 0.f; acc[1][1][i] = 0.f; }
        bf16x8 X[4][2], W[4][2];
        auto rd = [&](int slot, int u) {
            lcptr sl = (lcptr)L + slot * 16384;
#pragma unroll
            for (int nt = 0; nt < 2; ++nt) W[u][nt] = cat8(ldtr(sl + aW[nt] + u * 2048), ldtr(sl + aW[nt] + u * 2048 + 512));
#pragma unroll
            for (int mt = 0; mt < 2; ++mt) X[u][mt] = ld128(sl + aX[u] + mt * 4096);
        };
        auto mm = [&](int u) {
#pragma unroll
            for (int mt = 0; mt < 2; ++mt)
#pragma unroll
                for (int nt = 0; nt < 2; ++nt) acc[mt][nt] = MFMA32(W[u][nt], X[u][mt], acc[mt][nt]);
        };
        auto il = [&]() { SGB(0x8, 1); SGB(0x100, 2); SGB(0x8, 1); SGB(0x100, 2); SGB(0x8, 1); SGB(0x100, 2); SGB(0x8, 1); __builtin_amdgcn_sched_barrier(0); };
        auto bar = [&]() { __builtin_amdgcn_sched_barrier(0); asm volatile("" ::: "memory"); __builtin_amdgcn_s_barrier(); asm volatile("" ::: "memory"); __builtin_amdgcn_sched_barrier(0); };
        bar();
        if (FUSED) VMWAIT(0);
        STAMP(1); STAMPC(26);
        rd(0, 0); rd(0, 1);
#pragma unroll 1
        for (int s = 0; s < NST - 1; ++s) {
            const int c = s & 3, n = (s + 1) & 3;
            rd(c, 2); mm(0); il();
            rd(c, 3); mm(1); il();
            bar();
            rd(n, 0); mm(2); il();
            rd(n, 1); mm(3); il();
        }
        rd((NST - 1) & 3, 2); mm(0); il();
        rd((NST - 1) & 3, 3); mm(1); il();
        mm(2); mm(3);
        STAMP(2); STAMPC(27);
        if (FUSED) tag = make_tag(ev);
        BAR_L();
        STAMP(38);
        {
            f32x2 st[2]; float cv[2];
#pragma unroll
            for (int i = 0; i < 2; ++i) { st[i] = *(const LAS f32x2*)(L + L_STAT + (64 * wm + 32 * i + r32) * 8); cv[i] = *(const LAS float*)(L + L_CS + (64 * wn + 32 * i + r32) * 4); }
            bf16x8 cf[2], mf[2];
#pragma unroll
            for (int i = 0; i < 2; ++i) { const float hi = rbf(cv[i]); const unsigned c = h ? 0u : pk2(hi, cv[i] - hi), m = h ? 0u : pk2(-st[i].x, -st[i].x);
                cf[i] = __builtin_bit_cast(bf16x8, ((u32x4){c, 0u, 0u, 0u})); mf[i] = __builtin_bit_cast(bf16x8, ((u32x4){m, 0u, 0u, 0u})); }
#pragma unroll
            for (int mt = 0; mt < 2; ++mt)
#pragma unroll
                for (int nt = 0; nt < 2; ++nt) acc[mt][nt] = MFMA32(cf[nt], mf[mt], acc[mt][nt]);
#pragma unroll
            for (int mt = 0; mt < 2; ++mt) { const int tok = 64 * wm + 32 * mt + r32; const float sq = st[mt].y * C2_;
#pragma unroll
                for (int nt = 0; nt < 2; ++nt)
#pragma unroll
                    for (int u = 0; u < 4; ++u) { const int col = 64 * wn + 32 * nt + 8 * u + 4 * h;
                        const float sc = (128 * tj + col < D_) ? sq : st[mt].y;
                        *(LAS u32x2*)(L + (col >> 2) * 1024 + tok * 8) = (u32x2){pk2(acc[mt][nt][4 * u] * sc, acc[mt][nt][4 * u + 1] * sc), pk2(acc[mt][nt][4 * u + 2] * sc, acc[mt][nt][4 * u + 3] * sc)}; } }
        }
        BAR_L();
        STAMP(39);
    }
    char* dst = p.ws + WS_QKV + (size_t)(32 * tj) * 16384 + (size_t)ti * 1024;
#pragma unroll
    for (int i = 0; i < 4; ++i) { const int gq = 4 * wid + i; st16<FUSED>(dst + (size_t)gq * 16384 + lane * 16, *(const LAS u32x4*)(L + gq * 1024 + lane * 16)); }
    STAMP(3);
    publish<FUSED>(p, FL_A + (ti * 16 + tj) * FS, wid, lane, tag, 42);
    if (FUSED && wid == 1 && lane == 0) st_dev(p.ws + WS_FLAG + FL_P + (ti * 16 + tj) * FS, tag);
}

namespace pb {
constexpr int GS = 3104;
constexpr int L_K = 0, L_V = 10 * GS, L_ONE = 20 * GS, L_Q = 21 * GS, L_O = 102400;
static_assert(L_Q + 10240 <= L_O, "LDS map");
}
#define ATT_STAMP() do {} while (0)
template <bool EDGE>
DI f32x16 attn_wave(lcptr L, int hh, int qb, int lane, int tstart, u64* stq) {
    using namespace pb;
    const int r = lane & 31, h = lane >> 5;
    bf16x8 q0; s16x4 q1;
    { lcptr qp = L + L_Q + hh * 5 * 1024 + (32 * qb + r) * 8;
      q0 = cat8(ld64(qp + (2 * h) * 1024), ld64(qp + (2 * h + 1) * 1024));
      const s16x4 t = ld64(qp + 4 * 1024); q1 = h ? (s16x4){0, 0, 0, 0} : t; }
    lcptr kp = L + L_K + hh * 5 * GS + (32 * qb + r) * 8;
    const f32x16 z16 = {};
    bf16x8 k0[9]; s16x4 k1[9];
#pragma unroll
    for (int t = 0; t < 9; ++t) { k0[t] = cat8(ld64v(kp + (2 * h) * GS + t * 256), ld64v(kp + (2 * h + 1) * GS + t * 256)); k1[t] = ld64v(kp + 4 * GS + t * 256); }
    float m;
    { f32x16 a = MFMA32K8(k1[8], q1, MFMA32(k0[8], q0, z16)), b = MFMA32K8(k1[0], q1, MFMA32(k0[0], q0, z16));
      const float bias = (!EDGE || tstart == 0) ? 0.f : -INFINITY;
#pragma unroll
      for (int i = 0; i < 16; ++i) { const int c = (i & 3) + 8 * (i >> 2) + 4 * h; a[i] = (c <= r) ? a[i] : (EDGE ? b[i] + bias : b[i]); }
      float x = __builtin_fmaxf(__builtin_fmaxf(a[0], a[1]), a[2]), y = __builtin_fmaxf(__builtin_fmaxf(a[3], a[4]), a[5]);
#pragma unroll
      for (int i = 6; i < 14; i += 4) { x = __builtin_fmaxf(__builtin_fmaxf(x, a[i]), a[i + 1]); y = __builtin_fmaxf(__builtin_fmaxf(y, a[i + 2]), a[i + 3]); }
      m = __builtin_fmaxf(__builtin_fmaxf(__builtin_fmaxf(x, a[14]), a[15]), y); }
#pragma unroll
    for (int t = 1; t < 8; ++t) { const f32x16 a = MFMA32K8(k1[t], q1, MFMA32(k0[t], q0, z16));
        float x = __builtin_fmaxf(__builtin_fmaxf(a[0], a[1]), a[2]), y = __builtin_fmaxf(__builtin_fmaxf(a[3], a[4]), a[5]);
#pragma unroll
        for (int i = 6; i < 14; i += 4) { x = __builtin_fmaxf(__builtin_fmaxf(x, a[i]), a[i + 1]); y = __builtin_fmaxf(__builtin_fmaxf(y, a[i + 2]), a[i + 3]); }
        x = __builtin_fmaxf(__builtin_fmaxf(x, a[14]), a[15]);
        if (EDGE) x = t >= tstart ? x : -INFINITY;
        m = __builtin_fmaxf(__builtin_fmaxf(m, x), (EDGE && t < tstart) ? -INFINITY : y); }
    { auto rr = __builtin_amdgcn_permlane32_swap(__float_as_uint(m), __float_as_uint(m), false, false); m = __builtin_fmaxf(__uint_as_float(rr[0]), __uint_as_float(rr[1])); }
    f32x16 nm16;
#pragma unroll
    for (int i = 0; i < 16; ++i) nm16[i] = -m;
    ATT_STAMP();
    const int blk = (lane >> 4) & 1, vq = (lane & 15) >> 2, vp = lane & 3, dg = 4 * blk + vp;
    lcptr vpz = L + (dg < 5 ? L_V + (hh * 5 + dg) * GS : L_ONE) + (32 * qb + 4 * h + vq) * 8;
    f32x16 O = z16;
#pragma unroll
    for (int t = 1; t < 8; ++t) {
        const bf16x8 v0 = cat8(ldtr(vpz + t * 256), ldtr(vpz + t * 256 + 64)), v1 = cat8(ldtr(vpz + t * 256 + 128), ldtr(vpz + t * 256 + 192));
        f32x16 a = MFMA32K8(k1[t], q1, MFMA32(k0[t], q0, nm16));
        if (EDGE) { const float bias = t >= tstart ? 0.f : -INFINITY;
#pragma unroll
            for (int i = 0; i < 16; ++i) a[i] += bias; }
        unsigned P[8];
#pragma unroll
        for (int i = 0; i < 8; ++i) P[i] = pk2(__builtin_amdgcn_exp2f(a[2 * i]), __builtin_amdgcn_exp2f(a[2 * i + 1]));
        O = MFMA32(v0, __builtin_bit_cast(bf16x8, ((u32x4){P[0], P[1], P[2], P[3]})), O);
        O = MFMA32(v1, __builtin_bit_cast(bf16x8, ((u32x4){P[4], P[5], P[6], P[7]})), O);
    }
    { const bf16x8 v0 = cat8(ldtr(vpz + 8 * 256), ldtr(vpz + 8 * 256 + 64)), v1 = cat8(ldtr(vpz + 8 * 256 + 128), ldtr(vpz + 8 * 256 + 192));
      const bf16x8 w0 = cat8(ldtr(vpz), ldtr(vpz + 64)), w1 = cat8(ldtr(vpz + 128), ldtr(vpz + 192));
      f32x16 a = MFMA32K8(k1[8], q1, MFMA32(k0[8], q0, nm16)); const f32x16 b = MFMA32K8(k1[0], q1, MFMA32(k0[0], q0, nm16));
      const float bias = (!EDGE || tstart == 0) ? 0.f : -INFINITY;
#pragma unroll
      for (int i = 0; i < 16; ++i) { const int c = (i & 3) + 8 * (i >> 2) + 4 * h; a[i] = (c <= r) ? a[i] : (EDGE ? b[i] + bias : b[i]); }
      unsigned P[8], P0[8];
#pragma unroll
      for (int i = 0; i < 8; ++i) { P[i] = pk2(__builtin_amdgcn_exp2f(a[2 * i]), __builtin_amdgcn_exp2f(a[2 * i + 1]));
          const int c0 = ((2 * i) & 3) + 8 * ((2 * i) >> 2) + 4 * h; const unsigned mk = (c0 <= r ? 0xffffu : 0u) | (c0 + 1 <= r ? 0xffff0000u : 0u);
          P0[i] = P[i] & ~mk; P[i] &= mk; }
      O = MFMA32(v0, __builtin_bit_cast(bf16x8, ((u32x4){P[0], P[1], P[2], P[3]})), O);
      O = MFMA32(v1, __builtin_bit_cast(bf16x8, ((u32x4){P[4], P[5], P[6], P[7]})), O);
      O = MFMA32(w0, __builtin_bit_cast(bf16x8, ((u32x4){P0[0], P0[1], P0[2], P0[3]})), O);
      O = MFMA32(w1, __builtin_bit_cast(bf16x8, ((u32x4){P0[4], P0[5], P0[6], P0[7]})), O); }
    return O;
}
template <bool FUSED>
DI void phase_b(const Params& p, lptr L, int lane, int wid, int ti, int tj, u64 tag) {
    using namespace pb;
    const int tid = wid * 64 + lane;
    const unsigned lds0 = (unsigned)(uintptr_t)L;
    rp2_issue(p, lds0, lane, wid, ti, tj);
    if (ti < 2) {
        for (int n = tid; n < (2 - ti) * 20 * 64; n += 512) { const int pc = n >> 6, mb = pc % (2 - ti), gq = pc / (2 - ti);
            *(LAS u32x4*)(L + gq * GS + mb * 1024 + (n & 63) * 16) = (u32x4){0u, 0u, 0u, 0u}; }
    }
    if (tid < 384) *(LAS u32x2*)(L + L_ONE + tid * 8) = (u32x2){0x00003f80u, 0u};
    const int sub = wid & 3, mtile = sub < 3 ? ti - 2 + sub : ti;
    const char* g0 = p.ws + WS_QKV + (size_t)((sub < 3 ? 160 : 0) + 5 * (2 * tj + (wid >> 2))) * 16384 + (size_t)(mtile < 0 ? 0 : mtile) * 1024;
    const unsigned l0 = lds0 + (sub < 3 ? L_K + (wid >> 2) * 5 * GS + sub * 1024 : L_Q + (wid >> 2) * 5120);
    BAR_VL(0);
    rp2_store<FUSED>(p, L, lane, wid, ti, tj);
    if (FUSED) {
        if (wid == 7) { const int mb = lane / 6, sec = (lane % 6) >> 1, e = lane & 1, mt2 = ti - 2 + mb;
            const bool need = lane < 18 && mt2 >= 0 && (sec > 0 || mb == 2);
            poll_flags(p.ws + WS_FLAG + FL_A + ((mt2 < 0 ? 0 : mt2) * 16 + ((640 * sec + 40 * tj + 39 * e) >> 7)) * FS, need, tag); STAMPW(35, 7); }
    }
    BAR_L();
    STAMP(4);
    if (sub == 3) {
#pragma unroll
        for (int d = 0; d < 5; ++d) glds16<FUSED>(lane * 16, g0 + d * 16384, l0 + d * 1024);
        BAR_VL(0);
    } else if (mtile >= 0) {
#pragma unroll
        for (int d = 0; d < 5; ++d) glds16<FUSED>(lane * 16, g0 + d * 16384, l0 + d * GS);
#pragma unroll
        for (int d = 0; d < 5; ++d) glds16<FUSED>(lane * 16, g0 + (160 + d) * 16384, l0 + L_V + d * GS);
        BAR_VL(0);
    } else BAR_L();
    STAMP(5);
    const int hh = wid >> 2, qb = wid & 3, r = lane & 31, h = lane >> 5;
    const int q0g = 128 * ti + 32 * qb;
    f32x16 O;
    u64* stq = nullptr;
    if (ti >= 2) O = attn_wave<false>((lcptr)L, hh, qb, lane, 0, stq); else O = attn_wave<true>((lcptr)L, hh, qb, lane, q0g >= 256 ? 0 : 8 - (q0g >> 5), stq);
    { auto rr = __builtin_amdgcn_permlane32_swap(__float_as_uint(O[8]), __float_as_uint(O[8]), false, false);
      const float il = 1.0f / __uint_as_float(rr[1]);
      const int row = 32 * qb + r;
#pragma unroll
      for (int u = 0; u < 3; ++u) { const int dgp = 2 * u + h; if (dgp < 5) { const int cgp = 5 * hh + dgp;
          *(LAS u32x2*)(L + L_O + (cgp >> 1) * 2048 + row * 16 + (cgp & 1) * 8) = (u32x2){pk2(O[4 * u] * il, O[4 * u + 1] * il), pk2(O[4 * u + 2] * il, O[4 * u + 3] * il)}; } } }
    STAMP(6); STAMPW(37, 7);
    BAR_L();
    for (int n = wid; n < 10; n += 8)
        st16<FUSED>(p.ws + WS_O + (size_t)(5 * tj + (n >> 1)) * 32768 + (size_t)(128 * ti + 64 * (n & 1)) * 16 + lane * 16, *(const LAS u32x4*)(L + L_O + n * 1024 + lane * 16));
    STAMP(7);
    publish<FUSED>(p, FL_B + (ti * 16 + tj) * FS, wid, lane, tag);
}

template <int MODE> struct PC;
template <> struct PC<0> { static constexpr int NST = 10, RB = 10, RA = 6, LA = 102400, L_RES = 151552, L_X = 102400, L_STG = 122880; };
template <> struct PC<1> { static constexpr int NST = 27, RB = 8, RA = 8, LA = 81920, L_RES = 147456, L_X = 81920, L_STG = 102400; };
template <bool FUSED, int MODE>
DI void ce_prefetch(const Params& p, lptr L, int lane, int wid, int tj) {
    const char* wb = p.ws + (MODE ? WS_PWD + (size_t)(tj >> 1) * 54 * 5120 : WS_PWO + (size_t)(tj >> 1) * 20 * 5120) + (wid - 6) * 5120 + 2048;
    const unsigned l = (unsigned)(uintptr_t)L + (wid - 6) * 5120 + 2048;
#pragma unroll 1
    for (int s = 0; s < PC<MODE>::RB; ++s) gl_lin5<FUSED>(lane * 16, wb + (size_t)s * 10240, l + s * 10240);
}
template <bool FUSED, int MODE>
DI void phase_ce(const Params& p, lptr L, int lane, int wid, int ti, int tj, u64 tag) {
    typedef PC<MODE> C;
    constexpr int NST = C::NST, RA = C::RA, RB = C::RB;
    const unsigned lds0 = (unsigned)(uintptr_t)L;
    const int rh = tj & 1, ct = tj >> 1, r0 = 128 * ti + 64 * rh;
    const int wm = (wid >> 1) & 1, wk = wid & 1, r16 = lane & 15, g = lane >> 4;
    if (wid >= 6) {
        if (MODE == 0) {
            bar_vl_n<5, RB - 1>(RB - 1);
            BAR_VL(0);
#pragma unroll 1
            for (int s = 2; s < NST; ++s) BAR_L();
        } else {
            const char* wb = p.ws + WS_PWD + (size_t)ct * 54 * 5120 + (wid - 6) * 5120 + 2048 + (size_t)RB * 10240;
            const unsigned l0 = lds0 + (wid - 6) * 5120 + 2048; unsigned l = l0;
            ring_loader<RB, NST, 5, 2, false>([&]() { gl_lin5<FUSED>(lane * 16, wb, l); wb += 10240; l = l == l0 + (RB - 1) * 10240 ? l0 : l + 10240; }, [&](int) {}, [&](int) {});
        }
        BAR_L(); BAR_L();
    } else if (wid >= 4) {
        const int la = wid - 4;
        const unsigned voA = lane * 16;
#pragma unroll
        for (int i = 0; i < 5; ++i) { const int pc = 5 * la + i;
            if (MODE == 0) { const int n = 64 * pc + lane; glds16<false>((unsigned)((n / 10) * D_ * 2 + (n % 10) * 16), (const char*)p.x + (size_t)r0 * D_ * 2 + ct * 160, lds0 + C::L_RES + pc * 1024); }
            else glds16<FUSED>(voA, p.ws + WS_H + (size_t)(10 * ct + pc) * 32768 + (size_t)r0 * 16, lds0 + C::L_RES + pc * 1024); }
        if (FUSED) poll_flags(p.ws + WS_FLAG + (MODE ? FL_D : FL_B) + (ti * 16 + (lane & 15)) * FS, lane < 16, tag);
        STAMPW(MODE ? 17 : 9, 4);
        const char* ab = p.ws + (MODE ? WS_HH : WS_O) + (size_t)r0 * 16 + (size_t)(4 * la) * 32768 + 2048;
        unsigned vp[4];
#pragma unroll
        for (int i = 0; i < 4; ++i) vp[i] = voA + (unsigned)i * (32768 - 1024);
        const unsigned l0 = lds0 + C::LA + la * 4096 + 2048; unsigned l = l0;
        ring_loader<RA, NST, 4, 2, true>([&]() { gl_str4<FUSED>(vp, ab, l); ab += 8 * 32768; l = l == l0 + (RA - 1) * 8192 ? l0 : l + 8192; }, [&](int) {}, [&](int) {});
        BAR_L(); BAR_L();
    } else {
        const unsigned aA = (unsigned)(C::LA + (4 * wk + g) * 1024 + (32 * wm + r16) * 16), aB = (unsigned)(wk * 5120 + lane * 16);
        f32x4 acc[2][5];
#pragma unroll
        for (int i = 0; i < 2; ++i)
#pragma unroll
            for (int n = 0; n < 5; ++n) acc[i][n] = (f32x4){0.f, 0.f, 0.f, 0.f};
        bf16x8 Af[3][2], Bf[3][5];
        const int kofs = wk * 256;
        int sa = 0, sb = 0;
        auto rd = [&](int b) {
            lcptr la = (lcptr)L + aA + sa * 8192; lcptr lb = (lcptr)L + aB + sb * 10240;
            Af[b][0] = ld128(la + kofs); Af[b][1] = ld128(la + (256 - kofs));
#pragma unroll
            for (int n = 0; n < 5; ++n) Bf[b][n] = ld128(lb + n * 1024);
            sa = sa == RA - 1 ? 0 : sa + 1; sb = sb == RB - 1 ? 0 : sb + 1;
        };
        auto mm = [&](int b) {
#pragma unroll
            for (int n = 0; n < 5; ++n)
#pragma unroll
                for (int i = 0; i < 2; ++i) acc[i][n] = MFMA16(Bf[b][n], Af[b][i], acc[i][n]);
        };
        auto bar = [&]() { __builtin_amdgcn_sched_barrier(0); __builtin_amdgcn_s_waitcnt(0xC77F); asm volatile("" ::: "memory"); __builtin_amdgcn_s_barrier(); asm volatile("" ::: "memory"); __builtin_amdgcn_sched_barrier(0); };
        auto il = [&]() { SGB(0x8, 1); SGB(0x100, 2); SGB(0x8, 1); SGB(0x100, 2); SGB(0x8, 1); SGB(0x100, 2); SGB(0x8, 1); SGB(0x100, 1); SGB(0x8, 6); __builtin_amdgcn_sched_barrier(0); };
        bar(); STAMP(MODE ? 18 : 10); if (MODE) STAMPC(28); rd(0);
        bar(); rd(1);
        int s = 0;
#pragma unroll 1
        for (; s + 4 < NST; s += 3) {
            bar(); rd(2); mm(0); il();
            bar(); rd(0); mm(1); il();
            bar(); rd(1); mm(2); il();
        }
        if (NST % 3 == 0) { bar(); rd(2); mm(0); il(); mm(1); mm(2); }
        else { bar(); rd(2); mm(0); il(); bar(); rd(0); mm(1); il(); mm(2); mm(0); }
        STAMP(MODE ? 19 : 11); if (MODE) STAMPC(29);
        BAR_L();
        STAMP(MODE ? 46 : 45);
#pragma unroll
        for (int n = 0; n < 5; ++n) *(LAS f32x4*)(L + C::L_X + (wid * 5 + n) * 1024 + lane * 16) = acc[1][n];
        BAR_L();
        STAMP(MODE ? 31 : 30);
        const int row = 32 * wm + 16 * wk + r16;
        f32x4 pv[5]; u32x2 rr[5];
#pragma unroll
        for (int n = 0; n < 5; ++n) { pv[n] = *(const LAS f32x4*)(L + C::L_X + ((wid ^ 1) * 5 + n) * 1024 + lane * 16);
            rr[n] = MODE == 0 ? *(const LAS u32x2*)(L + C::L_RES + row * 160 + n * 32 + g * 8) : *(const LAS u32x2*)(L + C::L_RES + (2 * n + (g >> 1)) * 1024 + row * 16 + (g & 1) * 8); }
        u32x2 o[5]; f32x2 ps2 = {0.f, 0.f}, pq2 = {0.f, 0.f};
#pragma unroll
        for (int n = 0; n < 5; ++n) { const f32x4 v = acc[0][n] + pv[n];
            const f32x2 a = (f32x2){v[0], v[1]} + (f32x2){bflo(rr[n][0]), bfhi(rr[n][0])}, b = (f32x2){v[2], v[3]} + (f32x2){bflo(rr[n][1]), bfhi(rr[n][1])};
            o[n] = (u32x2){pk2(a[0], a[1]), pk2(b[0], b[1])};
            if (MODE == 0) { ps2 += a + b; pq2 += a * a + b * b; } }
        float ps = ps2[0] + ps2[1], pq = pq2[0] + pq2[1];
#pragma unroll
        for (int n = 0; n < 5; n += 2) { const int n1 = n + 1 < 5 ? n + 1 : n;
            auto s0 = __builtin_amdgcn_permlane16_swap(o[n][0], o[n1][0], false, false); auto s1 = __builtin_amdgcn_permlane16_swap(o[n][1], o[n1][1], false, false);
            const u32x4 v = {s0[0], s1[0], s0[1], s1[1]};
            const int ch = 2 * (n + (g & 1)) + (g >> 1);
            if (n + 1 < 5 || !(g & 1)) {
                if (MODE == 0) st16<FUSED>(p.ws + WS_H + (size_t)(10 * ct + ch) * 32768 + (size_t)(r0 + row) * 16, v);
                else st16<OUT_SC1>(p.out + (size_t)(r0 + row) * D_ * 2 + ct * 160 + ch * 16, v); } }
        if (MODE == 0) { ps = sum_x32(sum_x16(ps)); pq = sum_x32(sum_x16(pq));
            if (g == 0) { char* dst = p.ws + WS_PS + ((size_t)(ti * 8 + ct) * 128 + 64 * rh + row) * 8; const u64 v = ((u64)__float_as_uint(pq) << 32) | __float_as_uint(ps);
                if (FUSED) st_dev(dst, v); else *(u64*)dst = v; } }
    }
    STAMP(MODE ? 20 : 12);
    if (MODE == 0) publish<FUSED>(p, FL_C + (ti * 16 + tj) * FS, wid, lane, tag, 43);
}

namespace pd { constexpr int RB = 3, RA = 4, SB = 28672, LA = RB * SB, NST = 10, L_STAT = LA + RA * 16384, L_CF = L_STAT + 1024  , L_STG = LA; }
template <bool FUSED>
DI void d_bissue(const char* wb, unsigned l, int lane) { gl_lin7<FUSED>(lane * 16, wb, l); gl_lin7<FUSED>(lane * 16, wb + 7168, l + 7168); }
template <bool FUSED>
DI void d_prefetch(const Params& p, lptr L, int lane, int wid, int tj) {
    const char* wb = p.ws + WS_PWG + (size_t)tj * 286720 + (wid - 6) * 14336 + 3072;
    const unsigned l = (unsigned)(uintptr_t)L + (wid - 6) * 14336 + 3072;
#pragma unroll 1
    for (int s = 0; s < pd::RB; ++s) d_bissue<FUSED>(wb + (size_t)s * pd::SB, l + s * pd::SB, lane);
}
template <bool FUSED>
DI void phase_d(const Params& p, lptr L, int lane, int wid, int ti, int tj, u64 tag) {
    using namespace pd;
    const unsigned lds0 = (unsigned)(uintptr_t)L;
    if (wid >= 6) {
        const int lb = wid - 6;
        u32x2 ps[8]; float cp[2][16];
        const int row = 64 * lb + lane;
        const char* wb = p.ws + WS_PWG + (size_t)tj * 286720 + lb * 14336 + 3072 + (size_t)RB * SB;
        const unsigned l0 = lds0 + lb * 14336 + 3072; unsigned l = l0;
        auto issue = [&]() { d_bissue<FUSED>(wb, l, lane); wb += SB; l = l == l0 + (RB - 1) * SB ? l0 : l + SB; };
        static_assert(RB == 3 && NST == 10, "the waits below");
        BAR_VL(28);
#pragma unroll
        for (int c = 0; c < 8; ++c) ld64_async(ps[c], p.ws + WS_PS + ((size_t)(ti * 8 + c) * 128 + row) * 8);
        BAR_VL(22); issue();
#pragma unroll
        for (int i = 0; i < 16; ++i) ld32_async(cp[0][i], (const float*)(p.ws + WS_CSP) + (i * 16 + tj) * 224 + row);
        BAR_VL(38); issue();
#pragma unroll
        for (int i = 0; i < 16; ++i) ld32_async(cp[1][i], (const float*)(p.ws + WS_CSP) + (i * 16 + tj) * 224 + (row + 128 < 224 ? row + 128 : 223));
        BAR_VL(46); issue();
        BAR_VL(30); issue();
        BAR_VL(14); issue();
        { float a = 0.f, q = 0.f;
#pragma unroll
          for (int c = 0; c < 8; ++c) { asm volatile("" : "+v"(ps[c])); a += __uint_as_float(ps[c][0]); q += __uint_as_float(ps[c][1]); }
          *(LAS f32x2*)(L + L_STAT + row * 8) = ln_scalars(a, q);
#pragma unroll
          for (int z = 0; z < 2; ++z) { float sm = 0.f;
#pragma unroll
              for (int i = 0; i < 16; ++i) { asm volatile("" : "+v"(cp[z][i])); sm += cp[z][i]; }
              const int c = row + 128 * z;
              if (c < 224) { const float hi = rbf(sm); *(LAS u32x4*)(L + L_CF + (c >> 5) * 1024 + (c & 31) * 16) = (u32x4){pk2(hi, sm - hi), 0u, 0u, 0u};
                  *(LAS u32x4*)(L + L_CF + (c >> 5) * 1024 + 512 + (c & 31) * 16) = (u32x4){0u, 0u, 0u, 0u}; } } }
        BAR_VL(14); issue();
        BAR_VL(14); issue();
        BAR_VL(14);
        if (FUSED) {
            poll_flags(p.ws + WS_FLAG + FL_B + ((lane & 15) * 16 + (tj & ~1) + ((lane >> 4) & 1)) * FS, lane < 32, tag);
#pragma unroll 1
            for (int i = 0; i < 3; ++i) { int ln = (lb * 3 + i) * 64 + lane; ln = ln < 270 ? ln : 269;
                warm64(p.ws + WS_PWD + (size_t)(tj >> 1) * 276480 + (size_t)(2 * (ti & 3) + (tj & 1)) * 34560, (unsigned)ln * 128u, lds0); }
            BAR_VL(3);
        } else BAR_VL(0);
        BAR_L();
    } else if (wid >= 4) {
        const int la = wid - 4;
        if (FUSED) poll_flags(p.ws + WS_FLAG + FL_C + (ti * 16 + (lane & 15)) * FS, lane < 16, tag);
        STAMPW(13, 4);
        const char* ab = p.ws + WS_H + (size_t)(128 * ti + 64 * la) * 16 + 4096;
        unsigned vp[4];
#pragma unroll
        for (int i = 0; i < 4; ++i) vp[i] = lane * 16 + (unsigned)i * (32768 - 2048);
        const unsigned l0 = lds0 + LA + la * 1024 + 4096; unsigned l = l0;
        ring_loader<RA, NST, 8, 1, true>([&]() { gl_str4w<FUSED>(vp, ab, l); gl_str4w<FUSED>(vp, ab + 4 * 32768, l + 8192); ab += 8 * 32768; l = l == l0 + (RA - 1) * 16384 ? l0 : l + 16384; }, [&](int) {}, [&](int) {});
        BAR_L();
    } else {
        const int r32 = lane & 31, h = lane >> 5;
        const unsigned aA = (unsigned)(LA + h * 2048 + (32 * wid + r32) * 16), aB = (unsigned)(lane * 16);
        f32x16 acc[7];
#pragma unroll
        for (int t = 0; t < 7; ++t)
#pragma unroll
            for (int i = 0; i < 16; ++i) acc[t][i] = 0.f;
        bf16x8 A0, B0[7], A1, B1[7];
        auto rd = [&](bf16x8& Af, bf16x8* Bf, int sa, int sb, int u) {
            lcptr lb = (lcptr)L + aB + sb * SB + u * 7168;
            Af = ld128((lcptr)L + aA + sa * 16384 + u * 4096);
#pragma unroll
            for (int t = 0; t < 7; ++t) Bf[t] = ld128(lb + t * 1024);
        };
        auto mm = [&](const bf16x8& Af, const bf16x8* Bf) {
#pragma unroll
            for (int t = 0; t < 7; ++t) acc[t] = MFMA32(Bf[t], Af, acc[t]);
        };
        auto il = [&](const bf16x8& Af, const bf16x8* Bf) { SGB(0x8, 1); SGB(0x100, 2); SGB(0x8, 1); SGB(0x100, 2); SGB(0x8, 1); SGB(0x100, 2); SGB(0x8, 1); SGB(0x100, 2); SGB(0x8, 3); __builtin_amdgcn_sched_barrier(0);
            asm volatile("" :: "v"(Af), "v"(Bf[0]), "v"(Bf[1]), "v"(Bf[2]), "v"(Bf[3]), "v"(Bf[4]), "v"(Bf[5]), "v"(Bf[6])); __builtin_amdgcn_sched_barrier(0); };
        CBAR();
        STAMP(14); STAMPC(24);
        rd(A0, B0, 0, 0, 0);
        int sa = 0, sb = 0;
#pragma unroll 1
        for (int s = 0; s < NST - 1; ++s) {
            rd(A1, B1, sa, sb, 1); mm(A0, B0); il(A0, B0);
            rd(A0, B0, sa, sb, 2); mm(A1, B1); il(A1, B1);
            rd(A1, B1, sa, sb, 3); mm(A0, B0); il(A0, B0);
            sa = sa == RA - 1 ? 0 : sa + 1; sb = sb == RB - 1 ? 0 : sb + 1;
            CBAR();
            rd(A0, B0, sa, sb, 0); mm(A1, B1); il(A1, B1);
        }
        rd(A1, B1, sa, sb, 1); mm(A0, B0); il(A0, B0);
        rd(A0, B0, sa, sb, 2); mm(A1, B1); il(A1, B1);
        rd(A1, B1, sa, sb, 3); mm(A0, B0); il(A0, B0);
        mm(A1, B1);
        STAMP(15); STAMPC(25);
        BAR_L();
        STAMP(40);
        const int tok = 32 * wid + r32;
        const f32x2 st = *(const LAS f32x2*)(L + L_STAT + tok * 8);
        const float r2 = st.y * st.y;
        {
            bf16x8 cf[7];
#pragma unroll
            for (int t = 0; t < 7; ++t) cf[t] = ld128((lcptr)L + L_CF + t * 1024 + lane * 16);
            const bf16x8 mf = __builtin_bit_cast(bf16x8, ((u32x4){h ? 0u : pk2(-st.x, -st.x), 0u, 0u, 0u}));
#pragma unroll
            for (int t = 0; t < 7; ++t) acc[t] = MFMA32(cf[t], mf, acc[t]);
        }
        const f32x2 r22 = {r2, r2};
        auto glu4 = [&](float g0, float g1, float g2, float g3, float u0, float u1, float u2, float u3) {
            const f32x2 ya = (f32x2){u0, u1} * r22, yb = (f32x2){u2, u3} * r22;
            const f32x2 oa = (f32x2){__builtin_fmaxf(g0, 0.f), __builtin_fmaxf(g1, 0.f)} * ya, ob = (f32x2){__builtin_fmaxf(g2, 0.f), __builtin_fmaxf(g3, 0.f)} * yb;
            return (u32x2){pk2(oa[0], oa[1]), pk2(ob[0], ob[1])}; };
        char* dst = p.ws + WS_HH + (size_t)(14 * tj + h) * 32768 + (size_t)(128 * ti + tok) * 16;
        const int cmax = 216 - 14 * tj - h;
        auto put = [&](int c, u32x2 x, u32x2 y) {
            auto s0 = __builtin_amdgcn_permlane32_swap(x[0], y[0], false, false); auto s1 = __builtin_amdgcn_permlane32_swap(x[1], y[1], false, false);
            if (c < cmax) st16<FUSED>(dst + (size_t)c * 32768, (u32x4){s0[0], s1[0], s0[1], s1[1]}); };
#pragma unroll
        for (int a = 0; a < 3; ++a)
#pragma unroll
            for (int u = 0; u < 4; u += 2)
                put(4 * a + u, glu4(acc[2 * a][4 * u], acc[2 * a][4 * u + 1], acc[2 * a][4 * u + 2], acc[2 * a][4 * u + 3], acc[2 * a + 1][4 * u], acc[2 * a + 1][4 * u + 1], acc[2 * a + 1][4 * u + 2], acc[2 * a + 1][4 * u + 3]),
                    glu4(acc[2 * a][4 * u + 4], acc[2 * a][4 * u + 5], acc[2 * a][4 * u + 6], acc[2 * a][4 * u + 7], acc[2 * a + 1][4 * u + 4], acc[2 * a + 1][4 * u + 5], acc[2 * a + 1][4 * u + 6], acc[2 * a + 1][4 * u + 7]));
        put(12, glu4(acc[6][0], acc[6][1], acc[6][2], acc[6][3], acc[6][8], acc[6][9], acc[6][10], acc[6][11]), glu4(acc[6][4], acc[6][5], acc[6][6], acc[6][7], acc[6][12], acc[6][13], acc[6][14], acc[6][15]));
        STAMP(41);
    }
    STAMP(16);
    publish<FUSED>(p, FL_D + (ti * 16 + tj) * FS, wid, lane, tag, 44);
}

#define ROTA 2
#define ROTC 0
#define ROTD 0
#define ROTE 1
#define PROLOG() \
    extern __shared__ __attribute__((aligned(16))) char smem[]; \
    lptr L = (lptr)smem; \
    const int tid = threadIdx.x, lane = tid & 63, wid = __builtin_amdgcn_readfirstlane(tid >> 6); \
    const int xcd = blockIdx.x & 7, idx = blockIdx.x >> 3; \
    const int ti = 4 * (xcd & 3) + (idx & 3), tj = 8 * (xcd >> 2) + (idx >> 2); \
    const int rot10 = ((idx >> 2) + 3 * (idx & 3)) % 10, rot27 = ((idx >> 2) * 3 + (idx & 3) * 7) % 27; \
    const int rotC = ROTC ? rot10 : 0, rotD = ROTD ? rot10 : 0, rotE = ROTE ? rot27 : 0, rotA = ROTA == 1 ? rot10 : ROTA == 2 ? (((idx >> 2) + (idx & 3)) & 3) : ROTA == 3 ? ((idx >> 2) & 1) * 5 : 0; \
    (void)rot10; (void)rot27; (void)tid; (void)rotA; (void)rotC; (void)rotD; (void)rotE

__global__ __launch_bounds__(512, 2) void k_fused(const Params p) {
    PROLOG();
    STAMP(0);
    u64 tag = 0; u32x2 ev;
    ld64_async(ev, p.ws + WS_FLAG + FL_E);
    if (wid < 4) {
        const char* pc = (const char*)(__builtin_amdgcn_s_getpc() & ~127ull);
        int ln = ((idx * 4 + wid) % 6) * 64 + lane; ln = ln < CODE_LINES ? ln : CODE_LINES - 1;
        warm64(pc, (unsigned)ln * 128u, (unsigned)(uintptr_t)L);
    }
    if (tj >= 15) phase_a_stats<true>(p, L, lane, wid, ti, ev, tag); else phase_a<true>(p, L, lane, wid, ti, tj, rotA, ev, tag);
    if (tid == 0) st_dev(p.ws + WS_FLAG + FL_START + blockIdx.x * 8, tag);
    phase_b<true>(p, L, lane, wid, ti, tj, tag);
    if (wid >= 6) {
        poll_flags(p.ws + WS_FLAG + FL_P + ((lane & 15) * 16 + (lane < 16 ? tj : (tj & ~1) + ((lane >> 4) & 1))) * FS, lane < 48, tag);
        ce_prefetch<true, 0>(p, L, lane, wid, tj);
    }
    STAMP(8);
    phase_ce<true, 0>(p, L, lane, wid, ti, tj, tag);
    if (wid >= 6) d_prefetch<true>(p, L, lane, wid, tj);
    if (blockIdx.x == 0 && wid == 5) {
        for (unsigned spins = 0; spins < (1u << 17); ++spins) {
            bool ok = true;
#pragma unroll
            for (int i = 0; i < 4; ++i) ok &= ld_dev(p.ws + WS_FLAG + FL_START + (lane * 4 + i) * 8) == tag;
            if (__all(ok)) break;
            __builtin_amdgcn_s_sleep(1);
        }
        if (lane == 0) st_dev(p.ws + WS_FLAG + FL_E, tag);
    }
    phase_d<true>(p, L, lane, wid, ti, tj, tag);
    if (wid >= 6) ce_prefetch<true, 1>(p, L, lane, wid, tj);
    phase_ce<true, 1>(p, L, lane, wid, ti, tj, tag);
    STAMP(21);
}

extern "C" void kernel_launch(void* const* d_in, const int* in_sizes, int n_in, void* d_out, int out_size, void* d_ws, size_t ws_size, hipStream_t stream) {
    Params p;
    p.x = (const u16*)d_in[0]; p.wqkv = (const u16*)d_in[1]; p.wo = (const u16*)d_in[2];
    p.wg = (const u16*)d_in[4]; p.wu = (const u16*)d_in[5]; p.wd = (const u16*)d_in[6];
    p.out = (char*)d_out; p.ws = (char*)d_ws;
    (void)hipFuncSetAttribute((const void*)k_fused, hipFuncAttributeMaxDynamicSharedMemorySize, LDS_BYTES);
    k_fused<<<256, 512, LDS_BYTES, stream>>>(p);
}
```

```cpp
#include <hip/hip_runtime.h>
#include <hip/hip_bf16.h>
#include <stdint.h>

#define OUT_SC1 false
#define LAS __attribute__((address_space(3)))
#define DI __device__ __forceinline__
typedef unsigned short u16;
typedef unsigned long long u64;
typedef short bf16x8 __attribute__((ext_vector_type(8)));
typedef short s16x4 __attribute__((ext_vector_type(4)));
typedef float f32x2 __attribute__((ext_vector_type(2)));
typedef float f32x4 __attribute__((ext_vector_type(4)));
typedef float f32x16 __attribute__((ext_vector_type(16)));
typedef unsigned u32x2 __attribute__((ext_vector_type(2)));
typedef unsigned u32x4 __attribute__((ext_vector_type(4)));
typedef __bf16 bf16x2_t __attribute__((ext_vector_type(2)));
typedef LAS char* lptr;
typedef LAS const char* lcptr;

static constexpr int S_ = 2048, D_ = 640, H_ = 32, DH_ = 20, F_ = 1728;
static constexpr size_t WS_FLAG = 0;
#define FSTR 128
static constexpr size_t FS = FSTR;
static constexpr size_t FL_E = 0, FL_START = 1024, FL_A = 4096, FL_B = FL_A + 256 * FS, FL_C = FL_B + 256 * FS, FL_D = FL_C + 256 * FS, FL_P = FL_D + 256 * FS, FL_END = FL_P + 256 * FS;
static constexpr size_t WS_QKV = 262144;
static constexpr size_t WS_O = WS_QKV + (size_t)480 * 16384;
static constexpr size_t WS_H = WS_O + (size_t)80 * 32768;
static constexpr size_t WS_HH = WS_H + (size_t)80 * 32768;
static constexpr size_t WS_CSP = WS_HH + (size_t)216 * 32768;
static constexpr size_t WS_PS = WS_CSP + (size_t)16 * 16 * 224 * 4;
static constexpr size_t WS_S1 = WS_PS + (size_t)16 * 8 * 128 * 8;
static constexpr size_t WS_PWO = WS_S1 + (size_t)2048 * 8;
static constexpr size_t WS_PWD = WS_PWO + (size_t)8 * 20 * 5 * 1024;
static constexpr size_t WS_PWG = WS_PWD + (size_t)8 * 54 * 5 * 1024;
static constexpr size_t WS_END = WS_PWG + (size_t)16 * 40 * 7 * 1024;
static constexpr int LDS_BYTES = 163840;
static constexpr float C2_ = 0.22360679774997896f * 1.4426950408889634f;

struct Params { const u16* x; const u16* wqkv; const u16* wo; const u16* wg; const u16* wu; const u16* wd; char* out; char* ws; };

#define STAMP(i) do {} while (0)
#define STAMPC(i) do {} while (0)
#define STAMPW(i, w) do {} while (0)

DI unsigned pk2(float a, float b) { f32x2 v = {a, b}; return __builtin_bit_cast(unsigned, __builtin_convertvector(v, bf16x2_t)); }
DI float bflo(unsigned u) { return __uint_as_float(u << 16); }
DI float bfhi(unsigned u) { return __uint_as_float(u & 0xffff0000u); }
DI float rbf(float a) { return bflo(pk2(a, 0.f) & 0xffffu); }
template <bool SC1> DI void glds16(unsigned voff, const void* sbase, unsigned ldsaddr) {
    unsigned keep;
    if (SC1) asm volatile("s_mov_b32 %0, m0\n\ts_mov_b32 m0, %3\n\ts_nop 0\n\tglobal_load_lds_dwordx4 %1, %2 sc1\n\ts_mov_b32 m0, %0" : "=&s"(keep) : "v"(voff), "s"(sbase), "s"(ldsaddr) : "memory");
    else asm volatile("s_mov_b32 %0, m0\n\ts_mov_b32 m0, %3\n\ts_nop 0\n\tglobal_load_lds_dwordx4 %1, %2\n\ts_mov_b32 m0, %0" : "=&s"(keep) : "v"(voff), "s"(sbase), "s"(ldsaddr) : "memory");
}
#define GL_(v, off, sc) "global_load_lds_dwordx4 %" #v ", %[g] offset:" #off sc "\n\t"
#define GLHEAD "s_mov_b32 m0, %[l]\n\ts_nop 0\n\t"
template <bool SC1> DI void gl_lin5(unsigned v, const char* g, unsigned l) {
    if (SC1) asm volatile(GLHEAD GL_(0, -2048, " sc1") GL_(0, -1024, " sc1") GL_(0, 0, " sc1") GL_(0, 1024, " sc1") GL_(0, 2048, " sc1") :: "v"(v), [g] "s"(g), [l] "s"(l) : "memory");
    else asm volatile(GLHEAD GL_(0, -2048, "") GL_(0, -1024, "") GL_(0, 0, "") GL_(0, 1024, "") GL_(0, 2048, "") :: "v"(v), [g] "s"(g), [l] "s"(l) : "memory");
}
template <bool SC1> DI void gl_lin7(unsigned v, const char* g, unsigned l) {
    if (SC1) asm volatile(GLHEAD GL_(0, -3072, " sc1") GL_(0, -2048, " sc1") GL_(0, -1024, " sc1") GL_(0, 0, " sc1") GL_(0, 1024, " sc1") GL_(0, 2048, " sc1") GL_(0, 3072, " sc1") :: "v"(v), [g] "s"(g), [l] "s"(l) : "memory");
    else asm volatile(GLHEAD GL_(0, -3072, "") GL_(0, -2048, "") GL_(0, -1024, "") GL_(0, 0, "") GL_(0, 1024, "") GL_(0, 2048, "") GL_(0, 3072, "") :: "v"(v), [g] "s"(g), [l] "s"(l) : "memory");
}
template <bool SC1> DI void gl_str4(const unsigned* v, const char* g, unsigned l) {
    if (SC1) asm volatile(GLHEAD GL_(0, -2048, " sc1") GL_(1, -1024, " sc1") GL_(2, 0, " sc1") GL_(3, 1024, " sc1") :: "v"(v[0]), "v"(v[1]), "v"(v[2]), "v"(v[3]), [g] "s"(g), [l] "s"(l) : "memory");
    else asm volatile(GLHEAD GL_(0, -2048, "") GL_(1, -1024, "") GL_(2, 0, "") GL_(3, 1024, "") :: "v"(v[0]), "v"(v[1]), "v"(v[2]), "v"(v[3]), [g] "s"(g), [l] "s"(l) : "memory");
}
template <bool SC1> DI void gl_str4w(const unsigned* v, const char* g, unsigned l) {
    if (SC1) asm volatile(GLHEAD GL_(0, -4096, " sc1") GL_(1, -2048, " sc1") GL_(2, 0, " sc1") GL_(3, 2048, " sc1") :: "v"(v[0]), "v"(v[1]), "v"(v[2]), "v"(v[3]), [g] "s"(g), [l] "s"(l) : "memory");
    else asm volatile(GLHEAD GL_(0, -4096, "") GL_(1, -2048, "") GL_(2, 0, "") GL_(3, 2048, "") :: "v"(v[0]), "v"(v[1]), "v"(v[2]), "v"(v[3]), [g] "s"(g), [l] "s"(l) : "memory");
}
DI void gl_str8(const unsigned* v, const char* g, unsigned l) {
    asm volatile(GLHEAD GL_(0, -4096, "") GL_(1, -3072, "") GL_(2, -2048, "") GL_(3, -1024, "") GL_(4, 0, "") GL_(5, 1024, "") GL_(6, 2048, "") GL_(7, 3072, "")
                 :: "v"(v[0]), "v"(v[1]), "v"(v[2]), "v"(v[3]), "v"(v[4]), "v"(v[5]), "v"(v[6]), "v"(v[7]), [g] "s"(g), [l] "s"(l) : "memory");
}
template <bool SC1> DI void st16(void* ptr, u32x4 v) {
    if (SC1) asm volatile("global_store_dwordx4 %0, %1, off sc1\n\ts_nop 1" :: "v"(ptr), "v"(v) : "memory");
    else *(u32x4*)ptr = v;
}
DI u64 ld_dev(const void* p_) { return __hip_atomic_load((const u64*)p_, __ATOMIC_RELAXED, __HIP_MEMORY_SCOPE_AGENT); }
DI void st_dev(void* p_, u64 v) { __hip_atomic_store((u64*)p_, v, __ATOMIC_RELAXED, __HIP_MEMORY_SCOPE_AGENT); }
DI void ld64_async(u32x2& v, const void* ptr) { asm volatile("global_load_dwordx2 %0, %1, off sc1" : "=v"(v) : "v"(ptr) : "memory"); }
DI void ld32_async(float& v, const void* ptr) { asm volatile("global_load_dword %0, %1, off sc1" : "=v"(v) : "v"(ptr) : "memory"); }
#define VMWAIT(N) asm volatile("s_waitcnt vmcnt(%0)" :: "i"(N) : "memory")
DI void poll_flags(const void* flag, bool need, u64 tag) {
    for (unsigned spins = 0; spins < (1u << 18); ++spins) {
        const u64 v = need ? ld_dev(flag) : tag;
        if (__all(v == tag)) break;
    }
    asm volatile("" ::: "memory");
}
DI void warm64(const void* base, unsigned voff, unsigned lds0) {
    unsigned keep;
    asm volatile("s_mov_b32 %0, m0\n\ts_mov_b32 m0, %3\n\ts_nop 0\n\tglobal_load_lds_dword %1, %2 sc1\n\ts_mov_b32 m0, %0" : "=&s"(keep) : "v"(voff), "s"(base), "s"(lds0 + (unsigned)(LDS_BYTES - 256)) : "memory");
}
#define BAR_L() asm volatile("s_waitcnt lgkmcnt(0)\n\ts_barrier" ::: "memory")
#define BAR_VL(N) asm volatile("s_waitcnt vmcnt(%0) lgkmcnt(0)\n\ts_barrier" :: "i"(N) : "memory")
#define WAIT_LGKM0() do { __builtin_amdgcn_sched_barrier(0); __builtin_amdgcn_s_waitcnt(0xC07F); __builtin_amdgcn_sched_barrier(0); } while (0)
#define CBAR() do { WAIT_LGKM0(); asm volatile("" ::: "memory"); __builtin_amdgcn_s_barrier(); asm volatile("" ::: "memory"); __builtin_amdgcn_sched_barrier(0); } while (0)
#define SGB(mask, n) __builtin_amdgcn_sched_group_barrier(mask, n, 0)
template <int STEP, int MAXN> DI void bar_vl_n(int n) {
    if (n >= MAXN) { BAR_VL((MAXN * STEP > 63 ? 63 : MAXN * STEP)); }
    else if constexpr (MAXN > 0) bar_vl_n<STEP, MAXN - 1>(n);
}
template <int N> struct IC { static constexpr int v = N; };
template <int K, int END, class F> DI void static_for(F&& f) { if constexpr (K < END) { f(IC<K>{}); static_for<K + 1, END>(f); } }
template <int R, int NST, int NP, int LAG, bool PROLOGUE, int NEND = NST, class FI, class FP, class FQ>
DI void ring_loader(FI&& issue, FP&& pre, FQ&& post) {
    constexpr int L0 = (R < NST ? R : NST) - 1;
    if (PROLOGUE) {
#pragma unroll 1
        for (int k = 0; k <= L0; ++k) issue();
    }
    static_for<0, LAG + 1>([&](auto kc) { constexpr int k = decltype(kc)::v; BAR_VL((L0 - k) * NP); pre(k); if constexpr (k >= LAG && k - LAG + R < NST) issue(); post(k); });
    constexpr int KS = NST - R + LAG;
    if constexpr (KS > LAG + 1) {
#pragma unroll 1
        for (int k = LAG + 1; k < KS; ++k) { BAR_VL((R - 1 - LAG) * NP); pre(k); issue(); post(k); }
    }
    static_for<(KS > LAG + 1 ? KS : LAG + 1), NEND>([&](auto kc) { constexpr int k = decltype(kc)::v; BAR_VL((NST - 1 - k) * NP); pre(k); post(k); });
}
template <int R, int NST, int LAG, int P0> constexpr int ramp_issued(int k) { const int r = P0 + 2 * k, lim = k <= LAG ? R : R + k - LAG, v = r < lim ? r : lim; return v < NST ? v : NST; }
template <int R, int NST, int LAG, int P0> constexpr int ramp_len() { int k = LAG + 1; while (k < NST && P0 + 2 * k < R + k - LAG) ++k; return k < NST ? k : NST - 1; }
template <int R, int NST, int NP, int LAG, int P0, class FI>
DI void ring_loader_ramp2(FI&& issue) {
    static_for<0, P0>([&](auto) { issue(); });
    constexpr int KR = ramp_len<R, NST, LAG, P0>();
    static_for<0, KR>([&](auto kc) { constexpr int k = decltype(kc)::v; constexpr int a = ramp_issued<R, NST, LAG, P0>(k), b = ramp_issued<R, NST, LAG, P0>(k + 1);
        BAR_VL((a - 1 - k) * NP); static_for<a, b>([&](auto) { issue(); }); });
    constexpr int AK = ramp_issued<R, NST, LAG, P0>(KR) - 1 - KR;
    constexpr int KS = NST - 1 - AK;
    if constexpr (KS > KR) {
#pragma unroll 1
        for (int k = KR; k < KS; ++k) { BAR_VL(AK * NP); issue(); }
    }
    static_for<(KS > KR ? KS : KR), NST>([&](auto kc) { constexpr int k = decltype(kc)::v; BAR_VL((NST - 1 - k) * NP); });
}
DI bf16x8 ld128(lcptr p) { return *(const LAS bf16x8*)p; }
DI s16x4 ld64(lcptr p) { return *(const LAS s16x4*)p; }
DI s16x4 ld64v(lcptr p) { return *(const volatile LAS s16x4*)p; }
DI s16x4 ldtr(lcptr p) { typedef short v4 __attribute__((ext_vector_type(4))); return __builtin_bit_cast(s16x4, __builtin_amdgcn_ds_read_tr16_b64_v4i16((LAS v4*)p)); }
DI bf16x8 cat8(s16x4 a, s16x4 b) { return __builtin_shufflevector(a, b, 0, 1, 2, 3, 4, 5, 6, 7); }
#define MFMA16(a, b, c) __builtin_amdgcn_mfma_f32_16x16x32_bf16(a, b, c, 0, 0, 0)
#define MFMA32(a, b, c) __builtin_amdgcn_mfma_f32_32x32x16_bf16(a, b, c, 0, 0, 0)
#define MFMA32K8(a, b, c) __builtin_amdgcn_mfma_f32_32x32x8bf16_1k(a, b, c, 0, 0, 0)
template <int CTRL> DI float dpp_add(float v) { return v + __int_as_float(__builtin_amdgcn_update_dpp(0, __float_as_int(v), CTRL, 0xf, 0xf, true)); }
DI float sum8(float v) { v = dpp_add<0xB1>(v); v = dpp_add<0x4E>(v); return dpp_add<0x141>(v); }
DI float sum_x16(float v) { auto r = __builtin_amdgcn_permlane16_swap(__float_as_uint(v), __float_as_uint(v), false, false); return __uint_as_float(r[0]) + __uint_as_float(r[1]); }
DI float sum_x32(float v) { auto r = __builtin_amdgcn_permlane32_swap(__float_as_uint(v), __float_as_uint(v), false, false); return __uint_as_float(r[0]) + __uint_as_float(r[1]); }
DI bf16x8 ones8() { bf16x8 o = {0x3f80, 0x3f80, 0x3f80, 0x3f80, 0x3f80, 0x3f80, 0x3f80, 0x3f80}; asm volatile("" : "+v"(o)); return o; }

DI f32x2 ln_scalars(float sum, float sq) {
    const float mean = sum * (1.0f / D_);
    float var = sq * (1.0f / D_) - mean * mean; var = var > 0.f ? var : 0.f;
    const float sd = rbf(__builtin_sqrtf(rbf(rbf(var) + 1e-5f)));
    return (f32x2){rbf(mean), 1.0f / sd};
}
template <bool FUSED> DI void publish(const Params& p, size_t flag_off, int wid, int lane, u64 tag, int sidx = 47) {
    if (FUSED) { BAR_VL(0); STAMP(sidx); if (wid == 0 && lane == 0) st_dev(p.ws + WS_FLAG + flag_off, tag); }
}

namespace rp { constexpr int L_G = 132608, L_U = L_G + 10752, L_O = L_G + 21504, L_D = 102400; }
DI void rp1_addr(const Params& p, int lane, int lw, int ti, int tj, const char* (&ga)[7]) {
    const int ns = ti < 8 ? 3 : 2, s0 = ti < 8 ? 3 * ti : 24 + 2 * (ti - 8);
    const int ct = tj >> 1, z = 2 * ti + (tj & 1);
#pragma unroll
    for (int i = 0; i < 7; ++i) { const int n = lw + 4 * i;
        if (n < 21) { int u = 64 * n + lane; const int mat = u >= 672; u -= 672 * mat; int row = u / 14; int col = 112 * tj + 8 * (u - 14 * row); col = col > F_ - 8 ? F_ - 8 : col;
            row = row < 16 * ns ? row : 16 * ns - 1;
            ga[i] = (const char*)((mat ? p.wu : p.wg) + (size_t)(16 * s0 + row) * F_ + col);
        } else { const int m = n < 26 ? n - 21 : 4, u = 64 * m + lane, row = u / 10, c = u - 10 * row;
            ga[i] = (const char*)(p.wo + (size_t)(32 * (z < 20 ? z : 0) + row) * D_ + 80 * ct + 8 * c); } }
}
DI void glds16v(const void* vaddr, unsigned ldsaddr) {
    asm volatile("s_nop 0\n\ts_mov_b32 m0, %1\n\ts_nop 0\n\tglobal_load_lds_dwordx4 %0, off" :: "v"(vaddr), "s"(ldsaddr) : "memory");
}
template <int I0 = 0, int I1 = 7>
DI void rp1_issue(const char* (&ga)[7], unsigned lds0, int lw) {
#pragma unroll
    for (int i = I0; i < I1; ++i) { const int n = lw + 4 * i; glds16v(ga[i], (unsigned)__builtin_amdgcn_readfirstlane(lds0 + rp::L_G + (n < 26 ? n : 25) * 1024)); }
}
template <bool FUSED>
DI void rp1_store(const Params& p, lptr L, int lane, int lw, int ti, int tj, int part = 3) {
    const int ns = ti < 8 ? 3 : 2, s0 = ti < 8 ? 3 * ti : 24 + 2 * (ti - 8);
    const int ct = tj >> 1, z = 2 * ti + (tj & 1);
    const int h = lane >> 5, blk = (lane >> 4) & 1, g = lane >> 4, q = (lane & 15) >> 2, pp = lane & 3;
    const bf16x8 ones = ones8();
#pragma unroll 1
    for (int t = lw + (part == 1 ? 4 : 0); t < (part == 0 ? 4 : part == 2 ? 0 : 7); t += 4) {
        lcptr src = (lcptr)L + (t < 6 ? ((t & 1) ? rp::L_U : rp::L_G) + (32 * (t >> 1) + 16 * blk + 4 * pp) * 2 : (blk ? rp::L_U : rp::L_G) + (96 + 4 * pp) * 2) + (8 * h + q) * 224;
        s16x4 f[3][2];
#pragma unroll
        for (int s = 0; s < 3; ++s) { f[s][0] = ldtr(src + s * 16 * 224); f[s][1] = ldtr(src + s * 16 * 224 + 4 * 224); }
        f32x16 cs = {};
        char* dst = p.ws + WS_PWG + ((size_t)(tj * 40 + s0) * 7 + t) * 1024 + lane * 16;
#pragma unroll
        for (int s = 0; s < 3; ++s) if (s < ns) { const bf16x8 fr = cat8(f[s][0], f[s][1]); st16<FUSED>(dst + s * 7168, __builtin_bit_cast(u32x4, fr)); cs = MFMA32(fr, ones, cs); }
        if ((lane & 31) == 0) { float* cd = (float*)(p.ws + WS_CSP) + (ti * 16 + tj) * 224 + 32 * t + 4 * h;
#pragma unroll
            for (int u = 0; u < 4; ++u) st16<FUSED>(cd + 8 * u, __builtin_bit_cast(u32x4, ((f32x4){cs[4 * u], cs[4 * u + 1], cs[4 * u + 2], cs[4 * u + 3]}))); }
    }
    if (z < 20 && (part == 2 || part == 3)) {
#pragma unroll 1
        for (int nt = 3 - lw; nt < 5; nt += 4) {
            lcptr src = (lcptr)L + rp::L_O + (8 * g + q) * 160 + (16 * nt + 4 * pp) * 2;
            st16<FUSED>(p.ws + WS_PWO + ((size_t)(ct * 20 + z) * 5 + nt) * 1024 + lane * 16, __builtin_bit_cast(u32x4, cat8(ldtr(src), ldtr(src + 4 * 160)))); }
    }
}
DI void rp2_issue(const Params& p, unsigned lds0, int lane, int wid, int ti, int tj) {
    const int ct = tj >> 1, z = 2 * ti + (tj & 1), np = z + 32 < 54 ? 10 : 5;
#pragma unroll 1
    for (int n = wid; n < np; n += 4) { const int im = n >= 5, u = 64 * (n - 5 * im) + lane, row = u / 10, c = u - 10 * row;
        glds16<false>((unsigned)((32 * (z + 32 * im) + row) * D_ + 80 * ct + 8 * c) * 2u, p.wd, lds0 + rp::L_D + n * 1024); }
}
template <bool FUSED>
DI int rp2_store(const Params& p, lptr L, int lane, int wid, int ti, int tj) {
    const int ct = tj >> 1, z = 2 * ti + (tj & 1), np = z + 32 < 54 ? 10 : 5;
    const int g = lane >> 4, q = (lane & 15) >> 2, pp = lane & 3;
    int cnt = 0;
#pragma unroll 1
    for (int n = wid; n < np; n += 4, ++cnt) { const int im = n >= 5, nt = n - 5 * im;
        lcptr src = (lcptr)L + rp::L_D + im * 5120 + (8 * g + q) * 160 + (16 * nt + 4 * pp) * 2;
        st16<FUSED>(p.ws + WS_PWD + ((size_t)(ct * 54 + z + 32 * im) * 5 + nt) * 1024 + lane * 16, __builtin_bit_cast(u32x4, cat8(ldtr(src), ldtr(src + 4 * 160)))); }
    return cnt;
}
DI u64 make_tag(u32x2& ev) {
    asm volatile("" : "+v"(ev));
    const u64 e = (((u64)(unsigned)__builtin_amdgcn_readfirstlane(ev[1]) << 32) | (unsigned)__builtin_amdgcn_readfirstlane(ev[0])) + 1;
    return e;
}

namespace pa { constexpr int R = 4, NST = 10, LA = 0, LB = R * 16384, L_STAT = 2 * R * 16384, L_CS = L_STAT + 1024; }
template <bool FUSED>
DI void phase_a_stats(const Params& p, lptr L, int lane, int wid, int ti, u32x2& ev, u64& tag) {
    const int r = lane >> 3, c = lane & 7;
    if (wid >= 4) { const char* ga[7]; rp1_addr(p, lane, wid - 4, ti, 15, ga); rp1_issue(ga, (unsigned)(uintptr_t)L, wid - 4); }
#pragma unroll
    for (int hlf = 0; hlf < 2; ++hlf) {
        const int row = 128 * ti + 16 * wid + 8 * hlf + r;
        const u32x4* src = (const u32x4*)(p.x + (size_t)row * D_) + c;
        u32x4 v[10];
#pragma unroll
        for (int i = 0; i < 10; ++i) v[i] = src[8 * i];
        float a = 0.f, q = 0.f;
#pragma unroll
        for (int i = 0; i < 10; ++i)
#pragma unroll
            for (int e = 0; e < 4; ++e) { const float lo = bflo(v[i][e]), hi = bfhi(v[i][e]); a += lo + hi; q += lo * lo + hi * hi; }
        const f32x2 st = ln_scalars(sum8(a), sum8(q));
        if (c == 0) { const u64 w = ((u64)__float_as_uint(st.y) << 32) | __float_as_uint(st.x); char* dst = p.ws + WS_S1 + (size_t)row * 8; if (FUSED) st_dev(dst, w); else *(u64*)dst = w; }
    }
    BAR_VL(0);
    if (FUSED) { tag = make_tag(ev); if (wid == 0 && lane == 0) st_dev(p.ws + WS_FLAG + FL_A + (ti * 16 + 15) * FS, tag); }
    if (wid >= 4) rp1_store<FUSED>(p, L, lane, wid - 4, ti, 15);
    if (FUSED) { BAR_VL(0); if (wid == 0 && lane == 0) st_dev(p.ws + WS_FLAG + FL_P + (ti * 16 + 15) * FS, tag); }
}
template <bool FUSED>
DI void phase_a(const Params& p, lptr L, int lane, int wid, int ti, int tj, int rot, u32x2& ev, u64& tag) {
    using namespace pa;
    const unsigned lds0 = (unsigned)(uintptr_t)L;
    if (__builtin_expect(wid >= 4, 1)) {
        const int lw = wid - 4, r = lane >> 3, c = lane & 7;
        const bool isA = lw < 2;
        unsigned vo[2];
#pragma unroll
        for (int e = 0; e < 2; ++e) vo[e] = isA ? (unsigned)(r * D_ + ((c ^ ((4 * e + (r >> 1)) & 7)) * 8)) * 2u : (unsigned)(r * 3 * D_ + ((c ^ (4 * ((r >> 1) & 1) + 2 * e)) * 8)) * 2u;
        unsigned vp[8];
#pragma unroll
        for (int i = 0; i < 8; ++i) vp[i] = vo[i & 1] + (isA ? (unsigned)i * (8 * D_ * 2 - 1024) : (unsigned)(i & 3) * (8 * 3 * D_ * 2 - 1024));
        const char* gb = isA ? (const char*)p.x + (size_t)(128 * ti + 64 * lw) * D_ * 2 + 4096 + rot * 128 : (const char*)p.wqkv + ((size_t)(32 * (lw - 2) + 64 * rot) * 3 * D_ + 128 * tj) * 2 + 2048;
        const int gstep = isA ? 128 : 64 * 3 * D_ * 2;
        int ks = rot; unsigned sl = lds0 + (isA ? LA + lw * 8192 + 4096 : LB + (lw - 2) * 4096 + 2048), sslot = 0;
        auto issue = [&]() {
            if (isA) gl_str8(vp, gb, sl + sslot); else { gl_str4<false>(vp, gb, sl + sslot); gl_str4<false>(vp, gb + 128, sl + sslot + 8192); }
            gb += gstep; if (++ks == NST) { ks = 0; gb -= (size_t)NST * gstep; }
            sslot = (sslot + 16384) & (R * 16384 - 1);
        };
        const int g = lane >> 4, q = (lane & 15) >> 2, pp = lane & 3;
        unsigned aC[2];
#pragma unroll
        for (int t = 0; t < 2; ++t) aC[t] = (unsigned)(LB + ((lw >> 1) * 8 + g) * 1024 + q * 128 + (((2 * (2 * (lw & 1) + t) + (pp >> 1)) ^ (4 * ((q >> 1) & 1) + 2 * (g & 1))) * 16) + (pp & 1) * 8);
        f32x4 cs[2] = {{0.f, 0.f, 0.f, 0.f}, {0.f, 0.f, 0.f, 0.f}};
        const bf16x8 ones = ones8();
        s16x4 cf[2][2][2];
        __builtin_amdgcn_s_setprio(3);
        auto pre = [&](int s) { lcptr q0 = (lcptr)L + (s & 3) * 16384;
#pragma unroll
            for (int ks2 = 0; ks2 < 2; ++ks2)
#pragma unroll
                for (int t = 0; t < 2; ++t) { cf[ks2][t][0] = ldtr(q0 + aC[t] + ks2 * 4096); cf[ks2][t][1] = ldtr(q0 + aC[t] + ks2 * 4096 + 512); } };
        auto post = [&](int) {
#pragma unroll
            for (int ks2 = 0; ks2 < 2; ++ks2)
#pragma unroll
                for (int t = 0; t < 2; ++t) cs[t] = MFMA16(cat8(cf[ks2][t][0], cf[ks2][t][1]), ones, cs[t]); };
        issue(); issue();
        __builtin_amdgcn_sched_barrier(0);
        const char* ga[7]; rp1_addr(p, lane, lw, ti, tj, ga);
        BAR_VL(8); pre(0); issue();
        if (FUSED) tag = make_tag(ev);
        BAR_VL(8); post(0); pre(1); issue();
        BAR_VL(8); post(1); pre(2); issue();
        BAR_VL(8); post(2); pre(3); issue(); rp1_issue<0, 2>(ga, lds0, lw);
        BAR_VL(10); post(3); pre(4); issue(); rp1_issue<2, 4>(ga, lds0, lw);
        BAR_VL(12); post(4); pre(5); issue(); rp1_issue<4, 7>(ga, lds0, lw);
        BAR_VL(13); post(5); pre(6); issue();
        u32x2 sf, sv;
        if (FUSED) ld64_async(sf, p.ws + WS_FLAG + FL_A + (ti * 16 + 15) * FS);
        BAR_VL(FUSED ? 12 : 11); post(6); pre(7); issue();
        BAR_VL(8); post(7); pre(8);
        if (FUSED) { asm volatile("" : "+v"(sf)); if (!__all((((u64)sf[1] << 32) | sf[0]) == tag)) poll_flags(p.ws + WS_FLAG + FL_A + (ti * 16 + 15) * FS, lane == 0, tag); }
        ld64_async(sv, p.ws + WS_S1 + (size_t)(128 * ti + 64 * (lw & 1) + lane) * 8);
        BAR_VL(0); post(8); pre(9); post(9);
        asm volatile("" : "+v"(sv));
        if (isA) *(LAS u32x2*)(L + L_STAT + (64 * lw + lane) * 8) = sv;
        __builtin_amdgcn_s_setprio(0);
        if ((lane & 15) == 0) {
#pragma unroll
            for (int t = 0; t < 2; ++t) *(LAS f32x4*)(L + L_CS + (32 * lw + 16 * t + 4 * g) * 4) = cs[t];
        }
        BAR_L();
        BAR_L();
    } else {
        const int wm = wid >> 1, wn = wid & 1, r32 = lane & 31, h = lane >> 5, blk = (lane >> 4) & 1, q = (lane & 15) >> 2, pp = lane & 3;
        unsigned aX[4], aW[2];
        { const int row = 64 * wm + r32;
#pragma unroll
          for (int u = 0; u < 4; ++u) aX[u] = (unsigned)(LA + (row >> 3) * 1024 + (row & 7) * 128 + (((2 * u + h) ^ ((row >> 1) & 7)) * 16));
#pragma unroll
          for (int nt = 0; nt < 2; ++nt) aW[nt] = (unsigned)(LB + (wn * 8 + h) * 1024 + q * 128 + (((4 * nt + 2 * blk + (pp >> 1)) ^ (4 * ((q >> 1) & 1) + 2 * h)) * 16) + (pp & 1) * 8); }
        f32x16 acc[2][2];
#pragma unroll
        for (int i = 0; i < 16; ++i) { acc[0][0][i] = 0.f; acc[0][1][i] = 0.f; acc[1][0][i] = 0.f; acc[1][1][i] = 0.f; }
        bf16x8 X[4][2], W[4][2];
        auto rd = [&](int slot, int u) {
            lcptr sl = (lcptr)L + slot * 16384;
#pragma unroll
            for (int nt = 0; nt < 2; ++nt) W[u][nt] = cat8(ldtr(sl + aW[nt] + u * 2048), ldtr(sl + aW[nt] + u * 2048 + 512));
#pragma unroll
            for (int mt = 0; mt < 2; ++mt) X[u][mt] = ld128(sl + aX[u] + mt * 4096);
        };
        auto mm = [&](int u) {
#pragma unroll
            for (int mt = 0; mt < 2; ++mt)
#pragma unroll
                for (int nt = 0; nt < 2; ++nt) acc[mt][nt] = MFMA32(W[u][nt], X[u][mt], acc[mt][nt]);
        };
        auto il = [&]() { SGB(0x8, 1); SGB(0x100, 2); SGB(0x8, 1); SGB(0x100, 2); SGB(0x8, 1); SGB(0x100, 2); SGB(0x8, 1); __builtin_amdgcn_sched_barrier(0); };
        auto bar = [&]() { __builtin_amdgcn_sched_barrier(0); asm volatile("" ::: "memory"); __builtin_amdgcn_s_barrier(); asm volatile("" ::: "memory"); __builtin_amdgcn_sched_barrier(0); };
        bar();
        if (FUSED) VMWAIT(0);
        STAMP(1); STAMPC(26);
        rd(0, 0); rd(0, 1);
#pragma unroll 1
        for (int s = 0; s < NST - 1; ++s) {
            const int c = s & 3, n = (s + 1) & 3;
            rd(c, 2); mm(0); il();
            rd(c, 3); mm(1); il();
            bar();
            rd(n, 0); mm(2); il();
            rd(n, 1); mm(3); il();
        }
        rd((NST - 1) & 3, 2); mm(0); il();
        rd((NST - 1) & 3, 3); mm(1); il();
        mm(2); mm(3);
        STAMP(2); STAMPC(27);
        if (FUSED) tag = make_tag(ev);
        BAR_L();
        STAMP(38);
        {
            f32x2 st[2]; float cv[2];
#pragma unroll
            for (int i = 0; i < 2; ++i) { st[i] = *(const LAS f32x2*)(L + L_STAT + (64 * wm + 32 * i + r32) * 8); cv[i] = *(const LAS float*)(L + L_CS + (64 * wn + 32 * i + r32) * 4); }
            bf16x8 cf[2], mf[2];
#pragma unroll
            for (int i = 0; i < 2; ++i) { const float hi = rbf(cv[i]); const unsigned c = h ? 0u : pk2(hi, cv[i] - hi), m = h ? 0u : pk2(-st[i].x, -st[i].x);
                cf[i] = __builtin_bit_cast(bf16x8, ((u32x4){c, 0u, 0u, 0u})); mf[i] = __builtin_bit_cast(bf16x8, ((u32x4){m, 0u, 0u, 0u})); }
#pragma unroll
            for (int mt = 0; mt < 2; ++mt)
#pragma unroll
                for (int nt = 0; nt < 2; ++nt) acc[mt][nt] = MFMA32(cf[nt], mf[mt], acc[mt][nt]);
#pragma unroll
            for (int mt = 0; mt < 2; ++mt) { const int tok = 64 * wm + 32 * mt + r32; const float sq = st[mt].y * C2_;
#pragma unroll
                for (int nt = 0; nt < 2; ++nt)
#pragma unroll
                    for (int u = 0; u < 4; ++u) { const int col = 64 * wn + 32 * nt + 8 * u + 4 * h;
                        const float sc = (128 * tj + col < D_) ? sq : st[mt].y;
                        *(LAS u32x2*)(L + (col >> 2) * 1024 + tok * 8) = (u32x2){pk2(acc[mt][nt][4 * u] * sc, acc[mt][nt][4 * u + 1] * sc), pk2(acc[mt][nt][4 * u + 2] * sc, acc[mt][nt][4 * u + 3] * sc)}; } }
        }
        BAR_L();
        STAMP(39);
    }
    char* dst = p.ws + WS_QKV + (size_t)(32 * tj) * 16384 + (size_t)ti * 1024;
#pragma unroll
    for (int i = 0; i < 4; ++i) { const int gq = 4 * wid + i; st16<FUSED>(dst + (size_t)gq * 16384 + lane * 16, *(const LAS u32x4*)(L + gq * 1024 + lane * 16)); }
    STAMP(3);
    publish<FUSED>(p, FL_A + (ti * 16 + tj) * FS, wid, lane, tag, 42);
}

namespace pb {
constexpr int GS = 3104;
constexpr int L_K = 0, L_V = 10 * GS, L_ONE = 20 * GS, L_Q = 21 * GS, L_O = 112640;
static_assert(L_Q + 10240 <= L_O, "LDS map");
}
#define ATT_STAMP() do {} while (0)
template <bool EDGE>
DI f32x16 attn_wave(lcptr L, int hh, int qb, int lane, int tstart, u64* stq) {
    using namespace pb;
    const int r = lane & 31, h = lane >> 5;
    bf16x8 q0; s16x4 q1;
    { lcptr qp = L + L_Q + hh * 5 * 1024 + (32 * qb + r) * 8;
      q0 = cat8(ld64(qp + (2 * h) * 1024), ld64(qp + (2 * h + 1) * 1024));
      const s16x4 t = ld64(qp + 4 * 1024); q1 = h ? (s16x4){0, 0, 0, 0} : t; }
    lcptr kp = L + L_K + hh * 5 * GS + (32 * qb + r) * 8;
    const f32x16 z16 = {};
    const int rr_ = r - 4 * h;
    bf16x8 k0[9]; s16x4 k1[9];
#pragma unroll
    for (int t = 0; t < 9; ++t) { k0[t] = cat8(ld64v(kp + (2 * h) * GS + t * 256), ld64v(kp + (2 * h + 1) * GS + t * 256)); k1[t] = ld64v(kp + 4 * GS + t * 256); }
    float m;
    { f32x16 a = MFMA32K8(k1[8], q1, MFMA32(k0[8], q0, z16)), b = MFMA32K8(k1[0], q1, MFMA32(k0[0], q0, z16));
      const float bias = (!EDGE || tstart == 0) ? 0.f : -INFINITY;
#pragma unroll
      for (int i = 0; i < 16; ++i) a[i] = ((i & 3) + 8 * (i >> 2) <= rr_) ? a[i] : (EDGE ? b[i] + bias : b[i]);
      float x = __builtin_fmaxf(__builtin_fmaxf(a[0], a[1]), a[2]), y = __builtin_fmaxf(__builtin_fmaxf(a[3], a[4]), a[5]);
#pragma unroll
      for (int i = 6; i < 14; i += 4) { x = __builtin_fmaxf(__builtin_fmaxf(x, a[i]), a[i + 1]); y = __builtin_fmaxf(__builtin_fmaxf(y, a[i + 2]), a[i + 3]); }
      m = __builtin_fmaxf(__builtin_fmaxf(__builtin_fmaxf(x, a[14]), a[15]), y); }
#pragma unroll
    for (int t = 1; t < 8; ++t) { const f32x16 a = MFMA32K8(k1[t], q1, MFMA32(k0[t], q0, z16));
        float x = __builtin_fmaxf(__builtin_fmaxf(a[0], a[1]), a[2]), y = __builtin_fmaxf(__builtin_fmaxf(a[3], a[4]), a[5]);
#pragma unroll
        for (int i = 6; i < 14; i += 4) { x = __builtin_fmaxf(__builtin_fmaxf(x, a[i]), a[i + 1]); y = __builtin_fmaxf(__builtin_fmaxf(y, a[i + 2]), a[i + 3]); }
        x = __builtin_fmaxf(__builtin_fmaxf(x, a[14]), a[15]);
        if (EDGE) x = t >= tstart ? x : -INFINITY;
        m = __builtin_fmaxf(__builtin_fmaxf(m, x), (EDGE && t < tstart) ? -INFINITY : y); }
    { auto rr = __builtin_amdgcn_permlane32_swap(__float_as_uint(m), __float_as_uint(m), false, false); m = __builtin_fmaxf(__uint_as_float(rr[0]), __uint_as_float(rr[1])); }
    f32x16 nm16;
#pragma unroll
    for (int i = 0; i < 16; ++i) nm16[i] = -m;
    ATT_STAMP();
    const int blk = (lane >> 4) & 1, vq = (lane & 15) >> 2, vp = lane & 3, dg = 4 * blk + vp;
    lcptr vpz = L + (dg < 5 ? L_V + (hh * 5 + dg) * GS : L_ONE) + (32 * qb + 4 * h + vq) * 8;
    f32x16 O = z16;
#pragma unroll
    for (int t = 1; t < 8; ++t) {
        const bf16x8 v0 = cat8(ldtr(vpz + t * 256), ldtr(vpz + t * 256 + 64)), v1 = cat8(ldtr(vpz + t * 256 + 128), ldtr(vpz + t * 256 + 192));
        f32x16 a = MFMA32K8(k1[t], q1, MFMA32(k0[t], q0, nm16));
        if (EDGE) { const float bias = t >= tstart ? 0.f : -INFINITY;
#pragma unroll
            for (int i = 0; i < 16; ++i) a[i] += bias; }
        unsigned P[8];
#pragma unroll
        for (int i = 0; i < 8; ++i) P[i] = pk2(__builtin_amdgcn_exp2f(a[2 * i]), __builtin_amdgcn_exp2f(a[2 * i + 1]));
        O = MFMA32(v0, __builtin_bit_cast(bf16x8, ((u32x4){P[0], P[1], P[2], P[3]})), O);
        O = MFMA32(v1, __builtin_bit_cast(bf16x8, ((u32x4){P[4], P[5], P[6], P[7]})), O);
    }
    { const bf16x8 v0 = cat8(ldtr(vpz + 8 * 256), ldtr(vpz + 8 * 256 + 64)), v1 = cat8(ldtr(vpz + 8 * 256 + 128), ldtr(vpz + 8 * 256 + 192));
      const bf16x8 w0 = cat8(ldtr(vpz), ldtr(vpz + 64)), w1 = cat8(ldtr(vpz + 128), ldtr(vpz + 192));
      f32x16 a = MFMA32K8(k1[8], q1, MFMA32(k0[8], q0, nm16)); const f32x16 b = MFMA32K8(k1[0], q1, MFMA32(k0[0], q0, nm16));
      const float bias = (!EDGE || tstart == 0) ? 0.f : -INFINITY;
#pragma unroll
      for (int i = 0; i < 16; ++i) a[i] = ((i & 3) + 8 * (i >> 2) <= rr_) ? a[i] : (EDGE ? b[i] + bias : b[i]);
      unsigned P[8], P0[8];
#pragma unroll
      for (int i = 0; i < 8; ++i) { P[i] = pk2(__builtin_amdgcn_exp2f(a[2 * i]), __builtin_amdgcn_exp2f(a[2 * i + 1]));
          const int c0 = ((2 * i) & 3) + 8 * ((2 * i) >> 2); const unsigned mk = (c0 + 1 <= rr_) ? 0xffffffffu : (c0 <= rr_ ? 0xffffu : 0u);
          P0[i] = P[i] & ~mk; P[i] &= mk; }
      O = MFMA32(v0, __builtin_bit_cast(bf16x8, ((u32x4){P[0], P[1], P[2], P[3]})), O);
      O = MFMA32(v1, __builtin_bit_cast(bf16x8, ((u32x4){P[4], P[5], P[6], P[7]})), O);
      O = MFMA32(w0, __builtin_bit_cast(bf16x8, ((u32x4){P0[0], P0[1], P0[2], P0[3]})), O);
      O = MFMA32(w1, __builtin_bit_cast(bf16x8, ((u32x4){P0[4], P0[5], P0[6], P0[7]})), O); }
    return O;
}
template <bool FUSED>
DI void phase_b(const Params& p, lptr L, int lane, int wid, int ti, int tj, u64 tag) {
    using namespace pb;
    const int tid = wid * 64 + lane;
    const unsigned lds0 = (unsigned)(uintptr_t)L;
    if (!FUSED) { if (wid >= 4) { const char* ga[7]; rp1_addr(p, lane, wid - 4, ti, tj, ga); rp1_issue(ga, lds0, wid - 4); } BAR_VL(0); }
    if (wid < 4) rp2_issue(p, lds0, lane, wid, ti, tj);
    else if (tj < 15) rp1_store<FUSED>(p, L, lane, wid - 4, ti, tj, 2);
    if (ti < 2) {
        for (int n = tid; n < (2 - ti) * 20 * 64; n += 512) { const int pc = n >> 6, mb = pc % (2 - ti), gq = pc / (2 - ti);
            *(LAS u32x4*)(L + gq * GS + mb * 1024 + (n & 63) * 16) = (u32x4){0u, 0u, 0u, 0u}; }
    }
    if (tid < 384) *(LAS u32x2*)(L + L_ONE + tid * 8) = (u32x2){0x00003f80u, 0u};
    const int sub = wid & 3, mtile = sub < 3 ? ti - 2 + sub : ti;
    const char* g0 = p.ws + WS_QKV + (size_t)((sub < 3 ? 160 : 0) + 5 * (2 * tj + (wid >> 2))) * 16384 + (size_t)(mtile < 0 ? 0 : mtile) * 1024;
    const unsigned l0 = lds0 + (sub < 3 ? L_K + (wid >> 2) * 5 * GS + sub * 1024 : L_Q + (wid >> 2) * 5120);
    if (wid < 4) BAR_VL(0);
    else {
        BAR_L();
        if (FUSED && wid == 7) {
            const int mb = lane / 6, sec = (lane % 6) >> 1, e = lane & 1, mt2 = ti - 2 + mb;
            const bool need = lane < 18 && mt2 >= 0 && (sec > 0 || mb == 2);
            poll_flags(p.ws + WS_FLAG + FL_A + ((mt2 < 0 ? 0 : mt2) * 16 + ((640 * sec + 40 * tj + 39 * e) >> 7)) * FS, need, tag); STAMPW(35, 7); }
    }
    BAR_L();
    STAMP(4);
    if (sub == 3) {
#pragma unroll
        for (int d = 0; d < 5; ++d) glds16<FUSED>(lane * 16, g0 + d * 16384, l0 + d * 1024);
    } else if (mtile >= 0) {
#pragma unroll
        for (int d = 0; d < 5; ++d) glds16<FUSED>(lane * 16, g0 + d * 16384, l0 + d * GS);
#pragma unroll
        for (int d = 0; d < 5; ++d) glds16<FUSED>(lane * 16, g0 + (160 + d) * 16384, l0 + L_V + d * GS);
    }
    BAR_VL(0);
    if (FUSED && tid == 0 && tj < 15) st_dev(p.ws + WS_FLAG + FL_P + (ti * 16 + tj) * FS, tag);
    STAMP(5);
    const int hh = wid >> 2, qb = wid & 3, r = lane & 31, h = lane >> 5;
    const int q0g = 128 * ti + 32 * qb;
    f32x16 O;
    u64* stq = nullptr;
    if (ti >= 2) O = attn_wave<false>((lcptr)L, hh, qb, lane, 0, stq); else O = attn_wave<true>((lcptr)L, hh, qb, lane, q0g >= 256 ? 0 : 8 - (q0g >> 5), stq);
    { auto rr = __builtin_amdgcn_permlane32_swap(__float_as_uint(O[8]), __float_as_uint(O[8]), false, false);
      const float il = 1.0f / __uint_as_float(rr[1]);
      const int row = 32 * qb + r;
#pragma unroll
      for (int u = 0; u < 3; ++u) { const int dgp = 2 * u + h; if (dgp < 5) { const int cgp = 5 * hh + dgp;
          *(LAS u32x2*)(L + L_O + (cgp >> 1) * 2048 + row * 16 + (cgp & 1) * 8) = (u32x2){pk2(O[4 * u] * il, O[4 * u + 1] * il), pk2(O[4 * u + 2] * il, O[4 * u + 3] * il)}; } } }
    if (wid < 4) { if (tj < 15) rp1_store<FUSED>(p, L, lane, wid, ti, tj, 4); (void)rp2_store<FUSED>(p, L, lane, wid, ti, tj); }
    STAMP(6); STAMPW(37, 7);
    BAR_L();
    for (int n = wid; n < 10; n += 8)
        st16<FUSED>(p.ws + WS_O + (size_t)(5 * tj + (n >> 1)) * 32768 + (size_t)(128 * ti + 64 * (n & 1)) * 16 + lane * 16, *(const LAS u32x4*)(L + L_O + n * 1024 + lane * 16));
    STAMP(7);
    publish<FUSED>(p, FL_B + (ti * 16 + tj) * FS, wid, lane, tag);
}

template <int MODE> struct PC;
template <> struct PC<0> { static constexpr int NST = 10, RB = 10, RA = 6, LA = 102400, L_RES = 151552, L_X = 102400, L_STG = 122880; };
template <> struct PC<1> { static constexpr int NST = 27, RB = 10, RA = 5, LA = 102400, L_RES = 143360, L_X = 102400, L_STG = 102400; };
template <bool FUSED, int MODE>
DI void ce_prefetch(const Params& p, lptr L, int lane, int wid, int tj) {
    const char* wb = p.ws + (MODE ? WS_PWD + (size_t)(tj >> 1) * 54 * 5120 : WS_PWO + (size_t)(tj >> 1) * 20 * 5120) + (wid - 6) * 5120 + 2048;
    const unsigned l = (unsigned)(uintptr_t)L + (wid - 6) * 5120 + 2048;
#pragma unroll 1
    for (int s = 0; s < PC<MODE>::RB; ++s) gl_lin5<FUSED>(lane * 16, wb + (size_t)s * 10240, l + s * 10240);
}
template <bool FUSED, int MODE>
DI void phase_ce(const Params& p, lptr L, int lane, int wid, int ti, int tj, u64 tag) {
    typedef PC<MODE> C;
    constexpr int NST = C::NST, RA = C::RA, RB = C::RB;
    const unsigned lds0 = (unsigned)(uintptr_t)L;
    const int rh = tj & 1, ct = tj >> 1, r0 = 128 * ti + 64 * rh;
    const int wm = (wid >> 1) & 1, wk = wid & 1, r16 = lane & 15, g = lane >> 4;
    if (wid >= 6) {
        if (MODE == 0) {
            bar_vl_n<5, RB - 1>(RB - 1);
            BAR_VL(0);
#pragma unroll 1
            for (int s = 2; s < NST; ++s) BAR_L();
        } else {
            const char* wb = p.ws + WS_PWD + (size_t)ct * 54 * 5120 + (wid - 6) * 5120 + 2048 + (size_t)RB * 10240;
            const unsigned l0 = lds0 + (wid - 6) * 5120 + 2048; unsigned l = l0;
            ring_loader<RB, NST, 5, 2, false>([&]() { gl_lin5<FUSED>(lane * 16, wb, l); wb += 10240; l = l == l0 + (RB - 1) * 10240 ? l0 : l + 10240; }, [&](int) {}, [&](int) {});
        }
        BAR_L(); BAR_L();
    } else if (wid >= 4) {
        const int la = wid - 4;
        const unsigned voA = lane * 16;
#pragma unroll
        for (int i = 0; i < 5; ++i) { const int pc = 5 * la + i;
            if (MODE == 0) { const int n = 64 * pc + lane; glds16<false>((unsigned)((n / 10) * D_ * 2 + (n % 10) * 16), (const char*)p.x + (size_t)r0 * D_ * 2 + ct * 160, lds0 + C::L_RES + pc * 1024); }
            else glds16<FUSED>(voA, p.ws + WS_H + (size_t)(10 * ct + pc) * 32768 + (size_t)r0 * 16, lds0 + C::L_RES + pc * 1024); }
        if (FUSED) poll_flags(p.ws + WS_FLAG + (MODE ? FL_D : FL_B) + (ti * 16 + (lane & 15)) * FS, lane < 16, tag);
        STAMPW(MODE ? 17 : 9, 4);
        const char* ab = p.ws + (MODE ? WS_HH : WS_O) + (size_t)r0 * 16 + (size_t)(4 * la) * 32768 + 2048;
        unsigned vp[4];
#pragma unroll
        for (int i = 0; i < 4; ++i) vp[i] = voA + (unsigned)i * (32768 - 1024);
        const unsigned l0 = lds0 + C::LA + la * 4096 + 2048; unsigned l = l0;
        ring_loader_ramp2<RA, NST, 4, 2, 2>([&]() { gl_str4<FUSED>(vp, ab, l); ab += 8 * 32768; l = l == l0 + (RA - 1) * 8192 ? l0 : l + 8192; });
        BAR_L(); BAR_L();
    } else {
        const unsigned aA = (unsigned)(C::LA + (4 * wk + g) * 1024 + (32 * wm + r16) * 16), aB = (unsigned)(wk * 5120 + lane * 16);
        f32x4 acc[2][5];
#pragma unroll
        for (int i = 0; i < 2; ++i)
#pragma unroll
            for (int n = 0; n < 5; ++n) acc[i][n] = (f32x4){0.f, 0.f, 0.f, 0.f};
        bf16x8 Af[3][2], Bf[3][5];
        const int kofs = wk * 256;
        int sa = 0, sb = 0;
        auto rd = [&](int b) {
            lcptr la = (lcptr)L + aA + sa * 8192; lcptr lb = (lcptr)L + aB + sb * 10240;
            Af[b][0] = ld128(la + kofs); Af[b][1] = ld128(la + (256 - kofs));
#pragma unroll
            for (int n = 0; n < 5; ++n) Bf[b][n] = ld128(lb + n * 1024);
            sa = sa == RA - 1 ? 0 : sa + 1; sb = sb == RB - 1 ? 0 : sb + 1;
        };
        auto mm = [&](int b) {
#pragma unroll
            for (int n = 0; n < 5; ++n)
#pragma unroll
                for (int i = 0; i < 2; ++i) acc[i][n] = MFMA16(Bf[b][n], Af[b][i], acc[i][n]);
        };
        auto bar = [&]() { __builtin_amdgcn_sched_barrier(0); __builtin_amdgcn_s_waitcnt(0xC77F); asm volatile("" ::: "memory"); __builtin_amdgcn_s_barrier(); asm volatile("" ::: "memory"); __builtin_amdgcn_sched_barrier(0); };
        auto il = [&]() { SGB(0x8, 1); SGB(0x100, 2); SGB(0x8, 1); SGB(0x100, 2); SGB(0x8, 1); SGB(0x100, 2); SGB(0x8, 1); SGB(0x100, 1); SGB(0x8, 6); __builtin_amdgcn_sched_barrier(0); };
        bar(); STAMP(MODE ? 18 : 10); if (MODE) STAMPC(28); rd(0);
        bar(); rd(1);
        int s = 0;
#pragma unroll 1
        for (; s + 4 < NST; s += 3) {
            bar(); rd(2); mm(0); il();
            bar(); rd(0); mm(1); il();
            bar(); rd(1); mm(2); il();
        }
        if (NST % 3 == 0) { bar(); rd(2); mm(0); il(); mm(1); mm(2); }
        else { bar(); rd(2); mm(0); il(); bar(); rd(0); mm(1); il(); mm(2); mm(0); }
        STAMP(MODE ? 19 : 11); if (MODE) STAMPC(29);
        BAR_L();
        STAMP(MODE ? 46 : 45);
#pragma unroll
        for (int n = 0; n < 5; ++n) *(LAS f32x4*)(L + C::L_X + (wid * 5 + n) * 1024 + lane * 16) = acc[1][n];
        BAR_L();
        STAMP(MODE ? 31 : 30);
        const int row = 32 * wm + 16 * wk + r16;
        f32x4 pv[5]; u32x2 rr[5];
#pragma unroll
        for (int n = 0; n < 5; ++n) { pv[n] = *(const LAS f32x4*)(L + C::L_X + ((wid ^ 1) * 5 + n) * 1024 + lane * 16);
            rr[n] = MODE == 0 ? *(const LAS u32x2*)(L + C::L_RES + row * 160 + n * 32 + g * 8) : *(const LAS u32x2*)(L + C::L_RES + (2 * n + (g >> 1)) * 1024 + row * 16 + (g & 1) * 8); }
        u32x2 o[5]; f32x2 ps2 = {0.f, 0.f}, pq2 = {0.f, 0.f};
#pragma unroll
        for (int n = 0; n < 5; ++n) { const f32x4 v = acc[0][n] + pv[n];
            const f32x2 a = (f32x2){v[0], v[1]} + (f32x2){bflo(rr[n][0]), bfhi(rr[n][0])}, b = (f32x2){v[2], v[3]} + (f32x2){bflo(rr[n][1]), bfhi(rr[n][1])};
            o[n] = (u32x2){pk2(a[0], a[1]), pk2(b[0], b[1])};
            if (MODE == 0) { ps2 += a + b; pq2 += a * a + b * b; } }
        float ps = ps2[0] + ps2[1], pq = pq2[0] + pq2[1];
#pragma unroll
        for (int n = 0; n < 5; n += 2) { const int n1 = n + 1 < 5 ? n + 1 : n;
            auto s0 = __builtin_amdgcn_permlane16_swap(o[n][0], o[n1][0], false, false); auto s1 = __builtin_amdgcn_permlane16_swap(o[n][1], o[n1][1], false, false);
            const u32x4 v = {s0[0], s1[0], s0[1], s1[1]};
            const int ch = 2 * (n + (g & 1)) + (g >> 1);
            if (n + 1 < 5 || !(g & 1)) {
                if (MODE == 0) st16<FUSED>(p.ws + WS_H + (size_t)(10 * ct + ch) * 32768 + (size_t)(r0 + row) * 16, v);
                else st16<OUT_SC1>(p.out + (size_t)(r0 + row) * D_ * 2 + ct * 160 + ch * 16, v); } }
        if (MODE == 0) { ps = sum_x32(sum_x16(ps)); pq = sum_x32(sum_x16(pq));
            if (g == 0) { char* dst = p.ws + WS_PS + ((size_t)(ti * 8 + ct) * 128 + 64 * rh + row) * 8; const u64 v = ((u64)__float_as_uint(pq) << 32) | __float_as_uint(ps);
                if (FUSED) st_dev(dst, v); else *(u64*)dst = v; } }
    }
    STAMP(MODE ? 20 : 12);
    if (MODE == 0) publish<FUSED>(p, FL_C + (ti * 16 + tj) * FS, wid, lane, tag, 43);
}

namespace pd { constexpr int RB = 3, RA = 4, SB = 28672, LA = RB * SB, NST = 10, L_STAT = LA + RA * 16384, L_CF = L_STAT + 1024  , L_STG = LA; }
template <bool FUSED>
DI void d_bissue(const char* wb, unsigned l, int lane) { gl_lin7<FUSED>(lane * 16, wb, l); gl_lin7<FUSED>(lane * 16, wb + 7168, l + 7168); }
template <bool FUSED>
DI void d_prefetch(const Params& p, lptr L, int lane, int wid, int tj) {
    const char* wb = p.ws + WS_PWG + (size_t)tj * 286720 + (wid - 6) * 14336 + 3072;
    const unsigned l = (unsigned)(uintptr_t)L + (wid - 6) * 14336 + 3072;
#pragma unroll 1
    for (int s = 0; s < pd::RB; ++s) d_bissue<FUSED>(wb + (size_t)s * pd::SB, l + s * pd::SB, lane);
}
template <bool FUSED>
DI void phase_d(const Params& p, lptr L, int lane, int wid, int ti, int tj, u64 tag) {
    using namespace pd;
    const unsigned lds0 = (unsigned)(uintptr_t)L;
    if (wid >= 6) {
        const int lb = wid - 6;
        u32x2 ps[8]; float cp[2][16];
        const int row = 64 * lb + lane;
        const char* wb = p.ws + WS_PWG + (size_t)tj * 286720 + lb * 14336 + 3072 + (size_t)RB * SB;
        const unsigned l0 = lds0 + lb * 14336 + 3072; unsigned l = l0;
        auto issue = [&]() { d_bissue<FUSED>(wb, l, lane); wb += SB; l = l == l0 + (RB - 1) * SB ? l0 : l + SB; };
        static_assert(RB == 3 && NST == 10, "the waits below");
        BAR_VL(28);
#pragma unroll
        for (int c = 0; c < 8; ++c) ld64_async(ps[c], p.ws + WS_PS + ((size_t)(ti * 8 + c) * 128 + row) * 8);
        BAR_VL(22); issue();
#pragma unroll
        for (int i = 0; i < 16; ++i) ld32_async(cp[0][i], (const float*)(p.ws + WS_CSP) + (i * 16 + tj) * 224 + row);
        BAR_VL(38); issue();
#pragma unroll
        for (int i = 0; i < 16; ++i) ld32_async(cp[1][i], (const float*)(p.ws + WS_CSP) + (i * 16 + tj) * 224 + (row + 128 < 224 ? row + 128 : 223));
        BAR_VL(46); issue();
        BAR_VL(30); issue();
        BAR_VL(14); issue();
        { float a = 0.f, q = 0.f;
#pragma unroll
          for (int c = 0; c < 8; ++c) { asm volatile("" : "+v"(ps[c])); a += __uint_as_float(ps[c][0]); q += __uint_as_float(ps[c][1]); }
          *(LAS f32x2*)(L + L_STAT + row * 8) = ln_scalars(a, q);
#pragma unroll
          for (int z = 0; z < 2; ++z) { float sm = 0.f;
#pragma unroll
              for (int i = 0; i < 16; ++i) { asm volatile("" : "+v"(cp[z][i])); sm += cp[z][i]; }
              const int c = row + 128 * z;
              if (c < 224) { const float hi = rbf(sm); *(LAS u32x4*)(L + L_CF + (c >> 5) * 1024 + (c & 31) * 16) = (u32x4){pk2(hi, sm - hi), 0u, 0u, 0u};
                  *(LAS u32x4*)(L + L_CF + (c >> 5) * 1024 + 512 + (c & 31) * 16) = (u32x4){0u, 0u, 0u, 0u}; } } }
        BAR_VL(14); issue();
        BAR_VL(14); issue();
        BAR_VL(14);
        if (FUSED) {
#pragma unroll 1
            for (int i = 0; i < 3; ++i) { int ln = (lb * 3 + i) * 64 + lane; ln = ln < 270 ? ln : 269;
                warm64(p.ws + WS_PWD + (size_t)(tj >> 1) * 276480 + (size_t)(2 * (ti & 3) + (tj & 1)) * 34560, (unsigned)ln * 128u, lds0); }
            BAR_VL(3);
        } else BAR_VL(0);
        BAR_L();
    } else if (wid >= 4) {
        const int la = wid - 4;
        if (FUSED) poll_flags(p.ws + WS_FLAG + FL_C + (ti * 16 + (lane & 15)) * FS, lane < 16, tag);
        STAMPW(13, 4);
        const char* ab = p.ws + WS_H + (size_t)(128 * ti + 64 * la) * 16 + 4096;
        unsigned vp[4];
#pragma unroll
        for (int i = 0; i < 4; ++i) vp[i] = lane * 16 + (unsigned)i * (32768 - 2048);
        const unsigned l0 = lds0 + LA + la * 1024 + 4096; unsigned l = l0;
        ring_loader_ramp2<RA, NST, 8, 1, 2>([&]() { gl_str4w<FUSED>(vp, ab, l); gl_str4w<FUSED>(vp, ab + 4 * 32768, l + 8192); ab += 8 * 32768; l = l == l0 + (RA - 1) * 16384 ? l0 : l + 16384; });
        BAR_L();
    } else {
        const int r32 = lane & 31, h = lane >> 5;
        const unsigned aA = (unsigned)(LA + h * 2048 + (32 * wid + r32) * 16), aB = (unsigned)(lane * 16);
        f32x16 acc[7];
#pragma unroll
        for (int t = 0; t < 7; ++t)
#pragma unroll
            for (int i = 0; i < 16; ++i) acc[t][i] = 0.f;
        bf16x8 A0, B0[7], A1, B1[7];
        auto rd = [&](bf16x8& Af, bf16x8* Bf, int sa, int sb, int u) {
            lcptr lb = (lcptr)L + aB + sb * SB + u * 7168;
            Af = ld128((lcptr)L + aA + sa * 16384 + u * 4096);
#pragma unroll
            for (int t = 0; t < 7; ++t) Bf[t] = ld128(lb + t * 1024);
        };
        auto mm = [&](const bf16x8& Af, const bf16x8* Bf) {
#pragma unroll
            for (int t = 0; t < 7; ++t) acc[t] = MFMA32(Bf[t], Af, acc[t]);
        };
        auto il = [&](const bf16x8& Af, const bf16x8* Bf) { SGB(0x8, 1); SGB(0x100, 2); SGB(0x8, 1); SGB(0x100, 2); SGB(0x8, 1); SGB(0x100, 2); SGB(0x8, 1); SGB(0x100, 2); SGB(0x8, 3); __builtin_amdgcn_sched_barrier(0);
            asm volatile("" :: "v"(Af), "v"(Bf[0]), "v"(Bf[1]), "v"(Bf[2]), "v"(Bf[3]), "v"(Bf[4]), "v"(Bf[5]), "v"(Bf[6])); __builtin_amdgcn_sched_barrier(0); };
        CBAR();
        STAMP(14); STAMPC(24);
        rd(A0, B0, 0, 0, 0);
        int sa = 0, sb = 0;
#pragma unroll 1
        for (int s = 0; s < NST - 1; ++s) {
            rd(A1, B1, sa, sb, 1); mm(A0, B0); il(A0, B0);
            rd(A0, B0, sa, sb, 2); mm(A1, B1); il(A1, B1);
            rd(A1, B1, sa, sb, 3); mm(A0, B0); il(A0, B0);
            sa = sa == RA - 1 ? 0 : sa + 1; sb = sb == RB - 1 ? 0 : sb + 1;
            CBAR();
            rd(A0, B0, sa, sb, 0); mm(A1, B1); il(A1, B1);
        }
        rd(A1, B1, sa, sb, 1); mm(A0, B0); il(A0, B0);
        rd(A0, B0, sa, sb, 2); mm(A1, B1); il(A1, B1);
        rd(A1, B1, sa, sb, 3); mm(A0, B0); il(A0, B0);
        mm(A1, B1);
        STAMP(15); STAMPC(25);
        BAR_L();
        STAMP(40);
        const int tok = 32 * wid + r32;
        const f32x2 st = *(const LAS f32x2*)(L + L_STAT + tok * 8);
        const float r2 = st.y * st.y;
        {
            bf16x8 cf[7];
#pragma unroll
            for (int t = 0; t < 7; ++t) cf[t] = ld128((lcptr)L + L_CF + t * 1024 + lane * 16);
            const bf16x8 mf = __builtin_bit_cast(bf16x8, ((u32x4){h ? 0u : pk2(-st.x, -st.x), 0u, 0u, 0u}));
#pragma unroll
            for (int t = 0; t < 7; ++t) acc[t] = MFMA32(cf[t], mf, acc[t]);
        }
        const f32x2 r22 = {r2, r2};
        auto glu4 = [&](float g0, float g1, float g2, float g3, float u0, float u1, float u2, float u3) {
            const f32x2 ya = (f32x2){u0, u1} * r22, yb = (f32x2){u2, u3} * r22;
            const f32x2 oa = (f32x2){__builtin_fmaxf(g0, 0.f), __builtin_fmaxf(g1, 0.f)} * ya, ob = (f32x2){__builtin_fmaxf(g2, 0.f), __builtin_fmaxf(g3, 0.f)} * yb;
            return (u32x2){pk2(oa[0], oa[1]), pk2(ob[0], ob[1])}; };
        char* dst = p.ws + WS_HH + (size_t)(14 * tj + h) * 32768 + (size_t)(128 * ti + tok) * 16;
        const int cmax = 216 - 14 * tj - h;
        auto put = [&](int c, u32x2 x, u32x2 y) {
            auto s0 = __builtin_amdgcn_permlane32_swap(x[0], y[0], false, false); auto s1 = __builtin_amdgcn_permlane32_swap(x[1], y[1], false, false);
            if (c < cmax) st16<FUSED>(dst + (size_t)c * 32768, (u32x4){s0[0], s1[0], s0[1], s1[1]}); };
#pragma unroll
        for (int a = 0; a < 3; ++a)
#pragma unroll
            for (int u = 0; u < 4; u += 2)
                put(4 * a + u, glu4(acc[2 * a][4 * u], acc[2 * a][4 * u + 1], acc[2 * a][4 * u + 2], acc[2 * a][4 * u + 3], acc[2 * a + 1][4 * u], acc[2 * a + 1][4 * u + 1], acc[2 * a + 1][4 * u + 2], acc[2 * a + 1][4 * u + 3]),
                    glu4(acc[2 * a][4 * u + 4], acc[2 * a][4 * u + 5], acc[2 * a][4 * u + 6], acc[2 * a][4 * u + 7], acc[2 * a + 1][4 * u + 4], acc[2 * a + 1][4 * u + 5], acc[2 * a + 1][4 * u + 6], acc[2 * a + 1][4 * u + 7]));
        put(12, glu4(acc[6][0], acc[6][1], acc[6][2], acc[6][3], acc[6][8], acc[6][9], acc[6][10], acc[6][11]), glu4(acc[6][4], acc[6][5], acc[6][6], acc[6][7], acc[6][12], acc[6][13], acc[6][14], acc[6][15]));
        STAMP(41);
    }
    STAMP(16);
    publish<FUSED>(p, FL_D + (ti * 16 + tj) * FS, wid, lane, tag, 44);
}

#define ROTA 2
#define ROTC 0
#define ROTD 0
#define ROTE 1
#define PROLOG() \
    extern __shared__ __attribute__((aligned(16))) char smem[]; \
    lptr L = (lptr)smem; \
    const int tid = threadIdx.x, lane = tid & 63, wid = __builtin_amdgcn_readfirstlane(tid >> 6); \
    const int xcd = blockIdx.x & 7, idx = blockIdx.x >> 3; \
    const int ti = 4 * (xcd & 3) + (idx & 3), tj = 8 * (xcd >> 2) + (idx >> 2); \
    const int rot10 = ((idx >> 2) + 3 * (idx & 3)) % 10, rot27 = ((idx >> 2) * 3 + (idx & 3) * 7) % 27; \
    const int rotC = ROTC ? rot10 : 0, rotD = ROTD ? rot10 : 0, rotE = ROTE ? rot27 : 0, rotA = ROTA == 1 ? rot10 : ROTA == 2 ? (((idx >> 2) + (idx & 3)) & 3) : ROTA == 3 ? ((idx >> 2) & 1) * 5 : 0; \
    (void)rot10; (void)rot27; (void)tid; (void)rotA; (void)rotC; (void)rotD; (void)rotE

__global__ __launch_bounds__(512, 2) void k_fused(const Params p) {
    PROLOG();
    STAMP(0);
    u64 tag = 0; u32x2 ev;
    ld64_async(ev, p.ws + WS_FLAG + FL_E);
    if (tj >= 15) phase_a_stats<true>(p, L, lane, wid, ti, ev, tag); else phase_a<true>(p, L, lane, wid, ti, tj, rotA, ev, tag);
    if (tid == 0) st_dev(p.ws + WS_FLAG + FL_START + blockIdx.x * 8, tag);
    phase_b<true>(p, L, lane, wid, ti, tj, tag);
    if (wid >= 6) {
        poll_flags(p.ws + WS_FLAG + FL_P + ((lane & 15) * 16 + (tj & ~1) + ((lane >> 4) & 1)) * FS, lane < 32, tag);
        ce_prefetch<true, 0>(p, L, lane, wid, tj);
    }
    STAMP(8);
    phase_ce<true, 0>(p, L, lane, wid, ti, tj, tag);
    if (wid >= 6) {
        poll_flags(p.ws + WS_FLAG + FL_B + ((lane & 15) * 16 + (lane < 16 ? tj : (tj & ~1) + ((lane >> 4) & 1))) * FS, lane < 48, tag);
        d_prefetch<true>(p, L, lane, wid, tj);
    }
    if (blockIdx.x == 0 && wid == 5) {
        for (unsigned spins = 0; spins < (1u << 17); ++spins) {
            bool ok = true;
#pragma unroll
            for (int i = 0; i < 4; ++i) ok &= ld_dev(p.ws + WS_FLAG + FL_START + (lane * 4 + i) * 8) == tag;
            if (__all(ok)) break;
            __builtin_amdgcn_s_sleep(1);
        }
        if (lane == 0) st_dev(p.ws + WS_FLAG + FL_E, tag);
    }
    phase_d<true>(p, L, lane, wid, ti, tj, tag);
    if (wid >= 6) ce_prefetch<true, 1>(p, L, lane, wid, tj);
    phase_ce<true, 1>(p, L, lane, wid, ti, tj, tag);
    STAMP(21);
}

extern "C" void kernel_launch(void* const* d_in, const int* in_sizes, int n_in, void* d_out, int out_size, void* d_ws, size_t ws_size, hipStream_t stream) {
    Params p;
    p.x = (const u16*)d_in[0]; p.wqkv = (const u16*)d_in[1]; p.wo = (const u16*)d_in[2];
    p.wg = (const u16*)d_in[4]; p.wu = (const u16*)d_in[5]; p.wd = (const u16*)d_in[6];
    p.out = (char*)d_out; p.ws = (char*)d_ws;
    (void)hipFuncSetAttribute((const void*)k_fused, hipFuncAttributeMaxDynamicSharedMemorySize, LDS_BYTES);
    k_fused<<<256, 512, LDS_BYTES, stream>>>(p);
}
```

```cpp
#include <hip/hip_runtime.h>
#include <hip/hip_bf16.h>
#include <stdint.h>

#define OUT_SC1 false
#define LAS __attribute__((address_space(3)))
#define DI __device__ __forceinline__
typedef unsigned short u16;
typedef unsigned long long u64;
typedef short bf16x8 __attribute__((ext_vector_type(8)));
typedef short s16x4 __attribute__((ext_vector_type(4)));
typedef float f32x2 __attribute__((ext_vector_type(2)));
typedef float f32x4 __attribute__((ext_vector_type(4)));
typedef float f32x16 __attribute__((ext_vector_type(16)));
typedef unsigned u32x2 __attribute__((ext_vector_type(2)));
typedef unsigned u32x4 __attribute__((ext_vector_type(4)));
typedef __bf16 bf16x2_t __attribute__((ext_vector_type(2)));
typedef LAS char* lptr;
typedef LAS const char* lcptr;

static constexpr int S_ = 2048, D_ = 640, H_ = 32, DH_ = 20, F_ = 1728;
static constexpr size_t WS_FLAG = 0;
#define FSTR 128
static constexpr size_t FS = FSTR;
static constexpr size_t FL_E = 0, FL_START = 1024, FL_A = 4096, FL_B = FL_A + 256 * FS, FL_C = FL_B + 256 * FS, FL_D = FL_C + 256 * FS, FL_P = FL_D + 256 * FS, FL_END = FL_P + 256 * FS;
static constexpr size_t WS_QKV = 262144;
static constexpr size_t WS_O = WS_QKV + (size_t)480 * 16384;
static constexpr size_t WS_H = WS_O + (size_t)80 * 32768;
static constexpr size_t WS_HH = WS_H + (size_t)80 * 32768;
static constexpr size_t WS_CSP = WS_HH + (size_t)216 * 32768;
static constexpr size_t WS_PS = WS_CSP + (size_t)16 * 16 * 224 * 4;
static constexpr size_t WS_S1 = WS_PS + (size_t)16 * 8 * 128 * 8;
static constexpr size_t WS_PWO = WS_S1 + (size_t)2048 * 8;
static constexpr size_t WS_PWD = WS_PWO + (size_t)8 * 20 * 5 * 1024;
static constexpr size_t WS_PWG = WS_PWD + (size_t)8 * 54 * 5 * 1024;
static constexpr size_t WS_END = WS_PWG + (size_t)16 * 40 * 7 * 1024;
static constexpr int LDS_BYTES = 163840;
static constexpr float C2_ = 0.22360679774997896f * 1.4426950408889634f;

struct Params { const u16* x; const u16* wqkv; const u16* wo; const u16* wg; const u16* wu; const u16* wd; char* out; char* ws; };

#define STAMP(i) do {} while (0)
#define STAMPC(i) do {} while (0)
#define STAMPW(i, w) do {} while (0)

DI unsigned pk2(float a, float b) { f32x2 v = {a, b}; return __builtin_bit_cast(unsigned, __builtin_convertvector(v, bf16x2_t)); }
DI float bflo(unsigned u) { return __uint_as_float(u << 16); }
DI float bfhi(unsigned u) { return __uint_as_float(u & 0xffff0000u); }
DI float rbf(float a) { return bflo(pk2(a, 0.f) & 0xffffu); }
template <bool SC1> DI void glds16(unsigned voff, const void* sbase, unsigned ldsaddr) {
    unsigned keep;
    if (SC1) asm volatile("s_mov_b32 %0, m0\n\ts_mov_b32 m0, %3\n\ts_nop 0\n\tglobal_load_lds_dwordx4 %1, %2 sc1\n\ts_mov_b32 m0, %0" : "=&s"(keep) : "v"(voff), "s"(sbase), "s"(ldsaddr) : "memory");
    else asm volatile("s_mov_b32 %0, m0\n\ts_mov_b32 m0, %3\n\ts_nop 0\n\tglobal_load_lds_dwordx4 %1, %2\n\ts_mov_b32 m0, %0" : "=&s"(keep) : "v"(voff), "s"(sbase), "s"(ldsaddr) : "memory");
}
#define GL_(v, off, sc) "global_load_lds_dwordx4 %" #v ", %[g] offset:" #off sc "\n\t"
#define GLHEAD "s_mov_b32 m0, %[l]\n\ts_nop 0\n\t"
template <bool SC1> DI void gl_lin5(unsigned v, const char* g, unsigned l) {
    if (SC1) asm volatile(GLHEAD GL_(0, -2048, " sc1") GL_(0, -1024, " sc1") GL_(0, 0, " sc1") GL_(0, 1024, " sc1") GL_(0, 2048, " sc1") :: "v"(v), [g] "s"(g), [l] "s"(l) : "memory");
    else asm volatile(GLHEAD GL_(0, -2048, "") GL_(0, -1024, "") GL_(0, 0, "") GL_(0, 1024, "") GL_(0, 2048, "") :: "v"(v), [g] "s"(g), [l] "s"(l) : "memory");
}
template <bool SC1> DI void gl_lin7(unsigned v, const char* g, unsigned l) {
    if (SC1) asm volatile(GLHEAD GL_(0, -3072, " sc1") GL_(0, -2048, " sc1") GL_(0, -1024, " sc1") GL_(0, 0, " sc1") GL_(0, 1024, " sc1") GL_(0, 2048, " sc1") GL_(0, 3072, " sc1") :: "v"(v), [g] "s"(g), [l] "s"(l) : "memory");
    else asm volatile(GLHEAD GL_(0, -3072, "") GL_(0, -2048, "") GL_(0, -1024, "") GL_(0, 0, "") GL_(0, 1024, "") GL_(0, 2048, "") GL_(0, 3072, "") :: "v"(v), [g] "s"(g), [l] "s"(l) : "memory");
}
template <bool SC1> DI void gl_str4(const unsigned* v, const char* g, unsigned l) {
    if (SC1) asm volatile(GLHEAD GL_(0, -2048, " sc1") GL_(1, -1024, " sc1") GL_(2, 0, " sc1") GL_(3, 1024, " sc1") :: "v"(v[0]), "v"(v[1]), "v"(v[2]), "v"(v[3]), [g] "s"(g), [l] "s"(l) : "memory");
    else asm volatile(GLHEAD GL_(0, -2048, "") GL_(1, -1024, "") GL_(2, 0, "") GL_(3, 1024, "") :: "v"(v[0]), "v"(v[1]), "v"(v[2]), "v"(v[3]), [g] "s"(g), [l] "s"(l) : "memory");
}
template <bool SC1> DI void gl_str4w(const unsigned* v, const char* g, unsigned l) {
    if (SC1) asm volatile(GLHEAD GL_(0, -4096, " sc1") GL_(1, -2048, " sc1") GL_(2, 0, " sc1") GL_(3, 2048, " sc1") :: "v"(v[0]), "v"(v[1]), "v"(v[2]), "v"(v[3]), [g] "s"(g), [l] "s"(l) : "memory");
    else asm volatile(GLHEAD GL_(0, -4096, "") GL_(1, -2048, "") GL_(2, 0, "") GL_(3, 2048, "") :: "v"(v[0]), "v"(v[1]), "v"(v[2]), "v"(v[3]), [g] "s"(g), [l] "s"(l) : "memory");
}
DI void gl_str8(const unsigned* v, const char* g, unsigned l) {
    asm volatile(GLHEAD GL_(0, -4096, "") GL_(1, -3072, "") GL_(2, -2048, "") GL_(3, -1024, "") GL_(4, 0, "") GL_(5, 1024, "") GL_(6, 2048, "") GL_(7, 3072, "")
                 :: "v"(v[0]), "v"(v[1]), "v"(v[2]), "v"(v[3]), "v"(v[4]), "v"(v[5]), "v"(v[6]), "v"(v[7]), [g] "s"(g), [l] "s"(l) : "memory");
}
template <bool SC1> DI void st16(void* ptr, u32x4 v) {
    if (SC1) asm volatile("global_store_dwordx4 %0, %1, off sc1\n\ts_nop 1" :: "v"(ptr), "v"(v) : "memory");
    else *(u32x4*)ptr = v;
}
DI u64 ld_dev(const void* p_) { return __hip_atomic_load((const u64*)p_, __ATOMIC_RELAXED, __HIP_MEMORY_SCOPE_AGENT); }
DI void st_dev(void* p_, u64 v) { __hip_atomic_store((u64*)p_, v, __ATOMIC_RELAXED, __HIP_MEMORY_SCOPE_AGENT); }
DI void ld64_async(u32x2& v, const void* ptr) { asm volatile("global_load_dwordx2 %0, %1, off sc1" : "=v"(v) : "v"(ptr) : "memory"); }
DI void ld32_async(float& v, const void* ptr) { asm volatile("global_load_dword %0, %1, off sc1" : "=v"(v) : "v"(ptr) : "memory"); }
#define VMWAIT(N) asm volatile("s_waitcnt vmcnt(%0)" :: "i"(N) : "memory")
DI void poll_flags(const void* flag, bool need, u64 tag) {
    for (unsigned spins = 0; spins < (1u << 18); ++spins) {
        const u64 v = need ? ld_dev(flag) : tag;
        if (__all(v == tag)) break;
    }
    asm volatile("" ::: "memory");
}
DI void warm64(const void* base, unsigned voff, unsigned lds0) {
    unsigned keep;
    asm volatile("s_mov_b32 %0, m0\n\ts_mov_b32 m0, %3\n\ts_nop 0\n\tglobal_load_lds_dword %1, %2 sc1\n\ts_mov_b32 m0, %0" : "=&s"(keep) : "v"(voff), "s"(base), "s"(lds0 + (unsigned)(LDS_BYTES - 256)) : "memory");
}
#define BAR_L() asm volatile("s_waitcnt lgkmcnt(0)\n\ts_barrier" ::: "memory")
#define BAR_VL(N) asm volatile("s_waitcnt vmcnt(%0) lgkmcnt(0)\n\ts_barrier" :: "i"(N) : "memory")
#define WAIT_LGKM0() do { __builtin_amdgcn_sched_barrier(0); __builtin_amdgcn_s_waitcnt(0xC07F); __builtin_amdgcn_sched_barrier(0); } while (0)
#define CBAR() do { WAIT_LGKM0(); asm volatile("" ::: "memory"); __builtin_amdgcn_s_barrier(); asm volatile("" ::: "memory"); __builtin_amdgcn_sched_barrier(0); } while (0)
#define SGB(mask, n) __builtin_amdgcn_sched_group_barrier(mask, n, 0)
template <int STEP, int MAXN> DI void bar_vl_n(int n) {
    if (n >= MAXN) { BAR_VL((MAXN * STEP > 63 ? 63 : MAXN * STEP)); }
    else if constexpr (MAXN > 0) bar_vl_n<STEP, MAXN - 1>(n);
}
template <int N> struct IC { static constexpr int v = N; };
template <int K, int END, class F> DI void static_for(F&& f) { if constexpr (K < END) { f(IC<K>{}); static_for<K + 1, END>(f); } }
template <int R, int NST, int NP, int LAG, bool PROLOGUE, int NEND = NST, class FI, class FP, class FQ>
DI void ring_loader(FI&& issue, FP&& pre, FQ&& post) {
    constexpr int L0 = (R < NST ? R : NST) - 1;
    if (PROLOGUE) {
#pragma unroll 1
        for (int k = 0; k <= L0; ++k) issue();
    }
    static_for<0, LAG + 1>([&](auto kc) { constexpr int k = decltype(kc)::v; BAR_VL((L0 - k) * NP); pre(k); if constexpr (k >= LAG && k - LAG + R < NST) issue(); post(k); });
    constexpr int KS = NST - R + LAG;
    if constexpr (KS > LAG + 1) {
#pragma unroll 1
        for (int k = LAG + 1; k < KS; ++k) { BAR_VL((R - 1 - LAG) * NP); pre(k); issue(); post(k); }
    }
    static_for<(KS > LAG + 1 ? KS : LAG + 1), NEND>([&](auto kc) { constexpr int k = decltype(kc)::v; BAR_VL((NST - 1 - k) * NP); pre(k); post(k); });
}
template <int R, int NST, int LAG, int P0> constexpr int ramp_issued(int k) { const int r = P0 + 2 * k, lim = k <= LAG ? R : R + k - LAG, v = r < lim ? r : lim; return v < NST ? v : NST; }
template <int R, int NST, int LAG, int P0> constexpr int ramp_len() { int k = LAG + 1; while (k < NST && P0 + 2 * k < R + k - LAG) ++k; return k < NST ? k : NST - 1; }
template <int R, int NST, int NP, int LAG, int P0, bool PROLOGUE = true, class FI>
DI void ring_loader_ramp2(FI&& issue) {
    if (PROLOGUE) static_for<0, P0>([&](auto) { issue(); });
    constexpr int KR = ramp_len<R, NST, LAG, P0>();
    static_for<0, KR>([&](auto kc) { constexpr int k = decltype(kc)::v; constexpr int a = ramp_issued<R, NST, LAG, P0>(k), b = ramp_issued<R, NST, LAG, P0>(k + 1);
        BAR_VL((a - 1 - k) * NP); static_for<a, b>([&](auto) { issue(); }); });
    constexpr int AK = ramp_issued<R, NST, LAG, P0>(KR) - 1 - KR;
    constexpr int KS = NST - 1 - AK;
    if constexpr (KS > KR) {
#pragma unroll 1
        for (int k = KR; k < KS; ++k) { BAR_VL(AK * NP); issue(); }
    }
    static_for<(KS > KR ? KS : KR), NST>([&](auto kc) { constexpr int k = decltype(kc)::v; BAR_VL((NST - 1 - k) * NP); });
}
DI bf16x8 ld128(lcptr p) { return *(const LAS bf16x8*)p; }
DI s16x4 ld64(lcptr p) { return *(const LAS s16x4*)p; }
DI s16x4 ld64v(lcptr p) { return *(const volatile LAS s16x4*)p; }
DI s16x4 ldtr(lcptr p) { typedef short v4 __attribute__((ext_vector_type(4))); return __builtin_bit_cast(s16x4, __builtin_amdgcn_ds_read_tr16_b64_v4i16((LAS v4*)p)); }
DI bf16x8 cat8(s16x4 a, s16x4 b) { return __builtin_shufflevector(a, b, 0, 1, 2, 3, 4, 5, 6, 7); }
#define MFMA16(a, b, c) __builtin_amdgcn_mfma_f32_16x16x32_bf16(a, b, c, 0, 0, 0)
#define MFMA32(a, b, c) __builtin_amdgcn_mfma_f32_32x32x16_bf16(a, b, c, 0, 0, 0)
#define MFMA32K8(a, b, c) __builtin_amdgcn_mfma_f32_32x32x8bf16_1k(a, b, c, 0, 0, 0)
template <int CTRL> DI float dpp_add(float v) { return v + __int_as_float(__builtin_amdgcn_update_dpp(0, __float_as_int(v), CTRL, 0xf, 0xf, true)); }
DI float sum8(float v) { v = dpp_add<0xB1>(v); v = dpp_add<0x4E>(v); return dpp_add<0x141>(v); }
DI float sum_x16(float v) { auto r = __builtin_amdgcn_permlane16_swap(__float_as_uint(v), __float_as_uint(v), false, false); return __uint_as_float(r[0]) + __uint_as_float(r[1]); }
DI float sum_x32(float v) { auto r = __builtin_amdgcn_permlane32_swap(__float_as_uint(v), __float_as_uint(v), false, false); return __uint_as_float(r[0]) + __uint_as_float(r[1]); }
DI bf16x8 ones8() { bf16x8 o = {0x3f80, 0x3f80, 0x3f80, 0x3f80, 0x3f80, 0x3f80, 0x3f80, 0x3f80}; asm volatile("" : "+v"(o)); return o; }

DI f32x2 ln_scalars(float sum, float sq) {
    const float mean = sum * (1.0f / D_);
    float var = sq * (1.0f / D_) - mean * mean; var = var > 0.f ? var : 0.f;
    const float sd = rbf(__builtin_sqrtf(rbf(rbf(var) + 1e-5f)));
    return (f32x2){rbf(mean), 1.0f / sd};
}
template <bool FUSED> DI void publish(const Params& p, size_t flag_off, int wid, int lane, u64 tag, int sidx = 47) {
    if (FUSED) { BAR_VL(0); STAMP(sidx); if (wid == 0 && lane == 0) st_dev(p.ws + WS_FLAG + flag_off, tag); }
}

namespace rp { constexpr int L_G = 132608, L_U = L_G + 10752, L_O = L_G + 21504, L_D = 102400; }
DI const char* rp1_addr(const Params& p, int lane, int n, int ti, int tj) {
    const int ns = ti < 8 ? 3 : 2, s0 = ti < 8 ? 3 * ti : 24 + 2 * (ti - 8);
    const int ct = tj >> 1, z = 2 * ti + (tj & 1);
    if (n < 21) { int u = 64 * n + lane; const int mat = u >= 672; u -= 672 * mat; int row = u / 14; int col = 112 * tj + 8 * (u - 14 * row); col = col > F_ - 8 ? F_ - 8 : col;
        row = row < 16 * ns ? row : 16 * ns - 1;
        return (const char*)((mat ? p.wu : p.wg) + (size_t)(16 * s0 + row) * F_ + col); }
    const int m = n < 26 ? n - 21 : 4, u = 64 * m + lane, row = u / 10, c = u - 10 * row;
    return (const char*)(p.wo + (size_t)(32 * (z < 20 ? z : 0) + row) * D_ + 80 * ct + 8 * c);
}
DI void glds16v(const void* vaddr, unsigned ldsaddr) {
    asm volatile("s_nop 0\n\ts_mov_b32 m0, %1\n\ts_nop 0\n\tglobal_load_lds_dwordx4 %0, off" :: "v"(vaddr), "s"(ldsaddr) : "memory");
}
template <int I0 = 0, int I1 = 7>
DI void rp1_issue(const Params& p, unsigned lds0, int lane, int lw, int ti, int tj) {
#pragma unroll
    for (int i = I0; i < I1; ++i) { const int n = lw + 4 * i; glds16v(rp1_addr(p, lane, n, ti, tj), (unsigned)__builtin_amdgcn_readfirstlane(lds0 + rp::L_G + (n < 26 ? n : 25) * 1024)); }
}
template <bool FUSED>
DI void rp1_store(const Params& p, lptr L, int lane, int lw, int ti, int tj, int part = 3) {
    const int ns = ti < 8 ? 3 : 2, s0 = ti < 8 ? 3 * ti : 24 + 2 * (ti - 8);
    const int ct = tj >> 1, z = 2 * ti + (tj & 1);
    const int h = lane >> 5, blk = (lane >> 4) & 1, g = lane >> 4, q = (lane & 15) >> 2, pp = lane & 3;
    const bf16x8 ones = ones8();
#pragma unroll 1
    for (int t = lw + (part == 1 ? 4 : 0); t < (part == 0 ? 4 : part == 2 ? 0 : 7); t += 4) {
        lcptr src = (lcptr)L + (t < 6 ? ((t & 1) ? rp::L_U : rp::L_G) + (32 * (t >> 1) + 16 * blk + 4 * pp) * 2 : (blk ? rp::L_U : rp::L_G) + (96 + 4 * pp) * 2) + (8 * h + q) * 224;
        s16x4 f[3][2];
#pragma unroll
        for (int s = 0; s < 3; ++s) { f[s][0] = ldtr(src + s * 16 * 224); f[s][1] = ldtr(src + s * 16 * 224 + 4 * 224); }
        f32x16 cs = {};
        char* dst = p.ws + WS_PWG + ((size_t)(tj * 40 + s0) * 7 + t) * 1024 + lane * 16;
#pragma unroll
        for (int s = 0; s < 3; ++s) if (s < ns) { const bf16x8 fr = cat8(f[s][0], f[s][1]); st16<FUSED>(dst + s * 7168, __builtin_bit_cast(u32x4, fr)); cs = MFMA32(fr, ones, cs); }
        if ((lane & 31) == 0) { float* cd = (float*)(p.ws + WS_CSP) + (ti * 16 + tj) * 224 + 32 * t + 4 * h;
#pragma unroll
            for (int u = 0; u < 4; ++u) st16<FUSED>(cd + 8 * u, __builtin_bit_cast(u32x4, ((f32x4){cs[4 * u], cs[4 * u + 1], cs[4 * u + 2], cs[4 * u + 3]}))); }
    }
    if (z < 20 && (part == 2 || part == 3)) {
#pragma unroll 1
        for (int nt = 3 - lw; nt < 5; nt += 4) {
            lcptr src = (lcptr)L + rp::L_O + (8 * g + q) * 160 + (16 * nt + 4 * pp) * 2;
            st16<FUSED>(p.ws + WS_PWO + ((size_t)(ct * 20 + z) * 5 + nt) * 1024 + lane * 16, __builtin_bit_cast(u32x4, cat8(ldtr(src), ldtr(src + 4 * 160)))); }
    }
}
DI void rp2_issue(const Params& p, unsigned lds0, int lane, int wid, int ti, int tj) {
    const int ct = tj >> 1, z = 2 * ti + (tj & 1), np = z + 32 < 54 ? 10 : 5;
#pragma unroll 1
    for (int n = wid; n < np; n += 4) { const int im = n >= 5, u = 64 * (n - 5 * im) + lane, row = u / 10, c = u - 10 * row;
        glds16<false>((unsigned)((32 * (z + 32 * im) + row) * D_ + 80 * ct + 8 * c) * 2u, p.wd, lds0 + rp::L_D + n * 1024); }
}
template <bool FUSED>
DI int rp2_store(const Params& p, lptr L, int lane, int wid, int ti, int tj) {
    const int ct = tj >> 1, z = 2 * ti + (tj & 1), np = z + 32 < 54 ? 10 : 5;
    const int g = lane >> 4, q = (lane & 15) >> 2, pp = lane & 3;
    int cnt = 0;
#pragma unroll 1
    for (int n = wid; n < np; n += 4, ++cnt) { const int im = n >= 5, nt = n - 5 * im;
        lcptr src = (lcptr)L + rp::L_D + im * 5120 + (8 * g + q) * 160 + (16 * nt + 4 * pp) * 2;
        st16<FUSED>(p.ws + WS_PWD + ((size_t)(ct * 54 + z + 32 * im) * 5 + nt) * 1024 + lane * 16, __builtin_bit_cast(u32x4, cat8(ldtr(src), ldtr(src + 4 * 160)))); }
    return cnt;
}
DI u64 make_tag(u32x2& ev) {
    asm volatile("" : "+v"(ev));
    const u64 e = (((u64)(unsigned)__builtin_amdgcn_readfirstlane(ev[1]) << 32) | (unsigned)__builtin_amdgcn_readfirstlane(ev[0])) + 1;
    return e;
}

namespace pa { constexpr int R = 4, NST = 10, LA = 0, LB = R * 16384, L_STAT = 2 * R * 16384, L_CS = L_STAT + 1024; }
template <bool FUSED>
DI void phase_a_stats(const Params& p, lptr L, int lane, int wid, int ti, u32x2& ev, u64& tag) {
    const int r = lane >> 3, c = lane & 7;
    if (wid >= 4) rp1_issue(p, (unsigned)(uintptr_t)L, lane, wid - 4, ti, 15);
#pragma unroll
    for (int hlf = 0; hlf < 2; ++hlf) {
        const int row = 128 * ti + 16 * wid + 8 * hlf + r;
        const u32x4* src = (const u32x4*)(p.x + (size_t)row * D_) + c;
        u32x4 v[10];
#pragma unroll
        for (int i = 0; i < 10; ++i) v[i] = src[8 * i];
        float a = 0.f, q = 0.f;
#pragma unroll
        for (int i = 0; i < 10; ++i)
#pragma unroll
            for (int e = 0; e < 4; ++e) { const float lo = bflo(v[i][e]), hi = bfhi(v[i][e]); a += lo + hi; q += lo * lo + hi * hi; }
        const f32x2 st = ln_scalars(sum8(a), sum8(q));
        if (c == 0) { const u64 w = ((u64)__float_as_uint(st.y) << 32) | __float_as_uint(st.x); char* dst = p.ws + WS_S1 + (size_t)row * 8; if (FUSED) st_dev(dst, w); else *(u64*)dst = w; }
    }
    BAR_VL(0);
    if (FUSED) { tag = make_tag(ev); if (wid == 0 && lane == 0) st_dev(p.ws + WS_FLAG + FL_A + (ti * 16 + 15) * FS, tag); }
    if (wid >= 4) rp1_store<FUSED>(p, L, lane, wid - 4, ti, 15);
    if (FUSED) { BAR_VL(0); if (wid == 0 && lane == 0) st_dev(p.ws + WS_FLAG + FL_P + (ti * 16 + 15) * FS, tag); }
}
template <bool FUSED>
DI void phase_a(const Params& p, lptr L, int lane, int wid, int ti, int tj, int rot, u32x2& ev, u64& tag) {
    using namespace pa;
    const unsigned lds0 = (unsigned)(uintptr_t)L;
    if (__builtin_expect(wid >= 4, 1)) {
        const int lw = wid - 4, r = lane >> 3, c = lane & 7;
        const bool isA = lw < 2;
        unsigned vo[2];
#pragma unroll
        for (int e = 0; e < 2; ++e) vo[e] = isA ? (unsigned)(r * D_ + ((c ^ ((4 * e + (r >> 1)) & 7)) * 8)) * 2u : (unsigned)(r * 3 * D_ + ((c ^ (4 * ((r >> 1) & 1) + 2 * e)) * 8)) * 2u;
        unsigned vp[8];
#pragma unroll
        for (int i = 0; i < 8; ++i) vp[i] = vo[i & 1] + (isA ? (unsigned)i * (8 * D_ * 2 - 1024) : (unsigned)(i & 3) * (8 * 3 * D_ * 2 - 1024));
        const char* gb = isA ? (const char*)p.x + (size_t)(128 * ti + 64 * lw) * D_ * 2 + 4096 + rot * 128 : (const char*)p.wqkv + ((size_t)(32 * (lw - 2) + 64 * rot) * 3 * D_ + 128 * tj) * 2 + 2048;
        const int gstep = isA ? 128 : 64 * 3 * D_ * 2;
        int ks = rot; unsigned sl = lds0 + (isA ? LA + lw * 8192 + 4096 : LB + (lw - 2) * 4096 + 2048), sslot = 0;
        auto issue = [&]() {
            if (isA) gl_str8(vp, gb, sl + sslot); else { gl_str4<false>(vp, gb, sl + sslot); gl_str4<false>(vp, gb + 128, sl + sslot + 8192); }
            gb += gstep; if (++ks == NST) { ks = 0; gb -= (size_t)NST * gstep; }
            sslot = (sslot + 16384) & (R * 16384 - 1);
        };
        const int g = lane >> 4, q = (lane & 15) >> 2, pp = lane & 3;
        unsigned aC[2];
#pragma unroll
        for (int t = 0; t < 2; ++t) aC[t] = (unsigned)(LB + ((lw >> 1) * 8 + g) * 1024 + q * 128 + (((2 * (2 * (lw & 1) + t) + (pp >> 1)) ^ (4 * ((q >> 1) & 1) + 2 * (g & 1))) * 16) + (pp & 1) * 8);
        f32x4 cs[2] = {{0.f, 0.f, 0.f, 0.f}, {0.f, 0.f, 0.f, 0.f}};
        const bf16x8 ones = ones8();
        s16x4 cf[2][2][2];
        __builtin_amdgcn_s_setprio(3);
        auto pre = [&](int s) { lcptr q0 = (lcptr)L + (s & 3) * 16384;
#pragma unroll
            for (int ks2 = 0; ks2 < 2; ++ks2)
#pragma unroll
                for (int t = 0; t < 2; ++t) { cf[ks2][t][0] = ldtr(q0 + aC[t] + ks2 * 4096); cf[ks2][t][1] = ldtr(q0 + aC[t] + ks2 * 4096 + 512); } };
        auto post = [&](int) {
#pragma unroll
            for (int ks2 = 0; ks2 < 2; ++ks2)
#pragma unroll
                for (int t = 0; t < 2; ++t) cs[t] = MFMA16(cat8(cf[ks2][t][0], cf[ks2][t][1]), ones, cs[t]); };
        issue(); issue();
        BAR_VL(8); pre(0); issue();
        if (FUSED) tag = make_tag(ev);
        BAR_VL(8); post(0); pre(1); issue();
        BAR_VL(8); post(1); pre(2); issue();
        BAR_VL(8); post(2); pre(3); issue(); rp1_issue<0, 2>(p, lds0, lane, lw, ti, tj);
        BAR_VL(10); post(3); pre(4); issue(); rp1_issue<2, 4>(p, lds0, lane, lw, ti, tj);
        BAR_VL(12); post(4); pre(5); issue(); rp1_issue<4, 7>(p, lds0, lane, lw, ti, tj);
        BAR_VL(13); post(5); pre(6); issue();
        u32x2 sf, sv;
        if (FUSED) ld64_async(sf, p.ws + WS_FLAG + FL_A + (ti * 16 + 15) * FS);
        BAR_VL(FUSED ? 12 : 11); post(6); pre(7); issue();
        BAR_VL(8); post(7); pre(8);
        if (FUSED) { asm volatile("" : "+v"(sf)); if (!__all((((u64)sf[1] << 32) | sf[0]) == tag)) poll_flags(p.ws + WS_FLAG + FL_A + (ti * 16 + 15) * FS, lane == 0, tag); }
        ld64_async(sv, p.ws + WS_S1 + (size_t)(128 * ti + 64 * (lw & 1) + lane) * 8);
        BAR_VL(0); post(8); pre(9); post(9);
        asm volatile("" : "+v"(sv));
        if (isA) *(LAS u32x2*)(L + L_STAT + (64 * lw + lane) * 8) = sv;
        __builtin_amdgcn_s_setprio(0);
        if ((lane & 15) == 0) {
#pragma unroll
            for (int t = 0; t < 2; ++t) *(LAS f32x4*)(L + L_CS + (32 * lw + 16 * t + 4 * g) * 4) = cs[t];
        }
        BAR_L();
        BAR_L();
    } else {
        const int wm = wid >> 1, wn = wid & 1, r32 = lane & 31, h = lane >> 5, blk = (lane >> 4) & 1, q = (lane & 15) >> 2, pp = lane & 3;
        unsigned aX[4], aW[2];
        { const int row = 64 * wm + r32;
#pragma unroll
          for (int u = 0; u < 4; ++u) aX[u] = (unsigned)(LA + (row >> 3) * 1024 + (row & 7) * 128 + (((2 * u + h) ^ ((row >> 1) & 7)) * 16));
#pragma unroll
          for (int nt = 0; nt < 2; ++nt) aW[nt] = (unsigned)(LB + (wn * 8 + h) * 1024 + q * 128 + (((4 * nt + 2 * blk + (pp >> 1)) ^ (4 * ((q >> 1) & 1) + 2 * h)) * 16) + (pp & 1) * 8); }
        f32x16 acc[2][2];
#pragma unroll
        for (int i = 0; i < 16; ++i) { acc[0][0][i] = 0.f; acc[0][1][i] = 0.f; acc[1][0][i] = 0.f; acc[1][1][i] = 0.f; }
        bf16x8 X[4][2], W[4][2];
        auto rd = [&](int slot, int u) {
            lcptr sl = (lcptr)L + slot * 16384;
#pragma unroll
            for (int nt = 0; nt < 2; ++nt) W[u][nt] = cat8(ldtr(sl + aW[nt] + u * 2048), ldtr(sl + aW[nt] + u * 2048 + 512));
#pragma unroll
            for (int mt = 0; mt < 2; ++mt) X[u][mt] = ld128(sl + aX[u] + mt * 4096);
        };
        auto mm = [&](int u) {
#pragma unroll
            for (int mt = 0; mt < 2; ++mt)
#pragma unroll
                for (int nt = 0; nt < 2; ++nt) acc[mt][nt] = MFMA32(W[u][nt], X[u][mt], acc[mt][nt]);
        };
        auto il = [&]() { SGB(0x8, 1); SGB(0x100, 2); SGB(0x8, 1); SGB(0x100, 2); SGB(0x8, 1); SGB(0x100, 2); SGB(0x8, 1); __builtin_amdgcn_sched_barrier(0); };
        auto bar = [&]() { __builtin_amdgcn_sched_barrier(0); asm volatile("" ::: "memory"); __builtin_amdgcn_s_barrier(); asm volatile("" ::: "memory"); __builtin_amdgcn_sched_barrier(0); };
        bar();
        if (FUSED) VMWAIT(0);
        STAMP(1); STAMPC(26);
        rd(0, 0); rd(0, 1);
#pragma unroll 1
        for (int s = 0; s < NST - 1; ++s) {
            const int c = s & 3, n = (s + 1) & 3;
            rd(c, 2); mm(0); il();
            rd(c, 3); mm(1); il();
            bar();
            rd(n, 0); mm(2); il();
            rd(n, 1); mm(3); il();
        }
        rd((NST - 1) & 3, 2); mm(0); il();
        rd((NST - 1) & 3, 3); mm(1); il();
        mm(2); mm(3);
        STAMP(2); STAMPC(27);
        if (FUSED) tag = make_tag(ev);
        BAR_L();
        STAMP(38);
        {
            f32x2 st[2]; float cv[2];
#pragma unroll
            for (int i = 0; i < 2; ++i) { st[i] = *(const LAS f32x2*)(L + L_STAT + (64 * wm + 32 * i + r32) * 8); cv[i] = *(const LAS float*)(L + L_CS + (64 * wn + 32 * i + r32) * 4); }
            bf16x8 cf[2], mf[2];
#pragma unroll
            for (int i = 0; i < 2; ++i) { const float hi = rbf(cv[i]); const unsigned c = h ? 0u : pk2(hi, cv[i] - hi), m = h ? 0u : pk2(-st[i].x, -st[i].x);
                cf[i] = __builtin_bit_cast(bf16x8, ((u32x4){c, 0u, 0u, 0u})); mf[i] = __builtin_bit_cast(bf16x8, ((u32x4){m, 0u, 0u, 0u})); }
#pragma unroll
            for (int mt = 0; mt < 2; ++mt)
#pragma unroll
                for (int nt = 0; nt < 2; ++nt) acc[mt][nt] = MFMA32(cf[nt], mf[mt], acc[mt][nt]);
#pragma unroll
            for (int mt = 0; mt < 2; ++mt) { const int tok = 64 * wm + 32 * mt + r32; const float sq = st[mt].y * C2_;
#pragma unroll
                for (int nt = 0; nt < 2; ++nt)
#pragma unroll
                    for (int u = 0; u < 4; ++u) { const int col = 64 * wn + 32 * nt + 8 * u + 4 * h;
                        const float sc = (128 * tj + col < D_) ? sq : st[mt].y;
                        *(LAS u32x2*)(L + (col >> 2) * 1024 + tok * 8) = (u32x2){pk2(acc[mt][nt][4 * u] * sc, acc[mt][nt][4 * u + 1] * sc), pk2(acc[mt][nt][4 * u + 2] * sc, acc[mt][nt][4 * u + 3] * sc)}; } }
        }
        BAR_L();
        STAMP(39);
    }
    char* dst = p.ws + WS_QKV + (size_t)(32 * tj) * 16384 + (size_t)ti * 1024;
#pragma unroll
    for (int i = 0; i < 4; ++i) { const int gq = 4 * wid + i; st16<FUSED>(dst + (size_t)gq * 16384 + lane * 16, *(const LAS u32x4*)(L + gq * 1024 + lane * 16)); }
    STAMP(3);
    publish<FUSED>(p, FL_A + (ti * 16 + tj) * FS, wid, lane, tag, 42);
}

namespace pb {
constexpr int GS = 3104;
constexpr int L_K = 0, L_V = 10 * GS, L_ONE = 20 * GS, L_Q = 21 * GS, L_O = 112640;
static_assert(L_Q + 10240 <= L_O, "LDS map");
}
#define ATT_STAMP() do {} while (0)
template <bool EDGE>
DI f32x16 attn_wave(lcptr L, int hh, int qb, int lane, int tstart, u64* stq) {
    using namespace pb;
    const int r = lane & 31, h = lane >> 5;
    bf16x8 q0; s16x4 q1;
    { lcptr qp = L + L_Q + hh * 5 * 1024 + (32 * qb + r) * 8;
      q0 = cat8(ld64(qp + (2 * h) * 1024), ld64(qp + (2 * h + 1) * 1024));
      const s16x4 t = ld64(qp + 4 * 1024); q1 = h ? (s16x4){0, 0, 0, 0} : t; }
    lcptr kp = L + L_K + hh * 5 * GS + (32 * qb + r) * 8;
    const f32x16 z16 = {};
    const int rr_ = r - 4 * h;
    bf16x8 k0[9]; s16x4 k1[9];
#pragma unroll
    for (int t = 0; t < 9; ++t) { k0[t] = cat8(ld64v(kp + (2 * h) * GS + t * 256), ld64v(kp + (2 * h + 1) * GS + t * 256)); k1[t] = ld64v(kp + 4 * GS + t * 256); }
    float m; f32x16 sn;
    { f32x16 a = MFMA32K8(k1[8], q1, MFMA32(k0[8], q0, z16)), b = MFMA32K8(k1[0], q1, MFMA32(k0[0], q0, z16));
      sn = MFMA32K8(k1[1], q1, MFMA32(k0[1], q0, z16)); asm volatile("" : "+v"(sn));
      __builtin_amdgcn_sched_barrier(0);
      const float bias = (!EDGE || tstart == 0) ? 0.f : -INFINITY;
#pragma unroll
      for (int i = 0; i < 16; ++i) a[i] = ((i & 3) + 8 * (i >> 2) <= rr_) ? a[i] : (EDGE ? b[i] + bias : b[i]);
      float x = __builtin_fmaxf(__builtin_fmaxf(a[0], a[1]), a[2]), y = __builtin_fmaxf(__builtin_fmaxf(a[3], a[4]), a[5]);
#pragma unroll
      for (int i = 6; i < 14; i += 4) { x = __builtin_fmaxf(__builtin_fmaxf(x, a[i]), a[i + 1]); y = __builtin_fmaxf(__builtin_fmaxf(y, a[i + 2]), a[i + 3]); }
      m = __builtin_fmaxf(__builtin_fmaxf(__builtin_fmaxf(x, a[14]), a[15]), y); }
#pragma unroll
    for (int t = 1; t < 8; ++t) { const f32x16 a = sn;
        if (t < 7) { sn = MFMA32K8(k1[t + 1], q1, MFMA32(k0[t + 1], q0, z16)); asm volatile("" : "+v"(sn)); }
        __builtin_amdgcn_sched_barrier(0);
        float x = __builtin_fmaxf(__builtin_fmaxf(a[0], a[1]), a[2]), y = __builtin_fmaxf(__builtin_fmaxf(a[3], a[4]), a[5]);
#pragma unroll
        for (int i = 6; i < 14; i += 4) { x = __builtin_fmaxf(__builtin_fmaxf(x, a[i]), a[i + 1]); y = __builtin_fmaxf(__builtin_fmaxf(y, a[i + 2]), a[i + 3]); }
        x = __builtin_fmaxf(__builtin_fmaxf(x, a[14]), a[15]);
        if (EDGE) x = t >= tstart ? x : -INFINITY;
        m = __builtin_fmaxf(__builtin_fmaxf(m, x), (EDGE && t < tstart) ? -INFINITY : y);
        __builtin_amdgcn_sched_barrier(0); }
    { auto rr = __builtin_amdgcn_permlane32_swap(__float_as_uint(m), __float_as_uint(m), false, false); m = __builtin_fmaxf(__uint_as_float(rr[0]), __uint_as_float(rr[1])); }
    f32x16 nm16;
#pragma unroll
    for (int i = 0; i < 16; ++i) nm16[i] = -m;
    ATT_STAMP();
    const int blk = (lane >> 4) & 1, vq = (lane & 15) >> 2, vp = lane & 3, dg = 4 * blk + vp;
    lcptr vpz = L + (dg < 5 ? L_V + (hh * 5 + dg) * GS : L_ONE) + (32 * qb + 4 * h + vq) * 8;
    f32x16 O = z16;
    f32x16 an = MFMA32K8(k1[1], q1, MFMA32(k0[1], q0, nm16));
#pragma unroll
    for (int t = 1; t < 8; ++t) {
        const bf16x8 v0 = cat8(ldtr(vpz + t * 256), ldtr(vpz + t * 256 + 64)), v1 = cat8(ldtr(vpz + t * 256 + 128), ldtr(vpz + t * 256 + 192));
        f32x16 a = an;
        an = MFMA32K8(k1[t + 1], q1, MFMA32(k0[t + 1], q0, nm16));
        asm volatile("" : "+v"(an));
        __builtin_amdgcn_sched_barrier(0);
        if (EDGE) { const float bias = t >= tstart ? 0.f : -INFINITY;
#pragma unroll
            for (int i = 0; i < 16; ++i) a[i] += bias; }
        unsigned P[8];
#pragma unroll
        for (int i = 0; i < 8; ++i) P[i] = pk2(__builtin_amdgcn_exp2f(a[2 * i]), __builtin_amdgcn_exp2f(a[2 * i + 1]));
        O = MFMA32(v0, __builtin_bit_cast(bf16x8, ((u32x4){P[0], P[1], P[2], P[3]})), O);
        O = MFMA32(v1, __builtin_bit_cast(bf16x8, ((u32x4){P[4], P[5], P[6], P[7]})), O);
        __builtin_amdgcn_sched_barrier(0);
    }
    { const bf16x8 v0 = cat8(ldtr(vpz + 8 * 256), ldtr(vpz + 8 * 256 + 64)), v1 = cat8(ldtr(vpz + 8 * 256 + 128), ldtr(vpz + 8 * 256 + 192));
      const bf16x8 w0 = cat8(ldtr(vpz), ldtr(vpz + 64)), w1 = cat8(ldtr(vpz + 128), ldtr(vpz + 192));
      f32x16 a = an; const f32x16 b = MFMA32K8(k1[0], q1, MFMA32(k0[0], q0, nm16));
      const float bias = (!EDGE || tstart == 0) ? 0.f : -INFINITY;
#pragma unroll
      for (int i = 0; i < 16; ++i) a[i] = ((i & 3) + 8 * (i >> 2) <= rr_) ? a[i] : (EDGE ? b[i] + bias : b[i]);
      unsigned P[8], P0[8];
#pragma unroll
      for (int i = 0; i < 8; ++i) { P[i] = pk2(__builtin_amdgcn_exp2f(a[2 * i]), __builtin_amdgcn_exp2f(a[2 * i + 1]));
          const int c0 = ((2 * i) & 3) + 8 * ((2 * i) >> 2); const unsigned mk = (c0 + 1 <= rr_) ? 0xffffffffu : (c0 <= rr_ ? 0xffffu : 0u);
          P0[i] = P[i] & ~mk; P[i] &= mk; }
      O = MFMA32(v0, __builtin_bit_cast(bf16x8, ((u32x4){P[0], P[1], P[2], P[3]})), O);
      O = MFMA32(v1, __builtin_bit_cast(bf16x8, ((u32x4){P[4], P[5], P[6], P[7]})), O);
      O = MFMA32(w0, __builtin_bit_cast(bf16x8, ((u32x4){P0[0], P0[1], P0[2], P0[3]})), O);
      O = MFMA32(w1, __builtin_bit_cast(bf16x8, ((u32x4){P0[4], P0[5], P0[6], P0[7]})), O); }
    return O;
}
template <bool FUSED>
DI void phase_b(const Params& p, lptr L, int lane, int wid, int ti, int tj, u64 tag) {
    using namespace pb;
    const int tid = wid * 64 + lane;
    const unsigned lds0 = (unsigned)(uintptr_t)L;
    if (!FUSED) { if (wid >= 4) rp1_issue(p, lds0, lane, wid - 4, ti, tj); BAR_VL(0); }
    if (wid < 4) rp2_issue(p, lds0, lane, wid, ti, tj);
    else if (tj < 15) rp1_store<FUSED>(p, L, lane, wid - 4, ti, tj, 2);
    if (ti < 2) {
        for (int n = tid; n < (2 - ti) * 20 * 64; n += 512) { const int pc = n >> 6, mb = pc % (2 - ti), gq = pc / (2 - ti);
            *(LAS u32x4*)(L + gq * GS + mb * 1024 + (n & 63) * 16) = (u32x4){0u, 0u, 0u, 0u}; }
    }
    if (tid < 384) *(LAS u32x2*)(L + L_ONE + tid * 8) = (u32x2){0x00003f80u, 0u};
    const int sub = wid & 3, mtile = sub < 3 ? ti - 2 + sub : ti;
    const char* g0 = p.ws + WS_QKV + (size_t)((sub < 3 ? 160 : 0) + 5 * (2 * tj + (wid >> 2))) * 16384 + (size_t)(mtile < 0 ? 0 : mtile) * 1024;
    const unsigned l0 = lds0 + (sub < 3 ? L_K + (wid >> 2) * 5 * GS + sub * 1024 : L_Q + (wid >> 2) * 5120);
    if (wid < 4) BAR_VL(0);
    else {
        BAR_L();
        if (FUSED && wid == 7) {
            const int mb = lane / 6, sec = (lane % 6) >> 1, e = lane & 1, mt2 = ti - 2 + mb;
            const bool need = lane < 18 && mt2 >= 0 && (sec > 0 || mb == 2);
            poll_flags(p.ws + WS_FLAG + FL_A + ((mt2 < 0 ? 0 : mt2) * 16 + ((640 * sec + 40 * tj + 39 * e) >> 7)) * FS, need, tag); STAMPW(35, 7); }
    }
    BAR_L();
    STAMP(4);
    if (sub == 3) {
#pragma unroll
        for (int d = 0; d < 5; ++d) glds16<FUSED>(lane * 16, g0 + d * 16384, l0 + d * 1024);
    } else if (mtile >= 0) {
#pragma unroll
        for (int d = 0; d < 5; ++d) glds16<FUSED>(lane * 16, g0 + d * 16384, l0 + d * GS);
#pragma unroll
        for (int d = 0; d < 5; ++d) glds16<FUSED>(lane * 16, g0 + (160 + d) * 16384, l0 + L_V + d * GS);
    }
    BAR_VL(0);
    if (FUSED && tid == 0 && tj < 15) st_dev(p.ws + WS_FLAG + FL_P + (ti * 16 + tj) * FS, tag);
    STAMP(5);
    const int hh = wid >> 2, qb = wid & 3, r = lane & 31, h = lane >> 5;
    const int q0g = 128 * ti + 32 * qb;
    f32x16 O;
    u64* stq = nullptr;
    if (ti >= 2) O = attn_wave<false>((lcptr)L, hh, qb, lane, 0, stq); else O = attn_wave<true>((lcptr)L, hh, qb, lane, q0g >= 256 ? 0 : 8 - (q0g >> 5), stq);
    { auto rr = __builtin_amdgcn_permlane32_swap(__float_as_uint(O[8]), __float_as_uint(O[8]), false, false);
      const float il = 1.0f / __uint_as_float(rr[1]);
      const int row = 32 * qb + r;
#pragma unroll
      for (int u = 0; u < 3; ++u) { const int dgp = 2 * u + h; if (dgp < 5) { const int cgp = 5 * hh + dgp;
          *(LAS u32x2*)(L + L_O + (cgp >> 1) * 2048 + row * 16 + (cgp & 1) * 8) = (u32x2){pk2(O[4 * u] * il, O[4 * u + 1] * il), pk2(O[4 * u + 2] * il, O[4 * u + 3] * il)}; } } }
    if (wid < 4) { if (tj < 15) rp1_store<FUSED>(p, L, lane, wid, ti, tj, 4); (void)rp2_store<FUSED>(p, L, lane, wid, ti, tj); }
    STAMP(6); STAMPW(37, 7);
    BAR_L();
    for (int n = wid; n < 10; n += 8)
        st16<FUSED>(p.ws + WS_O + (size_t)(5 * tj + (n >> 1)) * 32768 + (size_t)(128 * ti + 64 * (n & 1)) * 16 + lane * 16, *(const LAS u32x4*)(L + L_O + n * 1024 + lane * 16));
    STAMP(7);
    publish<FUSED>(p, FL_B + (ti * 16 + tj) * FS, wid, lane, tag);
}

template <int MODE> struct PC;
template <> struct PC<0> { static constexpr int NST = 10, RB = 10, RA = 6, LA = 102400, L_RES = 151552, L_X = 102400, L_STG = 122880; };
template <> struct PC<1> { static constexpr int NST = 27, RB = 10, RA = 5, LA = 102400, L_RES = 143360, L_X = 102400, L_STG = 102400; };
#define EPRE 8
template <bool FUSED, int MODE>
DI void ce_prefetch(const Params& p, lptr L, int lane, int wid, int tj) {
    const char* wb = p.ws + (MODE ? WS_PWD + (size_t)(tj >> 1) * 54 * 5120 : WS_PWO + (size_t)(tj >> 1) * 20 * 5120) + (wid - 6) * 5120 + 2048;
    const unsigned l = (unsigned)(uintptr_t)L + (wid - 6) * 5120 + 2048;
#pragma unroll 1
    for (int s = 0; s < (MODE ? EPRE : PC<MODE>::RB); ++s) gl_lin5<FUSED>(lane * 16, wb + (size_t)s * 10240, l + s * 10240);
}
template <bool FUSED, int MODE>
DI void phase_ce(const Params& p, lptr L, int lane, int wid, int ti, int tj, u64 tag) {
    typedef PC<MODE> C;
    constexpr int NST = C::NST, RA = C::RA, RB = C::RB;
    const unsigned lds0 = (unsigned)(uintptr_t)L;
    const int rh = tj & 1, ct = tj >> 1, r0 = 128 * ti + 64 * rh;
    const int wm = (wid >> 1) & 1, wk = wid & 1, r16 = lane & 15, g = lane >> 4;
    if (wid >= 6) {
        if (MODE == 0) {
            bar_vl_n<5, RB - 1>(RB - 1);
            BAR_VL(0);
#pragma unroll 1
            for (int s = 2; s < NST; ++s) BAR_L();
        } else {
            const char* wb = p.ws + WS_PWD + (size_t)ct * 54 * 5120 + (wid - 6) * 5120 + 2048 + (size_t)EPRE * 10240;
            const unsigned l0 = lds0 + (wid - 6) * 5120 + 2048; unsigned l = l0 + (EPRE % RB) * 10240;
            ring_loader_ramp2<RB, NST, 5, 2, EPRE, false>([&]() { gl_lin5<FUSED>(lane * 16, wb, l); wb += 10240; l = l == l0 + (RB - 1) * 10240 ? l0 : l + 10240; });
        }
        BAR_L(); BAR_L();
    } else if (wid >= 4) {
        const int la = wid - 4;
        const unsigned voA = lane * 16;
#pragma unroll
        for (int i = 0; i < 5; ++i) { const int pc = 5 * la + i;
            if (MODE == 0) { const int n = 64 * pc + lane; glds16<false>((unsigned)((n / 10) * D_ * 2 + (n % 10) * 16), (const char*)p.x + (size_t)r0 * D_ * 2 + ct * 160, lds0 + C::L_RES + pc * 1024); }
            else glds16<FUSED>(voA, p.ws + WS_H + (size_t)(10 * ct + pc) * 32768 + (size_t)r0 * 16, lds0 + C::L_RES + pc * 1024); }
        if (FUSED) poll_flags(p.ws + WS_FLAG + (MODE ? FL_D : FL_B) + (ti * 16 + (lane & 15)) * FS, lane < 16, tag);
        STAMPW(MODE ? 17 : 9, 4);
        const char* ab = p.ws + (MODE ? WS_HH : WS_O) + (size_t)r0 * 16 + (size_t)(4 * la) * 32768 + 2048;
        unsigned vp[4];
#pragma unroll
        for (int i = 0; i < 4; ++i) vp[i] = voA + (unsigned)i * (32768 - 1024);
        const unsigned l0 = lds0 + C::LA + la * 4096 + 2048; unsigned l = l0;
        ring_loader_ramp2<RA, NST, 4, 2, 2>([&]() { gl_str4<FUSED>(vp, ab, l); ab += 8 * 32768; l = l == l0 + (RA - 1) * 8192 ? l0 : l + 8192; });
        BAR_L(); BAR_L();
    } else {
        const unsigned aA = (unsigned)(C::LA + (4 * wk + g) * 1024 + (32 * wm + r16) * 16), aB = (unsigned)(wk * 5120 + lane * 16);
        f32x4 acc[2][5];
#pragma unroll
        for (int i = 0; i < 2; ++i)
#pragma unroll
            for (int n = 0; n < 5; ++n) acc[i][n] = (f32x4){0.f, 0.f, 0.f, 0.f};
        bf16x8 Af[3][2], Bf[3][5];
        const int kofs = wk * 256;
        int sa = 0, sb = 0;
        auto rd = [&](int b) {
            lcptr la = (lcptr)L + aA + sa * 8192; lcptr lb = (lcptr)L + aB + sb * 10240;
            Af[b][0] = ld128(la + kofs); Af[b][1] = ld128(la + (256 - kofs));
#pragma unroll
            for (int n = 0; n < 5; ++n) Bf[b][n] = ld128(lb + n * 1024);
            sa = sa == RA - 1 ? 0 : sa + 1; sb = sb == RB - 1 ? 0 : sb + 1;
        };
        auto mm = [&](int b) {
#pragma unroll
            for (int n = 0; n < 5; ++n)
#pragma unroll
                for (int i = 0; i < 2; ++i) acc[i][n] = MFMA16(Bf[b][n], Af[b][i], acc[i][n]);
        };
        auto bar = [&]() { __builtin_amdgcn_sched_barrier(0); __builtin_amdgcn_s_waitcnt(0xC77F); asm volatile("" ::: "memory"); __builtin_amdgcn_s_barrier(); asm volatile("" ::: "memory"); __builtin_amdgcn_sched_barrier(0); };
        auto il = [&]() { SGB(0x8, 1); SGB(0x100, 2); SGB(0x8, 1); SGB(0x100, 2); SGB(0x8, 1); SGB(0x100, 2); SGB(0x8, 1); SGB(0x100, 1); SGB(0x8, 6); __builtin_amdgcn_sched_barrier(0); };
        bar(); STAMP(MODE ? 18 : 10); if (MODE) STAMPC(28); rd(0);
        bar(); rd(1);
        int s = 0;
#pragma unroll 1
        for (; s + 4 < NST; s += 3) {
            bar(); rd(2); mm(0); il();
            bar(); rd(0); mm(1); il();
            bar(); rd(1); mm(2); il();
        }
        if (NST % 3 == 0) { bar(); rd(2); mm(0); il(); mm(1); mm(2); }
        else { bar(); rd(2); mm(0); il(); bar(); rd(0); mm(1); il(); mm(2); mm(0); }
        STAMP(MODE ? 19 : 11); if (MODE) STAMPC(29);
        BAR_L();
        STAMP(MODE ? 46 : 45);
#pragma unroll
        for (int n = 0; n < 5; ++n) *(LAS f32x4*)(L + C::L_X + (wid * 5 + n) * 1024 + lane * 16) = acc[1][n];
        BAR_L();
        STAMP(MODE ? 31 : 30);
        const int row = 32 * wm + 16 * wk + r16;
        f32x4 pv[5]; u32x2 rr[5];
#pragma unroll
        for (int n = 0; n < 5; ++n) { pv[n] = *(const LAS f32x4*)(L + C::L_X + ((wid ^ 1) * 5 + n) * 1024 + lane * 16);
            rr[n] = MODE == 0 ? *(const LAS u32x2*)(L + C::L_RES + row * 160 + n * 32 + g * 8) : *(const LAS u32x2*)(L + C::L_RES + (2 * n + (g >> 1)) * 1024 + row * 16 + (g & 1) * 8); }
        u32x2 o[5]; f32x2 ps2 = {0.f, 0.f}, pq2 = {0.f, 0.f};
#pragma unroll
        for (int n = 0; n < 5; ++n) { const f32x4 v = acc[0][n] + pv[n];
            const f32x2 a = (f32x2){v[0], v[1]} + (f32x2){bflo(rr[n][0]), bfhi(rr[n][0])}, b = (f32x2){v[2], v[3]} + (f32x2){bflo(rr[n][1]), bfhi(rr[n][1])};
            o[n] = (u32x2){pk2(a[0], a[1]), pk2(b[0], b[1])};
            if (MODE == 0) { ps2 += a + b; pq2 += a * a + b * b; } }
        float ps = ps2[0] + ps2[1], pq = pq2[0] + pq2[1];
#pragma unroll
        for (int n = 0; n < 5; n += 2) { const int n1 = n + 1 < 5 ? n + 1 : n;
            auto s0 = __builtin_amdgcn_permlane16_swap(o[n][0], o[n1][0], false, false); auto s1 = __builtin_amdgcn_permlane16_swap(o[n][1], o[n1][1], false, false);
            const u32x4 v = {s0[0], s1[0], s0[1], s1[1]};
            const int ch = 2 * (n + (g & 1)) + (g >> 1);
            if (n + 1 < 5 || !(g & 1)) {
                if (MODE == 0) st16<FUSED>(p.ws + WS_H + (size_t)(10 * ct + ch) * 32768 + (size_t)(r0 + row) * 16, v);
                else st16<OUT_SC1>(p.out + (size_t)(r0 + row) * D_ * 2 + ct * 160 + ch * 16, v); } }
        if (MODE == 0) { ps = sum_x32(sum_x16(ps)); pq = sum_x32(sum_x16(pq));
            if (g == 0) { char* dst = p.ws + WS_PS + ((size_t)(ti * 8 + ct) * 128 + 64 * rh + row) * 8; const u64 v = ((u64)__float_as_uint(pq) << 32) | __float_as_uint(ps);
                if (FUSED) st_dev(dst, v); else *(u64*)dst = v; } }
    }
    STAMP(MODE ? 20 : 12);
    if (MODE == 0) publish<FUSED>(p, FL_C + (ti * 16 + tj) * FS, wid, lane, tag, 43);
}

namespace pd { constexpr int RB = 3, RA = 4, SB = 28672, LA = RB * SB, NST = 10, L_STAT = LA + RA * 16384, L_CF = L_STAT + 1024  , L_STG = LA; }
template <bool FUSED>
DI void d_bissue(const char* wb, unsigned l, int lane) { gl_lin7<FUSED>(lane * 16, wb, l); gl_lin7<FUSED>(lane * 16, wb + 7168, l + 7168); }
template <bool FUSED>
DI void d_prefetch(const Params& p, lptr L, int lane, int wid, int tj) {
    const char* wb = p.ws + WS_PWG + (size_t)tj * 286720 + (wid - 6) * 14336 + 3072;
    const unsigned l = (unsigned)(uintptr_t)L + (wid - 6) * 14336 + 3072;
#pragma unroll 1
    for (int s = 0; s < pd::RB; ++s) d_bissue<FUSED>(wb + (size_t)s * pd::SB, l + s * pd::SB, lane);
}
template <bool FUSED>
DI void phase_d(const Params& p, lptr L, int lane, int wid, int ti, int tj, u64 tag) {
    using namespace pd;
    const unsigned lds0 = (unsigned)(uintptr_t)L;
    if (wid >= 6) {
        const int lb = wid - 6;
        u32x2 ps[8]; float cp[2][16];
        const int row = 64 * lb + lane;
        const char* wb = p.ws + WS_PWG + (size_t)tj * 286720 + lb * 14336 + 3072 + (size_t)RB * SB;
        const unsigned l0 = lds0 + lb * 14336 + 3072; unsigned l = l0;
        auto issue = [&]() { d_bissue<FUSED>(wb, l, lane); wb += SB; l = l == l0 + (RB - 1) * SB ? l0 : l + SB; };
        static_assert(RB == 3 && NST == 10, "the waits below");
        BAR_VL(28);
#pragma unroll
        for (int c = 0; c < 8; ++c) ld64_async(ps[c], p.ws + WS_PS + ((size_t)(ti * 8 + c) * 128 + row) * 8);
        BAR_VL(22); issue();
#pragma unroll
        for (int i = 0; i < 16; ++i) ld32_async(cp[0][i], (const float*)(p.ws + WS_CSP) + (i * 16 + tj) * 224 + row);
        BAR_VL(38); issue();
#pragma unroll
        for (int i = 0; i < 16; ++i) ld32_async(cp[1][i], (const float*)(p.ws + WS_CSP) + (i * 16 + tj) * 224 + (row + 128 < 224 ? row + 128 : 223));
        BAR_VL(46); issue();
        BAR_VL(30); issue();
        BAR_VL(14); issue();
        { float a = 0.f, q = 0.f;
#pragma unroll
          for (int c = 0; c < 8; ++c) { asm volatile("" : "+v"(ps[c])); a += __uint_as_float(ps[c][0]); q += __uint_as_float(ps[c][1]); }
          *(LAS f32x2*)(L + L_STAT + row * 8) = ln_scalars(a, q);
#pragma unroll
          for (int z = 0; z < 2; ++z) { float sm = 0.f;
#pragma unroll
              for (int i = 0; i < 16; ++i) { asm volatile("" : "+v"(cp[z][i])); sm += cp[z][i]; }
              const int c = row + 128 * z;
              if (c < 224) { const float hi = rbf(sm); *(LAS u32x4*)(L + L_CF + (c >> 5) * 1024 + (c & 31) * 16) = (u32x4){pk2(hi, sm - hi), 0u, 0u, 0u};
                  *(LAS u32x4*)(L + L_CF + (c >> 5) * 1024 + 512 + (c & 31) * 16) = (u32x4){0u, 0u, 0u, 0u}; } } }
        BAR_VL(14); issue();
        BAR_VL(14); issue();
        BAR_VL(14);
        if (FUSED) {
#pragma unroll 1
            for (int i = 0; i < 3; ++i) { int ln = (lb * 3 + i) * 64 + lane; ln = ln < 270 ? ln : 269;
                warm64(p.ws + WS_PWD + (size_t)(tj >> 1) * 276480 + (size_t)(2 * (ti & 3) + (tj & 1)) * 34560, (unsigned)ln * 128u, lds0); }
            BAR_VL(3);
        } else BAR_VL(0);
        BAR_L();
    } else if (wid >= 4) {
        const int la = wid - 4;
        if (FUSED) poll_flags(p.ws + WS_FLAG + FL_C + (ti * 16 + (lane & 15)) * FS, lane < 16, tag);
        STAMPW(13, 4);
        const char* ab = p.ws + WS_H + (size_t)(128 * ti + 64 * la) * 16 + 4096;
        unsigned vp[4];
#pragma unroll
        for (int i = 0; i < 4; ++i) vp[i] = lane * 16 + (unsigned)i * (32768 - 2048);
        const unsigned l0 = lds0 + LA + la * 1024 + 4096; unsigned l = l0;
        ring_loader_ramp2<RA, NST, 8, 1, 2>([&]() { gl_str4w<FUSED>(vp, ab, l); gl_str4w<FUSED>(vp, ab + 4 * 32768, l + 8192); ab += 8 * 32768; l = l == l0 + (RA - 1) * 16384 ? l0 : l + 16384; });
        BAR_L();
    } else {
        const int r32 = lane & 31, h = lane >> 5;
        const unsigned aA = (unsigned)(LA + h * 2048 + (32 * wid + r32) * 16), aB = (unsigned)(lane * 16);
        f32x16 acc[7];
#pragma unroll
        for (int t = 0; t < 7; ++t)
#pragma unroll
            for (int i = 0; i < 16; ++i) acc[t][i] = 0.f;
        bf16x8 A0, B0[7], A1, B1[7];
        auto rd = [&](bf16x8& Af, bf16x8* Bf, int sa, int sb, int u) {
            lcptr lb = (lcptr)L + aB + sb * SB + u * 7168;
            Af = ld128((lcptr)L + aA + sa * 16384 + u * 4096);
#pragma unroll
            for (int t = 0; t < 7; ++t) Bf[t] = ld128(lb + t * 1024);
        };
        auto mm = [&](const bf16x8& Af, const bf16x8* Bf) {
#pragma unroll
            for (int t = 0; t < 7; ++t) acc[t] = MFMA32(Bf[t], Af, acc[t]);
        };
        auto il = [&](const bf16x8& Af, const bf16x8* Bf) { SGB(0x8, 1); SGB(0x100, 2); SGB(0x8, 1); SGB(0x100, 2); SGB(0x8, 1); SGB(0x100, 2); SGB(0x8, 1); SGB(0x100, 2); SGB(0x8, 3); __builtin_amdgcn_sched_barrier(0);
            asm volatile("" :: "v"(Af), "v"(Bf[0]), "v"(Bf[1]), "v"(Bf[2]), "v"(Bf[3]), "v"(Bf[4]), "v"(Bf[5]), "v"(Bf[6])); __builtin_amdgcn_sched_barrier(0); };
        CBAR();
        STAMP(14); STAMPC(24);
        rd(A0, B0, 0, 0, 0);
        int sa = 0, sb = 0;
#pragma unroll 1
        for (int s = 0; s < NST - 1; ++s) {
            rd(A1, B1, sa, sb, 1); mm(A0, B0); il(A0, B0);
            rd(A0, B0, sa, sb, 2); mm(A1, B1); il(A1, B1);
            rd(A1, B1, sa, sb, 3); mm(A0, B0); il(A0, B0);
            sa = sa == RA - 1 ? 0 : sa + 1; sb = sb == RB - 1 ? 0 : sb + 1;
            CBAR();
            rd(A0, B0, sa, sb, 0); mm(A1, B1); il(A1, B1);
        }
        rd(A1, B1, sa, sb, 1); mm(A0, B0); il(A0, B0);
        rd(A0, B0, sa, sb, 2); mm(A1, B1); il(A1, B1);
        rd(A1, B1, sa, sb, 3); mm(A0, B0); il(A0, B0);
        mm(A1, B1);
        STAMP(15); STAMPC(25);
        BAR_L();
        STAMP(40);
        const int tok = 32 * wid + r32;
        const f32x2 st = *(const LAS f32x2*)(L + L_STAT + tok * 8);
        const float r2 = st.y * st.y;
        {
            bf16x8 cf[7];
#pragma unroll
            for (int t = 0; t < 7; ++t) cf[t] = ld128((lcptr)L + L_CF + t * 1024 + lane * 16);
            const bf16x8 mf = __builtin_bit_cast(bf16x8, ((u32x4){h ? 0u : pk2(-st.x, -st.x), 0u, 0u, 0u}));
#pragma unroll
            for (int t = 0; t < 7; ++t) acc[t] = MFMA32(cf[t], mf, acc[t]);
        }
        const f32x2 r22 = {r2, r2};
        auto glu4 = [&](float g0, float g1, float g2, float g3, float u0, float u1, float u2, float u3) {
            const f32x2 ya = (f32x2){u0, u1} * r22, yb = (f32x2){u2, u3} * r22;
            const f32x2 oa = (f32x2){__builtin_fmaxf(g0, 0.f), __builtin_fmaxf(g1, 0.f)} * ya, ob = (f32x2){__builtin_fmaxf(g2, 0.f), __builtin_fmaxf(g3, 0.f)} * yb;
            return (u32x2){pk2(oa[0], oa[1]), pk2(ob[0], ob[1])}; };
        char* dst = p.ws + WS_HH + (size_t)(14 * tj + h) * 32768 + (size_t)(128 * ti + tok) * 16;
        const int cmax = 216 - 14 * tj - h;
        auto put = [&](int c, u32x2 x, u32x2 y) {
            auto s0 = __builtin_amdgcn_permlane32_swap(x[0], y[0], false, false); auto s1 = __builtin_amdgcn_permlane32_swap(x[1], y[1], false, false);
            if (c < cmax) st16<FUSED>(dst + (size_t)c * 32768, (u32x4){s0[0], s1[0], s0[1], s1[1]}); };
#pragma unroll
        for (int a = 0; a < 3; ++a)
#pragma unroll
            for (int u = 0; u < 4; u += 2)
                put(4 * a + u, glu4(acc[2 * a][4 * u], acc[2 * a][4 * u + 1], acc[2 * a][4 * u + 2], acc[2 * a][4 * u + 3], acc[2 * a + 1][4 * u], acc[2 * a + 1][4 * u + 1], acc[2 * a + 1][4 * u + 2], acc[2 * a + 1][4 * u + 3]),
                    glu4(acc[2 * a][4 * u + 4], acc[2 * a][4 * u + 5], acc[2 * a][4 * u + 6], acc[2 * a][4 * u + 7], acc[2 * a + 1][4 * u + 4], acc[2 * a + 1][4 * u + 5], acc[2 * a + 1][4 * u + 6], acc[2 * a + 1][4 * u + 7]));
        put(12, glu4(acc[6][0], acc[6][1], acc[6][2], acc[6][3], acc[6][8], acc[6][9], acc[6][10], acc[6][11]), glu4(acc[6][4], acc[6][5], acc[6][6], acc[6][7], acc[6][12], acc[6][13], acc[6][14], acc[6][15]));
        STAMP(41);
    }
    STAMP(16);
    publish<FUSED>(p, FL_D + (ti * 16 + tj) * FS, wid, lane, tag, 44);
}

#define ROTA 2
#define ROTC 0
#define ROTD 0
#define ROTE 1
#define PROLOG() \
    extern __shared__ __attribute__((aligned(16))) char smem[]; \
    lptr L = (lptr)smem; \
    const int tid = threadIdx.x, lane = tid & 63, wid = __builtin_amdgcn_readfirstlane(tid >> 6); \
    const int xcd = blockIdx.x & 7, idx = blockIdx.x >> 3; \
    const int ti = 4 * (xcd & 3) + (idx & 3), tj = 8 * (xcd >> 2) + (idx >> 2); \
    const int rot10 = ((idx >> 2) + 3 * (idx & 3)) % 10, rot27 = ((idx >> 2) * 3 + (idx & 3) * 7) % 27; \
    const int rotC = ROTC ? rot10 : 0, rotD = ROTD ? rot10 : 0, rotE = ROTE ? rot27 : 0, rotA = ROTA == 1 ? rot10 : ROTA == 2 ? (((idx >> 2) + (idx & 3)) & 3) : ROTA == 3 ? ((idx >> 2) & 1) * 5 : 0; \
    (void)rot10; (void)rot27; (void)tid; (void)rotA; (void)rotC; (void)rotD; (void)rotE

__global__ __launch_bounds__(512, 2) void k_fused(const Params p) {
    PROLOG();
    STAMP(0);
    u64 tag = 0; u32x2 ev;
    ld64_async(ev, p.ws + WS_FLAG + FL_E);
    if (tj >= 15) phase_a_stats<true>(p, L, lane, wid, ti, ev, tag); else phase_a<true>(p, L, lane, wid, ti, tj, rotA, ev, tag);
    if (tid == 0) st_dev(p.ws + WS_FLAG + FL_START + blockIdx.x * 8, tag);
    phase_b<true>(p, L, lane, wid, ti, tj, tag);
    if (wid >= 6) {
        poll_flags(p.ws + WS_FLAG + FL_P + ((lane & 15) * 16 + (tj & ~1) + ((lane >> 4) & 1)) * FS, lane < 32, tag);
        ce_prefetch<true, 0>(p, L, lane, wid, tj);
    }
    STAMP(8);
    phase_ce<true, 0>(p, L, lane, wid, ti, tj, tag);
    if (wid >= 6) {
        poll_flags(p.ws + WS_FLAG + FL_B + ((lane & 15) * 16 + (lane < 16 ? tj : (tj & ~1) + ((lane >> 4) & 1))) * FS, lane < 48, tag);
        d_prefetch<true>(p, L, lane, wid, tj);
    }
    if (blockIdx.x == 0 && wid == 5) {
        for (unsigned spins = 0; spins < (1u << 17); ++spins) {
            bool ok = true;
#pragma unroll
            for (int i = 0; i < 4; ++i) ok &= ld_dev(p.ws + WS_FLAG + FL_START + (lane * 4 + i) * 8) == tag;
            if (__all(ok)) break;
            __builtin_amdgcn_s_sleep(1);
        }
        if (lane == 0) st_dev(p.ws + WS_FLAG + FL_E, tag);
    }
    phase_d<true>(p, L, lane, wid, ti, tj, tag);
    if (wid >= 6) ce_prefetch<true, 1>(p, L, lane, wid, tj);
    phase_ce<true, 1>(p, L, lane, wid, ti, tj, tag);
    STAMP(21);
}

extern "C" void kernel_launch(void* const* d_in, const int* in_sizes, int n_in, void* d_out, int out_size, void* d_ws, size_t ws_size, hipStream_t stream) {
    Params p;
    p.x = (const u16*)d_in[0]; p.wqkv = (const u16*)d_in[1]; p.wo = (const u16*)d_in[2];
    p.wg = (const u16*)d_in[4]; p.wu = (const u16*)d_in[5]; p.wd = (const u16*)d_in[6];
    p.out = (char*)d_out; p.ws = (char*)d_ws;
    (void)hipFuncSetAttribute((const void*)k_fused, hipFuncAttributeMaxDynamicSharedMemorySize, LDS_BYTES);
    k_fused<<<256, 512, LDS_BYTES, stream>>>(p);
}
```

```cpp
#include <hip/hip_runtime.h>
#include <hip/hip_bf16.h>
#include <stdint.h>

#define LAS __attribute__((address_space(3)))
#define DI __device__ __forceinline__
typedef unsigned short u16;
typedef unsigned long long u64;
typedef short bf16x8 __attribute__((ext_vector_type(8)));
typedef short s16x4 __attribute__((ext_vector_type(4)));
typedef float f32x2 __attribute__((ext_vector_type(2)));
typedef float f32x4 __attribute__((ext_vector_type(4)));
typedef float f32x16 __attribute__((ext_vector_type(16)));
typedef unsigned u32x2 __attribute__((ext_vector_type(2)));
typedef unsigned u32x4 __attribute__((ext_vector_type(4)));
typedef __bf16 bf16x2_t __attribute__((ext_vector_type(2)));
typedef LAS char* lptr;
typedef LAS const char* lcptr;

static constexpr int S_ = 2048, D_ = 640, H_ = 32, DH_ = 20, F_ = 1728;
static constexpr size_t WS_FLAG = 0;
static constexpr size_t FS = 128;
static constexpr size_t FL_E = 0, FL_START = 1024, FL_A = 4096, FL_B = FL_A + 256 * FS, FL_C = FL_B + 256 * FS, FL_D = FL_C + 256 * FS, FL_P = FL_D + 256 * FS, FL_END = FL_P + 256 * FS;
static constexpr size_t WS_QKV = 262144;
static constexpr size_t WS_O = WS_QKV + (size_t)480 * 16384;
static constexpr size_t WS_H = WS_O + (size_t)80 * 32768;
static constexpr size_t WS_HH = WS_H + (size_t)80 * 32768;
static constexpr size_t WS_CSP = WS_HH + (size_t)216 * 32768;
static constexpr size_t WS_PS = WS_CSP + (size_t)16 * 16 * 224 * 4;
static constexpr size_t WS_S1 = WS_PS + (size_t)16 * 8 * 128 * 8;
static constexpr size_t WS_PWO = WS_S1 + (size_t)2048 * 8;
static constexpr size_t WS_PWD = WS_PWO + (size_t)8 * 20 * 5 * 1024;
static constexpr size_t WS_PWG = WS_PWD + (size_t)8 * 54 * 5 * 1024;
static constexpr size_t WS_END = WS_PWG + (size_t)16 * 20 * 14 * 1024;
static constexpr int LDS_BYTES = 163840;
static constexpr float C2_ = 0.22360679774997896f * 1.4426950408889634f;

struct Params { const u16* x; const u16* wqkv; const u16* wo; const u16* wg; const u16* wu; const u16* wd; char* out; char* ws; };

DI unsigned pk2(float a, float b) { f32x2 v = {a, b}; return __builtin_bit_cast(unsigned, __builtin_convertvector(v, bf16x2_t)); }
DI float bflo(unsigned u) { return __uint_as_float(u << 16); }
DI float bfhi(unsigned u) { return __uint_as_float(u & 0xffff0000u); }
DI float rbf(float a) { return bflo(pk2(a, 0.f) & 0xffffu); }
template <bool SC1> DI void glds16(unsigned voff, const void* sbase, unsigned ldsaddr) {
    unsigned keep;
    if (SC1) asm volatile("s_mov_b32 %0, m0\n\ts_mov_b32 m0, %3\n\ts_nop 0\n\tglobal_load_lds_dwordx4 %1, %2 sc1\n\ts_mov_b32 m0, %0" : "=&s"(keep) : "v"(voff), "s"(sbase), "s"(ldsaddr) : "memory");
    else asm volatile("s_mov_b32 %0, m0\n\ts_mov_b32 m0, %3\n\ts_nop 0\n\tglobal_load_lds_dwordx4 %1, %2\n\ts_mov_b32 m0, %0" : "=&s"(keep) : "v"(voff), "s"(sbase), "s"(ldsaddr) : "memory");
}
#define GL_(v, off, sc) "global_load_lds_dwordx4 %" #v ", %[g] offset:" #off sc "\n\t"
#define GLHEAD "s_mov_b32 m0, %[l]\n\ts_nop 0\n\t"
template <bool SC1> DI void gl_lin5(unsigned v, const char* g, unsigned l) {
    if (SC1) asm volatile(GLHEAD GL_(0, -2048, " sc1") GL_(0, -1024, " sc1") GL_(0, 0, " sc1") GL_(0, 1024, " sc1") GL_(0, 2048, " sc1") :: "v"(v), [g] "s"(g), [l] "s"(l) : "memory");
    else asm volatile(GLHEAD GL_(0, -2048, "") GL_(0, -1024, "") GL_(0, 0, "") GL_(0, 1024, "") GL_(0, 2048, "") :: "v"(v), [g] "s"(g), [l] "s"(l) : "memory");
}
template <bool SC1> DI void gl_lin7(unsigned v, const char* g, unsigned l) {
    if (SC1) asm volatile(GLHEAD GL_(0, -3072, " sc1") GL_(0, -2048, " sc1") GL_(0, -1024, " sc1") GL_(0, 0, " sc1") GL_(0, 1024, " sc1") GL_(0, 2048, " sc1") GL_(0, 3072, " sc1") :: "v"(v), [g] "s"(g), [l] "s"(l) : "memory");
    else asm volatile(GLHEAD GL_(0, -3072, "") GL_(0, -2048, "") GL_(0, -1024, "") GL_(0, 0, "") GL_(0, 1024, "") GL_(0, 2048, "") GL_(0, 3072, "") :: "v"(v), [g] "s"(g), [l] "s"(l) : "memory");
}
template <bool SC1> DI void gl_str4(const unsigned* v, const char* g, unsigned l) {
    if (SC1) asm volatile(GLHEAD GL_(0, -2048, " sc1") GL_(1, -1024, " sc1") GL_(2, 0, " sc1") GL_(3, 1024, " sc1") :: "v"(v[0]), "v"(v[1]), "v"(v[2]), "v"(v[3]), [g] "s"(g), [l] "s"(l) : "memory");
    else asm volatile(GLHEAD GL_(0, -2048, "") GL_(1, -1024, "") GL_(2, 0, "") GL_(3, 1024, "") :: "v"(v[0]), "v"(v[1]), "v"(v[2]), "v"(v[3]), [g] "s"(g), [l] "s"(l) : "memory");
}
template <bool SC1> DI void gl_str4w(const unsigned* v, const char* g, unsigned l) {
    if (SC1) asm volatile(GLHEAD GL_(0, -4096, " sc1") GL_(1, -2048, " sc1") GL_(2, 0, " sc1") GL_(3, 2048, " sc1") :: "v"(v[0]), "v"(v[1]), "v"(v[2]), "v"(v[3]), [g] "s"(g), [l] "s"(l) : "memory");
    else asm volatile(GLHEAD GL_(0, -4096, "") GL_(1, -2048, "") GL_(2, 0, "") GL_(3, 2048, "") :: "v"(v[0]), "v"(v[1]), "v"(v[2]), "v"(v[3]), [g] "s"(g), [l] "s"(l) : "memory");
}
DI void gl_str8(const unsigned* v, const char* g, unsigned l) {
    asm volatile(GLHEAD GL_(0, -4096, "") GL_(1, -3072, "") GL_(2, -2048, "") GL_(3, -1024, "") GL_(4, 0, "") GL_(5, 1024, "") GL_(6, 2048, "") GL_(7, 3072, "")
                 :: "v"(v[0]), "v"(v[1]), "v"(v[2]), "v"(v[3]), "v"(v[4]), "v"(v[5]), "v"(v[6]), "v"(v[7]), [g] "s"(g), [l] "s"(l) : "memory");
}
template <bool SC1> DI void st16(void* ptr, u32x4 v) {
    if (SC1) asm volatile("global_store_dwordx4 %0, %1, off sc1\n\ts_nop 1" :: "v"(ptr), "v"(v) : "memory");
    else *(u32x4*)ptr = v;
}
DI u64 ld_dev(const void* p_) { return __hip_atomic_load((const u64*)p_, __ATOMIC_RELAXED, __HIP_MEMORY_SCOPE_AGENT); }
DI void st_dev(void* p_, u64 v) { __hip_atomic_store((u64*)p_, v, __ATOMIC_RELAXED, __HIP_MEMORY_SCOPE_AGENT); }
DI void ld64_async(u32x2& v, const void* ptr) { asm volatile("global_load_dwordx2 %0, %1, off sc1" : "=v"(v) : "v"(ptr) : "memory"); }
template <int OFF> DI void ld64_async_o(u32x2& v, const void* ptr) { asm volatile("global_load_dwordx2 %0, %1, off offset:%2 sc1" : "=v"(v) : "v"(ptr), "i"(OFF) : "memory"); }
template <int OFF> DI void ld32_async_o(float& v, const void* ptr) { asm volatile("global_load_dword %0, %1, off offset:%2 sc1" : "=v"(v) : "v"(ptr), "i"(OFF) : "memory"); }
#define VMWAIT(N) asm volatile("s_waitcnt vmcnt(%0)" :: "i"(N) : "memory")
DI void poll_flags(const void* flag, bool need, u64 tag) {
    for (unsigned spins = 0; spins < (1u << 18); ++spins) {
        const u64 v = need ? ld_dev(flag) : tag;
        if (__all(v == tag)) break;
    }
    asm volatile("" ::: "memory");
}
DI void warm64(const void* base, unsigned voff, unsigned lds0) {
    unsigned keep;
    asm volatile("s_mov_b32 %0, m0\n\ts_mov_b32 m0, %3\n\ts_nop 0\n\tglobal_load_lds_dword %1, %2 sc1\n\ts_mov_b32 m0, %0" : "=&s"(keep) : "v"(voff), "s"(base), "s"(lds0 + (unsigned)(LDS_BYTES - 256)) : "memory");
}
#define BAR_L() asm volatile("s_waitcnt lgkmcnt(0)\n\ts_barrier" ::: "memory")
#define BAR_VL(N) asm volatile("s_waitcnt vmcnt(%0) lgkmcnt(0)\n\ts_barrier" :: "i"(N) : "memory")
#define WAIT_LGKM0() do { __builtin_amdgcn_sched_barrier(0); __builtin_amdgcn_s_waitcnt(0xC07F); __builtin_amdgcn_sched_barrier(0); } while (0)
#define CBAR() do { WAIT_LGKM0(); asm volatile("" ::: "memory"); __builtin_amdgcn_s_barrier(); asm volatile("" ::: "memory"); __builtin_amdgcn_sched_barrier(0); } while (0)
#define SGB(mask, n) __builtin_amdgcn_sched_group_barrier(mask, n, 0)
template <int STEP, int MAXN> DI void bar_vl_n(int n) {
    if (n >= MAXN) { BAR_VL((MAXN * STEP > 63 ? 63 : MAXN * STEP)); }
    else if constexpr (MAXN > 0) bar_vl_n<STEP, MAXN - 1>(n);
}
template <int N> struct IC { static constexpr int v = N; };
template <int K, int END, class F> DI void static_for(F&& f) { if constexpr (K < END) { f(IC<K>{}); static_for<K + 1, END>(f); } }
template <int R, int NST, int LAG, int P0> constexpr int ramp_issued(int k) { const int r = P0 + 2 * k, lim = k <= LAG ? R : R + k - LAG, v = r < lim ? r : lim; return v < NST ? v : NST; }
template <int R, int NST, int LAG, int P0> constexpr int ramp_len() { int k = LAG + 1; while (k < NST && P0 + 2 * k < R + k - LAG) ++k; return k < NST ? k : NST - 1; }
template <int R, int NST, int NP, int LAG, int P0, bool PROLOGUE = true, class FI>
DI void ring_loader(FI&& issue) {
    if (PROLOGUE) static_for<0, P0>([&](auto) { issue(); });
    constexpr int KR = ramp_len<R, NST, LAG, P0>();
    static_for<0, KR>([&](auto kc) { constexpr int k = decltype(kc)::v; constexpr int a = ramp_issued<R, NST, LAG, P0>(k), b = ramp_issued<R, NST, LAG, P0>(k + 1);
        BAR_VL((a - 1 - k) * NP); static_for<a, b>([&](auto) { issue(); }); });
    constexpr int AK = ramp_issued<R, NST, LAG, P0>(KR) - 1 - KR;
    constexpr int KS = NST - 1 - AK;
    if constexpr (KS > KR) {
#pragma unroll 1
        for (int k = KR; k < KS; ++k) { BAR_VL(AK * NP); issue(); }
    }
    static_for<(KS > KR ? KS : KR), NST>([&](auto kc) { constexpr int k = decltype(kc)::v; BAR_VL((NST - 1 - k) * NP); });
}
DI bf16x8 ld128(lcptr p) { return *(const LAS bf16x8*)p; }
DI s16x4 ld64(lcptr p) { return *(const LAS s16x4*)p; }
DI s16x4 ld64v(lcptr p) { return *(const volatile LAS s16x4*)p; }
DI s16x4 ldtr(lcptr p) { typedef short v4 __attribute__((ext_vector_type(4))); return __builtin_bit_cast(s16x4, __builtin_amdgcn_ds_read_tr16_b64_v4i16((LAS v4*)p)); }
DI bf16x8 cat8(s16x4 a, s16x4 b) { return __builtin_shufflevector(a, b, 0, 1, 2, 3, 4, 5, 6, 7); }
#define MFMA16(a, b, c) __builtin_amdgcn_mfma_f32_16x16x32_bf16(a, b, c, 0, 0, 0)
#define MFMA32(a, b, c) __builtin_amdgcn_mfma_f32_32x32x16_bf16(a, b, c, 0, 0, 0)
#define MFMA16K16(a, b, c) __builtin_amdgcn_mfma_f32_16x16x16bf16_1k(a, b, c, 0, 0, 0)
#define MFMA32K8(a, b, c) __builtin_amdgcn_mfma_f32_32x32x8bf16_1k(a, b, c, 0, 0, 0)
template <int CTRL> DI float dpp_add(float v) { return v + __int_as_float(__builtin_amdgcn_update_dpp(0, __float_as_int(v), CTRL, 0xf, 0xf, true)); }
DI float relu(float x) { const int i = __float_as_int(x); return __int_as_float(i > 0 ? i : 0); }
DI float sum8(float v) { v = dpp_add<0xB1>(v); v = dpp_add<0x4E>(v); return dpp_add<0x141>(v); }
DI float sum_x16(float v) { auto r = __builtin_amdgcn_permlane16_swap(__float_as_uint(v), __float_as_uint(v), false, false); return __uint_as_float(r[0]) + __uint_as_float(r[1]); }
DI float sum_x32(float v) { auto r = __builtin_amdgcn_permlane32_swap(__float_as_uint(v), __float_as_uint(v), false, false); return __uint_as_float(r[0]) + __uint_as_float(r[1]); }
DI bf16x8 ones8() { bf16x8 o = {0x3f80, 0x3f80, 0x3f80, 0x3f80, 0x3f80, 0x3f80, 0x3f80, 0x3f80}; asm volatile("" : "+v"(o)); return o; }

DI f32x2 ln_scalars(float sum, float sq) {
    const float mean = sum * (1.0f / D_);
    float var = sq * (1.0f / D_) - mean * mean; var = var > 0.f ? var : 0.f;
    const float sd = rbf(__builtin_sqrtf(rbf(rbf(var) + 1e-5f)));
    return (f32x2){rbf(mean), 1.0f / sd};
}
template <bool FUSED> DI void publish(const Params& p, size_t flag_off, int wid, int lane, u64 tag) {
    if (FUSED) { BAR_VL(0); if (wid == 0 && lane == 0) st_dev(p.ws + WS_FLAG + flag_off, tag); }
}

namespace rp { constexpr int L_G = 132608, L_O = L_G + 14336, L_D = 102400; }
DI int rp_ns(int ti) { return ti < 4 ? 2 : 1; }
DI int rp_s0(int ti) { return ti < 4 ? 2 * ti : ti + 4; }
DI int rp_zo(int ti, int tj) { return ti >= 6 ? 2 * (ti - 6) + (tj & 1) : -1; }
DI const char* rp1_addr(const Params& p, int lane, int n, int ti, int tj) {
    const int ns = rp_ns(ti), s0 = rp_s0(ti), zo = rp_zo(ti, tj);
    if (n < 14 * ns) { const int mat = n >= 7 * ns, u = 64 * (n - 7 * ns * mat) + lane, row = u / 14; int col = 112 * tj + 8 * (u - 14 * row); col = col > F_ - 8 ? F_ - 8 : col;
        return (const char*)((mat ? p.wu : p.wg) + (size_t)(32 * s0 + row) * F_ + col); }
    const int u = 64 * (n - 14 * ns) + lane, row = u / 10, c = u - 10 * row;
    return (const char*)(p.wo + (size_t)(32 * (zo < 0 ? 0 : zo) + row) * D_ + 80 * (tj >> 1) + 8 * c);
}
DI void glds16v(const void* vaddr, unsigned ldsaddr) {
    asm volatile("s_nop 0\n\ts_mov_b32 m0, %1\n\ts_nop 0\n\tglobal_load_lds_dwordx4 %0, off" :: "v"(vaddr), "s"(ldsaddr) : "memory");
}
template <int I0 = 0, int I1 = 7>
DI void rp1_issue(const Params& p, unsigned lds0, int lane, int lw, int ti, int tj) {
    const int last = 14 * rp_ns(ti) + (rp_zo(ti, tj) >= 0 ? 5 : 0) - 1;
#pragma unroll
    for (int i = I0; i < I1; ++i) { int n = lw + 4 * i; n = n < last ? n : last; glds16v(rp1_addr(p, lane, n, ti, tj), (unsigned)__builtin_amdgcn_readfirstlane(lds0 + rp::L_G + n * 1024)); }
}
template <bool FUSED>
DI void rp1_store(const Params& p, lptr L, int lane, int lw, int ti, int tj, int part = 3) {
    const int ns = rp_ns(ti), s0 = rp_s0(ti), zo = rp_zo(ti, tj);
    const int g = lane >> 4, q = (lane & 15) >> 2, pp = lane & 3;
    const bf16x8 ones = ones8();
    if (part != 2) {
#pragma unroll 1
        for (int f = lw; f < 14; f += 4) { const int wn = f >= 7, t = f - 7 * wn, uo = 7168 * ns;
            lcptr src = (lcptr)L + rp::L_G + (t < 6 ? (t & 1) * uo + (64 * wn + 16 * (t >> 1) + 4 * pp) * 2 : (pp >> 1) * uo + (48 + 8 * wn + 4 * (pp & 1)) * 2) + (8 * g + q) * 224;
            s16x4 fr[2][2];
#pragma unroll
            for (int s = 0; s < 2; ++s) { fr[s][0] = ldtr(src + s * 32 * 224); fr[s][1] = ldtr(src + s * 32 * 224 + 4 * 224); }
            f32x4 cs = {0.f, 0.f, 0.f, 0.f};
            char* dst = p.ws + WS_PWG + (size_t)tj * 286720 + (size_t)(s0 * 14 + f) * 1024 + lane * 16;
#pragma unroll
            for (int s = 0; s < 2; ++s) if (s < ns) { const bf16x8 x = cat8(fr[s][0], fr[s][1]); st16<FUSED>(dst + s * 14336, __builtin_bit_cast(u32x4, x)); cs = MFMA16(x, ones, cs); }
            if ((lane & 15) == 0) st16<FUSED>((float*)(p.ws + WS_CSP) + (tj * 16 + ti) * 224 + 16 * f + 4 * g, __builtin_bit_cast(u32x4, cs));
        }
    }
    if (zo >= 0 && (part == 2 || part == 3)) {
#pragma unroll 1
        for (int nt = 3 - lw; nt < 5; nt += 4) {
            lcptr src = (lcptr)L + rp::L_O + (8 * g + q) * 160 + (16 * nt + 4 * pp) * 2;
            st16<FUSED>(p.ws + WS_PWO + ((size_t)((tj >> 1) * 20 + zo) * 5 + nt) * 1024 + lane * 16, __builtin_bit_cast(u32x4, cat8(ldtr(src), ldtr(src + 4 * 160)))); }
    }
}
DI void rp2_issue(const Params& p, unsigned lds0, int lane, int wid, int ti, int tj) {
    const int ct = tj >> 1, z = 2 * ti + (tj & 1), np = z + 32 < 54 ? 10 : 5;
#pragma unroll 1
    for (int n = wid; n < np; n += 4) { const int im = n >= 5, u = 64 * (n - 5 * im) + lane, row = u / 10, c = u - 10 * row;
        glds16<false>((unsigned)((32 * (z + 32 * im) + row) * D_ + 80 * ct + 8 * c) * 2u, p.wd, lds0 + rp::L_D + n * 1024); }
}
template <bool FUSED>
DI int rp2_store(const Params& p, lptr L, int lane, int wid, int ti, int tj) {
    const int ct = tj >> 1, z = 2 * ti + (tj & 1), np = z + 32 < 54 ? 10 : 5;
    const int g = lane >> 4, q = (lane & 15) >> 2, pp = lane & 3;
    int cnt = 0;
#pragma unroll 1
    for (int n = wid; n < np; n += 4, ++cnt) { const int im = n >= 5, nt = n - 5 * im;
        lcptr src = (lcptr)L + rp::L_D + im * 5120 + (8 * g + q) * 160 + (16 * nt + 4 * pp) * 2;
        st16<FUSED>(p.ws + WS_PWD + ((size_t)(ct * 54 + z + 32 * im) * 5 + nt) * 1024 + lane * 16, __builtin_bit_cast(u32x4, cat8(ldtr(src), ldtr(src + 4 * 160)))); }
    return cnt;
}
DI u64 make_tag(u32x2& ev) {
    asm volatile("" : "+v"(ev));
    const u64 e = (((u64)(unsigned)__builtin_amdgcn_readfirstlane(ev[1]) << 32) | (unsigned)__builtin_amdgcn_readfirstlane(ev[0])) + 1;
    return e;
}

namespace pa { constexpr int R = 4, NST = 10, LA = 0, LB = R * 16384, L_STAT = 2 * R * 16384, L_CS = L_STAT + 1024; }
template <bool FUSED>
DI void phase_a_stats(const Params& p, lptr L, int lane, int wid, int ti, u32x2& ev, u64& tag) {
    const int r = lane >> 3, c = lane & 7;
    if (wid >= 4) rp1_issue(p, (unsigned)(uintptr_t)L, lane, wid - 4, ti, 15);
#pragma unroll
    for (int hlf = 0; hlf < 2; ++hlf) {
        const int row = 128 * ti + 16 * wid + 8 * hlf + r;
        const u32x4* src = (const u32x4*)(p.x + (size_t)row * D_) + c;
        u32x4 v[10];
#pragma unroll
        for (int i = 0; i < 10; ++i) v[i] = src[8 * i];
        float a = 0.f, q = 0.f;
#pragma unroll
        for (int i = 0; i < 10; ++i)
#pragma unroll
            for (int e = 0; e < 4; ++e) { const float lo = bflo(v[i][e]), hi = bfhi(v[i][e]); a += lo + hi; q += lo * lo + hi * hi; }
        const f32x2 st = ln_scalars(sum8(a), sum8(q));
        if (c == 0) { const u64 w = ((u64)__float_as_uint(st.y) << 32) | __float_as_uint(st.x); char* dst = p.ws + WS_S1 + (size_t)row * 8; if (FUSED) st_dev(dst, w); else *(u64*)dst = w; }
    }
    BAR_VL(0);
    if (FUSED) { tag = make_tag(ev); if (wid == 0 && lane == 0) st_dev(p.ws + WS_FLAG + FL_A + (ti * 16 + 15) * FS, tag); }
    if (wid >= 4) rp1_store<FUSED>(p, L, lane, wid - 4, ti, 15);
    if (FUSED) { BAR_VL(0); if (wid == 0 && lane == 0) st_dev(p.ws + WS_FLAG + FL_P + (ti * 16 + 15) * FS, tag); }
}
template <bool FUSED>
DI void phase_a(const Params& p, lptr L, int lane, int wid, int ti, int tj, int rot, u32x2& ev, u64& tag) {
    using namespace pa;
    const unsigned lds0 = (unsigned)(uintptr_t)L;
    if (__builtin_expect(wid >= 4, 1)) {
        const int lw = wid - 4, r = lane >> 3, c = lane & 7;
        const bool isA = lw < 2;
        unsigned vo[2];
#pragma unroll
        for (int e = 0; e < 2; ++e) vo[e] = isA ? (unsigned)(r * D_ + ((c ^ ((4 * e + (r >> 1)) & 7)) * 8)) * 2u : (unsigned)(r * 3 * D_ + ((c ^ (4 * ((r >> 1) & 1) + 2 * e)) * 8)) * 2u;
        unsigned vp[8];
#pragma unroll
        for (int i = 0; i < 8; ++i) vp[i] = vo[i & 1] + (isA ? (unsigned)i * (8 * D_ * 2 - 1024) : (unsigned)(i & 3) * (8 * 3 * D_ * 2 - 1024));
        const char* gb = isA ? (const char*)p.x + (size_t)(128 * ti + 64 * lw) * D_ * 2 + 4096 + rot * 128 : (const char*)p.wqkv + ((size_t)(32 * (lw - 2) + 64 * rot) * 3 * D_ + 128 * tj) * 2 + 2048;
        const int gstep = isA ? 128 : 64 * 3 * D_ * 2;
        int ks = rot; unsigned sl = lds0 + (isA ? LA + lw * 8192 + 4096 : LB + (lw - 2) * 4096 + 2048), sslot = 0;
        auto issue = [&]() {
            if (isA) gl_str8(vp, gb, sl + sslot); else { gl_str4<false>(vp, gb, sl + sslot); gl_str4<false>(vp, gb + 128, sl + sslot + 8192); }
            gb += gstep; if (++ks == NST) { ks = 0; gb -= (size_t)NST * gstep; }
            sslot = (sslot + 16384) & (R * 16384 - 1);
        };
        const int g = lane >> 4, q = (lane & 15) >> 2, pp = lane & 3;
        unsigned aC[2];
#pragma unroll
        for (int t = 0; t < 2; ++t) aC[t] = (unsigned)(LB + ((lw >> 1) * 8 + g) * 1024 + q * 128 + (((2 * (2 * (lw & 1) + t) + (pp >> 1)) ^ (4 * ((q >> 1) & 1) + 2 * (g & 1))) * 16) + (pp & 1) * 8);
        f32x4 cs[2] = {{0.f, 0.f, 0.f, 0.f}, {0.f, 0.f, 0.f, 0.f}};
        const bf16x8 ones = ones8();
        s16x4 cf[2][2][2];
        __builtin_amdgcn_s_setprio(3);
        auto pre = [&](int s) { lcptr q0 = (lcptr)L + (s & 3) * 16384;
#pragma unroll
            for (int ks2 = 0; ks2 < 2; ++ks2)
#pragma unroll
                for (int t = 0; t < 2; ++t) { cf[ks2][t][0] = ldtr(q0 + aC[t] + ks2 * 4096); cf[ks2][t][1] = ldtr(q0 + aC[t] + ks2 * 4096 + 512); } };
        auto post = [&](int) {
#pragma unroll
            for (int ks2 = 0; ks2 < 2; ++ks2)
#pragma unroll
                for (int t = 0; t < 2; ++t) cs[t] = MFMA16(cat8(cf[ks2][t][0], cf[ks2][t][1]), ones, cs[t]); };
        issue(); issue();
        BAR_VL(8); pre(0); issue();
        if (FUSED) tag = make_tag(ev);
        BAR_VL(8); post(0); pre(1); issue();
        BAR_VL(8); post(1); pre(2); issue();
        BAR_VL(8); post(2); pre(3); issue(); rp1_issue<0, 2>(p, lds0, lane, lw, ti, tj);
        BAR_VL(10); post(3); pre(4); issue(); rp1_issue<2, 4>(p, lds0, lane, lw, ti, tj);
        BAR_VL(12); post(4); pre(5); issue(); rp1_issue<4, 7>(p, lds0, lane, lw, ti, tj);
        BAR_VL(13); post(5); pre(6); issue();
        u32x2 sf, sv;
        if (FUSED) ld64_async(sf, p.ws + WS_FLAG + FL_A + (ti * 16 + 15) * FS);
        BAR_VL(FUSED ? 12 : 11); post(6); pre(7); issue();
        BAR_VL(8); post(7); pre(8);
        if (FUSED) { asm volatile("" : "+v"(sf)); if (!__all((((u64)sf[1] << 32) | sf[0]) == tag)) poll_flags(p.ws + WS_FLAG + FL_A + (ti * 16 + 15) * FS, lane == 0, tag); }
        ld64_async(sv, p.ws + WS_S1 + (size_t)(128 * ti + 64 * (lw & 1) + lane) * 8);
        BAR_VL(0); post(8); pre(9); post(9);
        asm volatile("" : "+v"(sv));
        if (isA) *(LAS u32x2*)(L + L_STAT + (64 * lw + lane) * 8) = sv;
        __builtin_amdgcn_s_setprio(0);
        if ((lane & 15) == 0) {
#pragma unroll
            for (int t = 0; t < 2; ++t) *(LAS f32x4*)(L + L_CS + (32 * lw + 16 * t + 4 * g) * 4) = cs[t];
        }
        BAR_L();
        BAR_L();
    } else {
        const int wm = wid >> 1, wn = wid & 1, r32 = lane & 31, h = lane >> 5, blk = (lane >> 4) & 1, q = (lane & 15) >> 2, pp = lane & 3;
        unsigned aX[4], aW[2];
        { const int row = 64 * wm + r32;
#pragma unroll
          for (int u = 0; u < 4; ++u) aX[u] = (unsigned)(LA + (row >> 3) * 1024 + (row & 7) * 128 + (((2 * u + h) ^ ((row >> 1) & 7)) * 16));
#pragma unroll
          for (int nt = 0; nt < 2; ++nt) aW[nt] = (unsigned)(LB + (wn * 8 + h) * 1024 + q * 128 + (((4 * nt + 2 * blk + (pp >> 1)) ^ (4 * ((q >> 1) & 1) + 2 * h)) * 16) + (pp & 1) * 8); }
        f32x16 acc[2][2];
#pragma unroll
        for (int i = 0; i < 16; ++i) { acc[0][0][i] = 0.f; acc[0][1][i] = 0.f; acc[1][0][i] = 0.f; acc[1][1][i] = 0.f; }
        bf16x8 X[4][2], W[4][2];
        auto rd = [&](int slot, int u) {
            lcptr sl = (lcptr)L + slot * 16384;
#pragma unroll
            for (int nt = 0; nt < 2; ++nt) W[u][nt] = cat8(ldtr(sl + aW[nt] + u * 2048), ldtr(sl + aW[nt] + u * 2048 + 512));
#pragma unroll
            for (int mt = 0; mt < 2; ++mt) X[u][mt] = ld128(sl + aX[u] + mt * 4096);
        };
        auto mm = [&](int u) {
#pragma unroll
            for (int mt = 0; mt < 2; ++mt)
#pragma unroll
                for (int nt = 0; nt < 2; ++nt) acc[mt][nt] = MFMA32(W[u][nt], X[u][mt], acc[mt][nt]);
        };
        auto il = [&]() { SGB(0x8, 1); SGB(0x100, 2); SGB(0x8, 1); SGB(0x100, 2); SGB(0x8, 1); SGB(0x100, 2); SGB(0x8, 1); __builtin_amdgcn_sched_barrier(0); };
        auto bar = [&]() { __builtin_amdgcn_sched_barrier(0); asm volatile("" ::: "memory"); __builtin_amdgcn_s_barrier(); asm volatile("" ::: "memory"); __builtin_amdgcn_sched_barrier(0); };
        bar();
        if (FUSED) VMWAIT(0);
        rd(0, 0); rd(0, 1);
#pragma unroll 1
        for (int s = 0; s < NST - 1; ++s) {
            const int c = s & 3, n = (s + 1) & 3;
            rd(c, 2); mm(0); il();
            rd(c, 3); mm(1); il();
            bar();
            rd(n, 0); mm(2); il();
            rd(n, 1); mm(3); il();
        }
        rd((NST - 1) & 3, 2); mm(0); il();
        rd((NST - 1) & 3, 3); mm(1); il();
        mm(2); mm(3);
        if (FUSED) tag = make_tag(ev);
        BAR_L();
        {
            f32x2 st[2]; float cv[2];
#pragma unroll
            for (int i = 0; i < 2; ++i) { st[i] = *(const LAS f32x2*)(L + L_STAT + (64 * wm + 32 * i + r32) * 8); cv[i] = *(const LAS float*)(L + L_CS + (64 * wn + 32 * i + r32) * 4); }
            s16x4 cf[2], mf[2];
#pragma unroll
            for (int i = 0; i < 2; ++i) { const float hi = rbf(cv[i]); const unsigned c = h ? 0u : pk2(hi, cv[i] - hi), m = h ? 0u : pk2(-st[i].x, -st[i].x);
                cf[i] = __builtin_bit_cast(s16x4, ((u32x2){c, 0u})); mf[i] = __builtin_bit_cast(s16x4, ((u32x2){m, 0u})); }
#pragma unroll
            for (int mt = 0; mt < 2; ++mt)
#pragma unroll
                for (int nt = 0; nt < 2; ++nt) acc[mt][nt] = MFMA32K8(cf[nt], mf[mt], acc[mt][nt]);
#pragma unroll
            for (int mt = 0; mt < 2; ++mt) { const int tok = 64 * wm + 32 * mt + r32; const float sq = st[mt].y * C2_;
#pragma unroll
                for (int nt = 0; nt < 2; ++nt)
#pragma unroll
                    for (int u = 0; u < 4; ++u) { const int col = 64 * wn + 32 * nt + 8 * u + 4 * h;
                        const float sc = (128 * tj + col < D_) ? sq : st[mt].y;
                        *(LAS u32x2*)(L + (col >> 2) * 1024 + tok * 8) = (u32x2){pk2(acc[mt][nt][4 * u] * sc, acc[mt][nt][4 * u + 1] * sc), pk2(acc[mt][nt][4 * u + 2] * sc, acc[mt][nt][4 * u + 3] * sc)}; } }
        }
        BAR_L();
    }
    char* dst = p.ws + WS_QKV + (size_t)(32 * tj) * 16384 + (size_t)ti * 1024;
#pragma unroll
    for (int i = 0; i < 4; ++i) { const int gq = 4 * wid + i; st16<FUSED>(dst + (size_t)gq * 16384 + lane * 16, *(const LAS u32x4*)(L + gq * 1024 + lane * 16)); }
    publish<FUSED>(p, FL_A + (ti * 16 + tj) * FS, wid, lane, tag);
}

namespace pb {
constexpr int GS = 3104;
constexpr int L_K = 0, L_V = 10 * GS, L_ONE = 20 * GS, L_Q = 21 * GS, L_O = 112640;
static_assert(L_Q + 10240 <= L_O, "LDS map");
}
template <bool EDGE>
DI f32x16 attn_wave(lcptr L, int hh, int qb, int lane, int tstart) {
    using namespace pb;
    const int r = lane & 31, h = lane >> 5;
    bf16x8 q0; s16x4 q1;
    { lcptr qp = L + L_Q + hh * 5 * 1024 + (32 * qb + r) * 8;
      q0 = cat8(ld64(qp + (2 * h) * 1024), ld64(qp + (2 * h + 1) * 1024));
      const s16x4 t = ld64(qp + 4 * 1024); q1 = h ? (s16x4){0, 0, 0, 0} : t; }
    lcptr kp = L + L_K + hh * 5 * GS + (32 * qb + r) * 8;
    const f32x16 z16 = {};
    const int rr_ = r - 4 * h;
    bf16x8 k0[9]; s16x4 k1[9];
#pragma unroll
    for (int t = 0; t < 9; ++t) { k0[t] = cat8(ld64v(kp + (2 * h) * GS + t * 256), ld64v(kp + (2 * h + 1) * GS + t * 256)); k1[t] = ld64v(kp + 4 * GS + t * 256); }
    float m; f32x16 sn;
    { f32x16 a = MFMA32K8(k1[8], q1, MFMA32(k0[8], q0, z16)), b = MFMA32K8(k1[0], q1, MFMA32(k0[0], q0, z16));
      sn = MFMA32K8(k1[1], q1, MFMA32(k0[1], q0, z16)); asm volatile("" : "+v"(sn));
      __builtin_amdgcn_sched_barrier(0);
      const float bias = (!EDGE || tstart == 0) ? 0.f : -INFINITY;
#pragma unroll
      for (int i = 0; i < 16; ++i) a[i] = ((i & 3) + 8 * (i >> 2) <= rr_) ? a[i] : (EDGE ? b[i] + bias : b[i]);
      float x = __builtin_fmaxf(__builtin_fmaxf(a[0], a[1]), a[2]), y = __builtin_fmaxf(__builtin_fmaxf(a[3], a[4]), a[5]);
#pragma unroll
      for (int i = 6; i < 14; i += 4) { x = __builtin_fmaxf(__builtin_fmaxf(x, a[i]), a[i + 1]); y = __builtin_fmaxf(__builtin_fmaxf(y, a[i + 2]), a[i + 3]); }
      m = __builtin_fmaxf(__builtin_fmaxf(__builtin_fmaxf(x, a[14]), a[15]), y); }
#pragma unroll
    for (int t = 1; t < 8; ++t) { const f32x16 a = sn;
        if (t < 7) { sn = MFMA32K8(k1[t + 1], q1, MFMA32(k0[t + 1], q0, z16)); asm volatile("" : "+v"(sn)); }
        __builtin_amdgcn_sched_barrier(0);
        float x = __builtin_fmaxf(__builtin_fmaxf(a[0], a[1]), a[2]), y = __builtin_fmaxf(__builtin_fmaxf(a[3], a[4]), a[5]);
#pragma unroll
        for (int i = 6; i < 14; i += 4) { x = __builtin_fmaxf(__builtin_fmaxf(x, a[i]), a[i + 1]); y = __builtin_fmaxf(__builtin_fmaxf(y, a[i + 2]), a[i + 3]); }
        x = __builtin_fmaxf(__builtin_fmaxf(x, a[14]), a[15]);
        if (EDGE) x = t >= tstart ? x : -INFINITY;
        m = __builtin_fmaxf(__builtin_fmaxf(m, x), (EDGE && t < tstart) ? -INFINITY : y);
        __builtin_amdgcn_sched_barrier(0); }
    { auto rr = __builtin_amdgcn_permlane32_swap(__float_as_uint(m), __float_as_uint(m), false, false); m = __builtin_fmaxf(__uint_as_float(rr[0]), __uint_as_float(rr[1])); }
    f32x16 nm16;
#pragma unroll
    for (int i = 0; i < 16; ++i) nm16[i] = -m;

    const int blk = (lane >> 4) & 1, vq = (lane & 15) >> 2, vp = lane & 3, dg = 4 * blk + vp;
    lcptr vpz = L + (dg < 5 ? L_V + (hh * 5 + dg) * GS : L_ONE) + (32 * qb + 4 * h + vq) * 8;
    f32x16 O = z16;
    f32x16 an = MFMA32K8(k1[1], q1, MFMA32(k0[1], q0, nm16));
#pragma unroll
    for (int t = 1; t < 8; ++t) {
        const bf16x8 v0 = cat8(ldtr(vpz + t * 256), ldtr(vpz + t * 256 + 64)), v1 = cat8(ldtr(vpz + t * 256 + 128), ldtr(vpz + t * 256 + 192));
        f32x16 a = an;
        an = MFMA32K8(k1[t + 1], q1, MFMA32(k0[t + 1], q0, nm16));
        asm volatile("" : "+v"(an));
        __builtin_amdgcn_sched_barrier(0);
        if (EDGE) { const float bias = t >= tstart ? 0.f : -INFINITY;
#pragma unroll
            for (int i = 0; i < 16; ++i) a[i] += bias; }
        unsigned P[8];
#pragma unroll
        for (int i = 0; i < 8; ++i) P[i] = pk2(__builtin_amdgcn_exp2f(a[2 * i]), __builtin_amdgcn_exp2f(a[2 * i + 1]));
        O = MFMA32(v0, __builtin_bit_cast(bf16x8, ((u32x4){P[0], P[1], P[2], P[3]})), O);
        O = MFMA32(v1, __builtin_bit_cast(bf16x8, ((u32x4){P[4], P[5], P[6], P[7]})), O);
        __builtin_amdgcn_sched_barrier(0);
    }
    { const bf16x8 v0 = cat8(ldtr(vpz + 8 * 256), ldtr(vpz + 8 * 256 + 64)), v1 = cat8(ldtr(vpz + 8 * 256 + 128), ldtr(vpz + 8 * 256 + 192));
      const bf16x8 w0 = cat8(ldtr(vpz), ldtr(vpz + 64)), w1 = cat8(ldtr(vpz + 128), ldtr(vpz + 192));
      f32x16 a = an; const f32x16 b = MFMA32K8(k1[0], q1, MFMA32(k0[0], q0, nm16));
      const float bias = (!EDGE || tstart == 0) ? 0.f : -INFINITY;
#pragma unroll
      for (int i = 0; i < 16; ++i) a[i] = ((i & 3) + 8 * (i >> 2) <= rr_) ? a[i] : (EDGE ? b[i] + bias : b[i]);
      unsigned P[8], P0[8];
#pragma unroll
      for (int i = 0; i < 8; ++i) { P[i] = pk2(__builtin_amdgcn_exp2f(a[2 * i]), __builtin_amdgcn_exp2f(a[2 * i + 1]));
          const int c0 = ((2 * i) & 3) + 8 * ((2 * i) >> 2); const unsigned mk = (c0 + 1 <= rr_) ? 0xffffffffu : (c0 <= rr_ ? 0xffffu : 0u);
          P0[i] = P[i] & ~mk; P[i] &= mk; }
      O = MFMA32(v0, __builtin_bit_cast(bf16x8, ((u32x4){P[0], P[1], P[2], P[3]})), O);
      O = MFMA32(v1, __builtin_bit_cast(bf16x8, ((u32x4){P[4], P[5], P[6], P[7]})), O);
      O = MFMA32(w0, __builtin_bit_cast(bf16x8, ((u32x4){P0[0], P0[1], P0[2], P0[3]})), O);
      O = MFMA32(w1, __builtin_bit_cast(bf16x8, ((u32x4){P0[4], P0[5], P0[6], P0[7]})), O); }
    return O;
}
template <bool FUSED>
DI void phase_b(const Params& p, lptr L, int lane, int wid, int ti, int tj, u64 tag) {
    using namespace pb;
    const int tid = wid * 64 + lane;
    const unsigned lds0 = (unsigned)(uintptr_t)L;
    if (!FUSED) { if (wid >= 4) rp1_issue(p, lds0, lane, wid - 4, ti, tj); BAR_VL(0); }
    if (wid < 4) rp2_issue(p, lds0, lane, wid, ti, tj);
    else if (tj < 15) rp1_store<FUSED>(p, L, lane, wid - 4, ti, tj, 2);
    if (ti < 2) {
        for (int n = tid; n < (2 - ti) * 20 * 64; n += 512) { const int pc = n >> 6, mb = pc % (2 - ti), gq = pc / (2 - ti);
            *(LAS u32x4*)(L + gq * GS + mb * 1024 + (n & 63) * 16) = (u32x4){0u, 0u, 0u, 0u}; }
    }
    if (tid < 384) *(LAS u32x2*)(L + L_ONE + tid * 8) = (u32x2){0x00003f80u, 0u};
    const int sub = wid & 3, mtile = sub < 3 ? ti - 2 + sub : ti;
    const char* g0 = p.ws + WS_QKV + (size_t)((sub < 3 ? 160 : 0) + 5 * (2 * tj + (wid >> 2))) * 16384 + (size_t)(mtile < 0 ? 0 : mtile) * 1024;
    const unsigned l0 = lds0 + (sub < 3 ? L_K + (wid >> 2) * 5 * GS + sub * 1024 : L_Q + (wid >> 2) * 5120);
    if (wid < 4) BAR_VL(0);
    else {
        BAR_L();
        if (FUSED && wid == 7) {
            const int mb = lane / 6, sec = (lane % 6) >> 1, e = lane & 1, mt2 = ti - 2 + mb;
            const bool need = lane < 18 && mt2 >= 0 && (sec > 0 || mb == 2);
            poll_flags(p.ws + WS_FLAG + FL_A + ((mt2 < 0 ? 0 : mt2) * 16 + ((640 * sec + 40 * tj + 39 * e) >> 7)) * FS, need, tag); }
    }
    BAR_L();
    if (sub == 3) {
#pragma unroll
        for (int d = 0; d < 5; ++d) glds16<FUSED>(lane * 16, g0 + d * 16384, l0 + d * 1024);
    } else if (mtile >= 0) {
#pragma unroll
        for (int d = 0; d < 5; ++d) glds16<FUSED>(lane * 16, g0 + d * 16384, l0 + d * GS);
#pragma unroll
        for (int d = 0; d < 5; ++d) glds16<FUSED>(lane * 16, g0 + (160 + d) * 16384, l0 + L_V + d * GS);
    }
    BAR_VL(0);
    if (FUSED && tid == 0 && tj < 15) st_dev(p.ws + WS_FLAG + FL_P + (ti * 16 + tj) * FS, tag);
    const int hh = wid >> 2, qb = wid & 3, r = lane & 31, h = lane >> 5;
    const int q0g = 128 * ti + 32 * qb;
    f32x16 O;
    if (ti >= 2) O = attn_wave<false>((lcptr)L, hh, qb, lane, 0); else O = attn_wave<true>((lcptr)L, hh, qb, lane, q0g >= 256 ? 0 : 8 - (q0g >> 5));
    { auto rr = __builtin_amdgcn_permlane32_swap(__float_as_uint(O[8]), __float_as_uint(O[8]), false, false);
      const float il = 1.0f / __uint_as_float(rr[1]);
      const int row = 32 * qb + r;
#pragma unroll
      for (int u = 0; u < 3; ++u) { const int dgp = 2 * u + h; if (dgp < 5) { const int cgp = 5 * hh + dgp;
          *(LAS u32x2*)(L + L_O + (cgp >> 1) * 2048 + row * 16 + (cgp & 1) * 8) = (u32x2){pk2(O[4 * u] * il, O[4 * u + 1] * il), pk2(O[4 * u + 2] * il, O[4 * u + 3] * il)}; } } }
    if (wid < 4) { if (tj < 15) rp1_store<FUSED>(p, L, lane, wid, ti, tj, 4); (void)rp2_store<FUSED>(p, L, lane, wid, ti, tj); }
    BAR_L();
    for (int n = wid; n < 10; n += 8)
        st16<FUSED>(p.ws + WS_O + (size_t)(5 * tj + (n >> 1)) * 32768 + (size_t)(128 * ti + 64 * (n & 1)) * 16 + lane * 16, *(const LAS u32x4*)(L + L_O + n * 1024 + lane * 16));
    publish<FUSED>(p, FL_B + (ti * 16 + tj) * FS, wid, lane, tag);
}

template <int MODE> struct PC;
template <> struct PC<0> { static constexpr int NST = 10, RB = 10, RA = 6, LA = 102400, L_RES = 151552, L_X = 102400, L_STG = 122880; };
template <> struct PC<1> { static constexpr int NST = 27, RB = 10, RA = 5, LA = 102400, L_RES = 143360, L_X = 102400, L_STG = 102400; };
constexpr int EPRE = 8;
template <bool FUSED, int MODE>
DI void ce_prefetch(const Params& p, lptr L, int lane, int wid, int tj) {
    const char* wb = p.ws + (MODE ? WS_PWD + (size_t)(tj >> 1) * 54 * 5120 : WS_PWO + (size_t)(tj >> 1) * 20 * 5120) + (wid - 6) * 5120 + 2048;
    const unsigned l = (unsigned)(uintptr_t)L + (wid - 6) * 5120 + 2048;
#pragma unroll 1
    for (int s = 0; s < (MODE ? EPRE : PC<MODE>::RB); ++s) gl_lin5<FUSED>(lane * 16, wb + (size_t)s * 10240, l + s * 10240);
}
template <bool FUSED, int MODE>
DI void phase_ce(const Params& p, lptr L, int lane, int wid, int ti, int tj, u64 tag) {
    typedef PC<MODE> C;
    constexpr int NST = C::NST, RA = C::RA, RB = C::RB;
    const unsigned lds0 = (unsigned)(uintptr_t)L;
    const int rh = tj & 1, ct = tj >> 1, r0 = 128 * ti + 64 * rh;
    const int wm = (wid >> 1) & 1, wk = wid & 1, r16 = lane & 15, g = lane >> 4;
    if (wid >= 6) {
        if (MODE == 0) {
            bar_vl_n<5, RB - 1>(RB - 1);
            BAR_VL(0);
#pragma unroll 1
            for (int s = 2; s < NST; ++s) BAR_L();
        } else {
            const char* wb = p.ws + WS_PWD + (size_t)ct * 54 * 5120 + (wid - 6) * 5120 + 2048 + (size_t)EPRE * 10240;
            const unsigned l0 = lds0 + (wid - 6) * 5120 + 2048; unsigned l = l0 + (EPRE % RB) * 10240;
            ring_loader<RB, NST, 5, 2, EPRE, false>([&]() { gl_lin5<FUSED>(lane * 16, wb, l); wb += 10240; l = l == l0 + (RB - 1) * 10240 ? l0 : l + 10240; });
        }
        BAR_L(); BAR_L();
    } else if (wid >= 4) {
        const int la = wid - 4;
        const unsigned voA = lane * 16;
#pragma unroll
        for (int i = 0; i < 5; ++i) { const int pc = 5 * la + i;
            if (MODE == 0) { const int n = 64 * pc + lane; glds16<false>((unsigned)((n / 10) * D_ * 2 + (n % 10) * 16), (const char*)p.x + (size_t)r0 * D_ * 2 + ct * 160, lds0 + C::L_RES + pc * 1024); }
            else glds16<FUSED>(voA, p.ws + WS_H + (size_t)(10 * ct + pc) * 32768 + (size_t)r0 * 16, lds0 + C::L_RES + pc * 1024); }
        if (FUSED) poll_flags(p.ws + WS_FLAG + (MODE ? FL_D : FL_B) + (ti * 16 + (lane & 15)) * FS, lane < 16, tag);
        const char* ab = p.ws + (MODE ? WS_HH : WS_O) + (size_t)r0 * 16 + (size_t)(4 * la) * 32768 + 2048;
        unsigned vp[4];
#pragma unroll
        for (int i = 0; i < 4; ++i) vp[i] = voA + (unsigned)i * (32768 - 1024);
        const unsigned l0 = lds0 + C::LA + la * 4096 + 2048; unsigned l = l0;
        ring_loader<RA, NST, 4, 2, 2>([&]() { gl_str4<FUSED>(vp, ab, l); ab += 8 * 32768; l = l == l0 + (RA - 1) * 8192 ? l0 : l + 8192; });
        BAR_L(); BAR_L();
    } else {
        const unsigned aA = (unsigned)(C::LA + (4 * wk + g) * 1024 + (32 * wm + r16) * 16), aB = (unsigned)(wk * 5120 + lane * 16);
        f32x4 acc[2][5];
#pragma unroll
        for (int i = 0; i < 2; ++i)
#pragma unroll
            for (int n = 0; n < 5; ++n) acc[i][n] = (f32x4){0.f, 0.f, 0.f, 0.f};
        bf16x8 Af[3][2], Bf[3][5];
        const int kofs = wk * 256;
        int sa = 0, sb = 0;
        auto rd = [&](int b) {
            lcptr la = (lcptr)L + aA + sa * 8192; lcptr lb = (lcptr)L + aB + sb * 10240;
            Af[b][0] = ld128(la + kofs); Af[b][1] = ld128(la + (256 - kofs));
#pragma unroll
            for (int n = 0; n < 5; ++n) Bf[b][n] = ld128(lb + n * 1024);
            sa = sa == RA - 1 ? 0 : sa + 1; sb = sb == RB - 1 ? 0 : sb + 1;
        };
        auto mm = [&](int b) {
#pragma unroll
            for (int n = 0; n < 5; ++n)
#pragma unroll
                for (int i = 0; i < 2; ++i) acc[i][n] = MFMA16(Bf[b][n], Af[b][i], acc[i][n]);
        };
        auto bar = [&]() { __builtin_amdgcn_sched_barrier(0); __builtin_amdgcn_s_waitcnt(0xC77F); asm volatile("" ::: "memory"); __builtin_amdgcn_s_barrier(); asm volatile("" ::: "memory"); __builtin_amdgcn_sched_barrier(0); };
        auto il = [&]() { SGB(0x8, 1); SGB(0x100, 2); SGB(0x8, 1); SGB(0x100, 2); SGB(0x8, 1); SGB(0x100, 2); SGB(0x8, 1); SGB(0x100, 1); SGB(0x8, 6); __builtin_amdgcn_sched_barrier(0); };
        bar(); rd(0);
        bar(); rd(1);
        int s = 0;
#pragma unroll 1
        for (; s + 4 < NST; s += 3) {
            bar(); rd(2); mm(0); il();
            bar(); rd(0); mm(1); il();
            bar(); rd(1); mm(2); il();
        }
        if (NST % 3 == 0) { bar(); rd(2); mm(0); il(); mm(1); mm(2); }
        else { bar(); rd(2); mm(0); il(); bar(); rd(0); mm(1); il(); mm(2); mm(0); }
        BAR_L();
#pragma unroll
        for (int n = 0; n < 5; ++n) *(LAS f32x4*)(L + C::L_X + (wid * 5 + n) * 1024 + lane * 16) = acc[1][n];
        BAR_L();
        const int row = 32 * wm + 16 * wk + r16;
        f32x4 pv[5]; u32x2 rr[5];
#pragma unroll
        for (int n = 0; n < 5; ++n) { pv[n] = *(const LAS f32x4*)(L + C::L_X + ((wid ^ 1) * 5 + n) * 1024 + lane * 16);
            rr[n] = MODE == 0 ? *(const LAS u32x2*)(L + C::L_RES + row * 160 + n * 32 + g * 8) : *(const LAS u32x2*)(L + C::L_RES + (2 * n + (g >> 1)) * 1024 + row * 16 + (g & 1) * 8); }
        u32x2 o[5]; f32x2 ps2 = {0.f, 0.f}, pq2 = {0.f, 0.f};
#pragma unroll
        for (int n = 0; n < 5; ++n) { const f32x4 v = acc[0][n] + pv[n];
            const f32x2 a = (f32x2){v[0], v[1]} + (f32x2){bflo(rr[n][0]), bfhi(rr[n][0])}, b = (f32x2){v[2], v[3]} + (f32x2){bflo(rr[n][1]), bfhi(rr[n][1])};
            o[n] = (u32x2){pk2(a[0], a[1]), pk2(b[0], b[1])};
            if (MODE == 0) { ps2 += a + b; pq2 += a * a + b * b; } }
        float ps = ps2[0] + ps2[1], pq = pq2[0] + pq2[1];
#pragma unroll
        for (int n = 0; n < 5; n += 2) { const int n1 = n + 1 < 5 ? n + 1 : n;
            auto s0 = __builtin_amdgcn_permlane16_swap(o[n][0], o[n1][0], false, false); auto s1 = __builtin_amdgcn_permlane16_swap(o[n][1], o[n1][1], false, false);
            const u32x4 v = {s0[0], s1[0], s0[1], s1[1]};
            const int ch = 2 * (n + (g & 1)) + (g >> 1);
            if (n + 1 < 5 || !(g & 1)) {
                if (MODE == 0) st16<FUSED>(p.ws + WS_H + (size_t)(10 * ct + ch) * 32768 + (size_t)(r0 + row) * 16, v);
                else st16<false>(p.out + (size_t)(r0 + row) * D_ * 2 + ct * 160 + ch * 16, v); } }
        if (MODE == 0) { ps = sum_x32(sum_x16(ps)); pq = sum_x32(sum_x16(pq));
            if (g == 0) { char* dst = p.ws + WS_PS + ((size_t)(ti * 8 + ct) * 128 + 64 * rh + row) * 8; const u64 v = ((u64)__float_as_uint(pq) << 32) | __float_as_uint(ps);
                if (FUSED) st_dev(dst, v); else *(u64*)dst = v; } }
    }
    if (MODE == 0) publish<FUSED>(p, FL_C + (ti * 16 + tj) * FS, wid, lane, tag);
}

namespace pd { constexpr int RB = 3, RA = 4, SB = 28672, LA = RB * SB, NST = 10, L_STAT = LA + RA * 16384, L_CF = L_STAT + 1024  , L_STG = LA; }
template <bool FUSED>
DI void d_bissue(const char* wb, unsigned l, int lane) { gl_lin7<FUSED>(lane * 16, wb, l); gl_lin7<FUSED>(lane * 16, wb + 7168, l + 7168); }
template <bool FUSED>
DI void d_prefetch(const Params& p, lptr L, int lane, int wid, int tj) {
    const char* wb = p.ws + WS_PWG + (size_t)tj * 286720 + (wid - 6) * 14336 + 3072;
    const unsigned l = (unsigned)(uintptr_t)L + (wid - 6) * 14336 + 3072;
#pragma unroll 1
    for (int s = 0; s < pd::RB; ++s) d_bissue<FUSED>(wb + (size_t)s * pd::SB, l + s * pd::SB, lane);
}
template <bool FUSED>
DI void phase_d(const Params& p, lptr L, int lane, int wid, int ti, int tj, u64 tag) {
    using namespace pd;
    const unsigned lds0 = (unsigned)(uintptr_t)L;
    if (wid >= 6) {
        const int lb = wid - 6;
        u32x2 ps[8]; float cp[2][16];
        const int row = 64 * lb + lane;
        const char* wb = p.ws + WS_PWG + (size_t)tj * 286720 + lb * 14336 + 3072 + (size_t)RB * SB;
        const unsigned l0 = lds0 + lb * 14336 + 3072; unsigned l = l0;
        auto issue = [&]() { d_bissue<FUSED>(wb, l, lane); wb += SB; l = l == l0 + (RB - 1) * SB ? l0 : l + SB; };
        static_assert(RB == 3 && NST == 10, "the waits below");
        const char* pa_ = p.ws + WS_PS + ((size_t)(ti * 8) * 128 + row) * 8 + 4096;
        const char* ca = p.ws + WS_CSP + ((size_t)tj * 16 * 224 + row) * 4 + 4 * 896;
        const char* cb = p.ws + WS_CSP + ((size_t)tj * 16 * 224 + (row + 128 < 224 ? row + 128 : 223)) * 4 + 4 * 896;
        auto cload = [&](float (&d)[16], const char* a) { const char* a2 = a + 8 * 896;
            static_for<0, 9>([&](auto ic) { constexpr int i = decltype(ic)::v; ld32_async_o<(i - 4) * 896>(d[i], a); });
            static_for<9, 16>([&](auto ic) { constexpr int i = decltype(ic)::v; ld32_async_o<(i - 12) * 896>(d[i], a2); }); };
        auto colsum = [&](int z) { float sm = 0.f;
#pragma unroll
            for (int i = 0; i < 16; ++i) { asm volatile("" : "+v"(cp[z][i])); sm += cp[z][i]; }
            const int c = row + 128 * z;
            if (c < 224) { const float hi = rbf(sm); *(LAS unsigned*)(L + L_CF + c * 4) = pk2(hi, sm - hi); } };
        BAR_VL(28);
        static_for<0, 8>([&](auto ic) { constexpr int c = decltype(ic)::v; ld64_async_o<c * 1024 - 4096>(ps[c], pa_); });
        BAR_VL(22); issue();
        cload(cp[0], ca);
        BAR_VL(38); issue();
        cload(cp[1], cb);
        BAR_VL(46); issue();
        BAR_VL(30); issue();
        BAR_VL(14); issue();
        { float a = 0.f, q = 0.f;
#pragma unroll
          for (int c = 0; c < 8; ++c) { asm volatile("" : "+v"(ps[c])); a += __uint_as_float(ps[c][0]); q += __uint_as_float(ps[c][1]); }
          *(LAS f32x2*)(L + L_STAT + row * 8) = ln_scalars(a, q); }
        BAR_VL(14); issue();
        colsum(0); colsum(1);
        BAR_VL(14); issue();
        BAR_VL(14);
        if (FUSED) {
#pragma unroll 1
            for (int i = 0; i < 3; ++i) { int ln = (lb * 3 + i) * 64 + lane; ln = ln < 270 ? ln : 269;
                warm64(p.ws + WS_PWD + (size_t)(tj >> 1) * 276480 + (size_t)(2 * (ti & 3) + (tj & 1)) * 34560, (unsigned)ln * 128u, lds0); }
            BAR_VL(3);
        } else BAR_VL(0);
        BAR_L();
    } else if (wid >= 4) {
        const int la = wid - 4;
        if (FUSED) poll_flags(p.ws + WS_FLAG + FL_C + (ti * 16 + (lane & 15)) * FS, lane < 16, tag);
        const char* ab = p.ws + WS_H + (size_t)(128 * ti + 64 * la) * 16 + 4096;
        unsigned vp[4];
#pragma unroll
        for (int i = 0; i < 4; ++i) vp[i] = lane * 16 + (unsigned)i * (32768 - 2048);
        const unsigned l0 = lds0 + LA + la * 1024 + 4096; unsigned l = l0;
        ring_loader<RA, NST, 8, 1, 2>([&]() { gl_str4w<FUSED>(vp, ab, l); gl_str4w<FUSED>(vp, ab + 4 * 32768, l + 8192); ab += 8 * 32768; l = l == l0 + (RA - 1) * 16384 ? l0 : l + 16384; });
        BAR_L();
    } else {
        const int wm = wid >> 1, wn = wid & 1, r16 = lane & 15, g = lane >> 4;
        const unsigned aA = (unsigned)(LA + g * 2048 + (64 * wm + r16) * 16), aB = (unsigned)(wn * 7168 + lane * 16);
        f32x4 acc[4][7];
#pragma unroll
        for (int i = 0; i < 4; ++i)
#pragma unroll
            for (int t = 0; t < 7; ++t) acc[i][t] = (f32x4){0.f, 0.f, 0.f, 0.f};
        bf16x8 A0[4], B0[7], A1[4], B1[7];
        auto rd = [&](bf16x8* Af, bf16x8* Bf, int sa, int sb, int ks) {
#pragma unroll
            for (int t = 0; t < 7; ++t) Bf[t] = ld128((lcptr)L + aB + sb * SB + ks * 14336 + t * 1024);
#pragma unroll
            for (int i = 0; i < 4; ++i) Af[i] = ld128((lcptr)L + aA + sa * 16384 + ks * 8192 + i * 256);
        };
        auto mm = [&](const bf16x8* Af, const bf16x8* Bf) {
#pragma unroll
            for (int i = 0; i < 4; ++i)
#pragma unroll
                for (int t = 0; t < 7; ++t) acc[i][t] = MFMA16(Bf[t], Af[i], acc[i][t]);
        };
        auto il = [&]() {
#pragma unroll
            for (int i = 0; i < 11; ++i) { SGB(0x8, 1); SGB(0x100, 1); }
            SGB(0x8, 17); __builtin_amdgcn_sched_barrier(0); };
        CBAR();
        rd(A0, B0, 0, 0, 0);
        int sa = 0, sb = 0;
#pragma unroll 1
        for (int s = 0; s < NST - 1; ++s) {
            WAIT_LGKM0();
            rd(A1, B1, sa, sb, 1); mm(A0, B0); il();
            sa = sa == RA - 1 ? 0 : sa + 1; sb = sb == RB - 1 ? 0 : sb + 1;
            CBAR();
            rd(A0, B0, sa, sb, 0); mm(A1, B1); il();
        }
        WAIT_LGKM0();
        rd(A1, B1, sa, sb, 1); mm(A0, B0); il();
        WAIT_LGKM0();
        mm(A1, B1);
        BAR_L();
        f32x2 st[4]; unsigned cw[7];
#pragma unroll
        for (int i = 0; i < 4; ++i) st[i] = *(const LAS f32x2*)(L + L_STAT + (64 * wm + 16 * i + r16) * 8);
#pragma unroll
        for (int t = 0; t < 7; ++t) cw[t] = *(const LAS unsigned*)(L + L_CF + ((7 * wn + t) * 16 + r16) * 4);
        {
            s16x4 mf[4];
#pragma unroll
            for (int i = 0; i < 4; ++i) mf[i] = __builtin_bit_cast(s16x4, ((u32x2){g ? 0u : pk2(-st[i].x, -st[i].x), 0u}));
#pragma unroll
            for (int t = 0; t < 7; ++t) { const s16x4 cf = __builtin_bit_cast(s16x4, ((u32x2){g ? 0u : cw[t], 0u}));
#pragma unroll
                for (int i = 0; i < 4; ++i) acc[i][t] = MFMA16K16(cf, mf[i], acc[i][t]); }
        }
        auto glu4 = [&](float rs, const f32x4& ga, const f32x4& up) {
            const float r2 = rs * rs; const f32x2 r22 = {r2, r2}, ya = (f32x2){up[0], up[1]} * r22, yb = (f32x2){up[2], up[3]} * r22;
            const f32x2 oa = (f32x2){relu(ga[0]), relu(ga[1])} * ya, ob = (f32x2){relu(ga[2]), relu(ga[3])} * yb;
            return (u32x2){pk2(oa[0], oa[1]), pk2(ob[0], ob[1])}; };
        char* hb = p.ws + WS_HH + (size_t)(14 * tj) * 32768 + (size_t)(128 * ti + 64 * wm + r16) * 16;
        const int cmax = 216 - 14 * tj, c0 = wn ? 8 : 0;
        auto put = [&](u32x2 x, u32x2 y, int chunk, int mtile) {
            auto s0 = __builtin_amdgcn_permlane16_swap(x[0], y[0], false, false); auto s1 = __builtin_amdgcn_permlane16_swap(x[1], y[1], false, false);
            if (chunk < cmax) st16<FUSED>(hb + (size_t)chunk * 32768 + mtile * 256, (u32x4){s0[0], s1[0], s0[1], s1[1]}); };
#pragma unroll
        for (int i = 0; i < 4; ++i)
            put(glu4(st[i].y, acc[i][0], acc[i][1]), glu4(st[i].y, acc[i][2], acc[i][3]), c0 + 2 * (g & 1) + (g >> 1), i);
#pragma unroll
        for (int i = 0; i < 4; i += 2)
            put(glu4(st[i].y, acc[i][4], acc[i][5]), glu4(st[i + 1].y, acc[i + 1][4], acc[i + 1][5]), c0 + 4 + (g >> 1), i + (g & 1));
        {
            u32x2 mx[2];
#pragma unroll
            for (int a = 0; a < 2; ++a) { f32x4 X = acc[2 * a][6], Y = acc[2 * a + 1][6];
#pragma unroll
                for (int e = 0; e < 4; ++e) { auto rr = __builtin_amdgcn_permlane32_swap(__float_as_uint(X[e]), __float_as_uint(Y[e]), false, false); X[e] = __uint_as_float(rr[0]); Y[e] = __uint_as_float(rr[1]); }
                mx[a] = glu4((lane >> 5) ? st[2 * a + 1].y : st[2 * a].y, X, Y); }
            put(mx[0], mx[1], wn ? 7 : 6, 2 * (g & 1) + (g >> 1));
        }
    }
    publish<FUSED>(p, FL_D + (ti * 16 + tj) * FS, wid, lane, tag);
}

#define PROLOG() \
    extern __shared__ __attribute__((aligned(16))) char smem[]; \
    lptr L = (lptr)smem; \
    const int tid = threadIdx.x, lane = tid & 63, wid = __builtin_amdgcn_readfirstlane(tid >> 6); \
    const int xcd = blockIdx.x & 7, idx = blockIdx.x >> 3; \
    const int ti = 4 * (xcd & 3) + (idx & 3), tj = 8 * (xcd >> 2) + (idx >> 2); \
    const int rotA = ((idx >> 2) + (idx & 3)) & 3;        \
    (void)tid

__global__ __launch_bounds__(512, 2) void k_fused(const Params p) {
    PROLOG();
    u64 tag = 0; u32x2 ev;
    ld64_async(ev, p.ws + WS_FLAG + FL_E);
    if (tj >= 15) phase_a_stats<true>(p, L, lane, wid, ti, ev, tag); else phase_a<true>(p, L, lane, wid, ti, tj, rotA, ev, tag);
    if (tid == 0) st_dev(p.ws + WS_FLAG + FL_START + blockIdx.x * 8, tag);
    phase_b<true>(p, L, lane, wid, ti, tj, tag);
    if (wid >= 6) {
        poll_flags(p.ws + WS_FLAG + FL_P + ((lane & 15) * 16 + (tj & ~1) + ((lane >> 4) & 1)) * FS, lane < 32, tag);
        ce_prefetch<true, 0>(p, L, lane, wid, tj);
    }
    phase_ce<true, 0>(p, L, lane, wid, ti, tj, tag);
    if (wid >= 6) {
        poll_flags(p.ws + WS_FLAG + FL_B + ((lane & 15) * 16 + (lane < 16 ? tj : (tj & ~1) + ((lane >> 4) & 1))) * FS, lane < 48, tag);
        d_prefetch<true>(p, L, lane, wid, tj);
    }
    if (blockIdx.x == 0 && wid == 5) {
        for (unsigned spins = 0; spins < (1u << 17); ++spins) {
            bool ok = true;
#pragma unroll
            for (int i = 0; i < 4; ++i) ok &= ld_dev(p.ws + WS_FLAG + FL_START + (lane * 4 + i) * 8) == tag;
            if (__all(ok)) break;
            __builtin_amdgcn_s_sleep(1);
        }
        if (lane == 0) st_dev(p.ws + WS_FLAG + FL_E, tag);
    }
    phase_d<true>(p, L, lane, wid, ti, tj, tag);
    if (wid >= 6) ce_prefetch<true, 1>(p, L, lane, wid, tj);
    phase_ce<true, 1>(p, L, lane, wid, ti, tj, tag);
}

extern "C" void kernel_launch(void* const* d_in, const int* in_sizes, int n_in, void* d_out, int out_size, void* d_ws, size_t ws_size, hipStream_t stream) {
    Params p;
    p.x = (const u16*)d_in[0]; p.wqkv = (const u16*)d_in[1]; p.wo = (const u16*)d_in[2];
    p.wg = (const u16*)d_in[4]; p.wu = (const u16*)d_in[5]; p.wd = (const u16*)d_in[6];
    p.out = (char*)d_out; p.ws = (char*)d_ws;
    (void)hipFuncSetAttribute((const void*)k_fused, hipFuncAttributeMaxDynamicSharedMemorySize, LDS_BYTES);
    k_fused<<<256, 512, LDS_BYTES, stream>>>(p);
}
```
